```python
import torch

def _softmax(x, axis=-1):
    return torch.softmax(x, dim=axis)

def _gelu(x):
    return torch.nn.functional.gelu(x, approximate='tanh')

def _silu(x):
    return torch.nn.functional.silu(x)

def _sigmoid(x):
    return torch.sigmoid(x)

def _layernorm(x, axis=-1, eps=1e-05):
    m = x.mean(dim=axis, keepdim=True)
    v = x.var(dim=axis, keepdim=True, unbiased=False)
    return (x - m) / torch.sqrt(v + eps)

def _scatter_add(tgt, idx, src, axis=0):
    return tgt.clone().index_add_(axis, idx, src)

def _segment_sum(src, seg, n):
    out = torch.zeros((n,) + src.shape[1:], dtype=src.dtype)
    return out.index_add_(0, seg, src)

def _pad(x, pw):
    flat = tuple((v for pair in reversed(pw) for v in pair))
    return torch.nn.functional.pad(x, flat)
kernel_name = 'adaLN_cond__mha_block_cached__mixer_channel_mlp__prompt'

def setup_inputs():

    _w = lambda a: (lambda t: t.to(torch.bfloat16) if t.is_floating_point() else t)(torch.as_tensor(a))

    def _A_setup():
        B, Sq, D = (1, 2048, 1024)
        H, Sk, Dh = (32, 2048, 128)
        return {'x': _w((lambda *s: torch.randn(*s, dtype=torch.bfloat16) * 0.05)(B, Sq, D)), 'k_cache': _w((lambda *s: torch.randn(*s, dtype=torch.bfloat16) * 3.2)(B, H, Sk, Dh)), 'v_cache': _w((lambda *s: torch.randn(*s, dtype=torch.bfloat16) * 0.4)(B, H, Sk, Dh)), 'wq': _w((lambda *s: torch.randn(*s, dtype=torch.bfloat16) * 3.2)(D, H * Dh) * D ** (-0.5)), 'wo': _w((lambda *s: torch.randn(*s, dtype=torch.bfloat16) * 0.4)(H * Dh, D) * (H * Dh) ** (-0.5))}
    _A_in = _A_setup()

    def _B_setup():
        B, S, D, F = (1, 2048, 1024, 3584)
        return {'x': _w((lambda *s: torch.randn(*s, dtype=torch.bfloat16) * 0.05)(B, S, D)), 'w1': _w((lambda *s: torch.randn(*s, dtype=torch.bfloat16) * 3.2)(D, F) * D ** (-0.5)), 'b1': _w((lambda *s: torch.randn(*s, dtype=torch.bfloat16) * 1.6)(F)), 'w2': _w((lambda *s: torch.randn(*s, dtype=torch.bfloat16) * 0.1)(F, D) * F ** (-0.5)), 'b2': _w((lambda *s: torch.randn(*s, dtype=torch.bfloat16) * 0.2)(D))}
    _B_in = _B_setup()
    return {'A_x': _A_in['x'], 'A_k_cache': _A_in['k_cache'], 'A_v_cache': _A_in['v_cache'], 'A_wq': _A_in['wq'], 'A_wo': _A_in['wo'], 'B_x': _B_in['x'], 'B_w1': _B_in['w1'], 'B_b1': _B_in['b1'], 'B_w2': _B_in['w2'], 'B_b2': _B_in['b2'], 'c': _w((lambda *s: torch.randn(*s, dtype=torch.bfloat16) * 0.1)(1, 1024) * 8.0), 'w_mod': _w((lambda *s: torch.randn(*s, dtype=torch.bfloat16) * 0.1)(1024, 6 * 1024) * 1024 ** (-0.5) * 8.0)}

def reference(A_x, A_k_cache, A_v_cache, A_wq, A_wo, B_x, B_w1, B_b1, B_w2, B_b2, c, w_mod):

    def _A_ref(x, k_cache, v_cache, wq, wo):
        B, Sq, D = x.shape
        H, Dh = (32, 128)
        q = (lambda x, *d: x.permute(*d))((x @ wq).reshape(B, Sq, H, Dh), 0, 2, 1, 3)
        scores = torch.einsum('bhqd,bhkd->bhqk', q, k_cache) * Dh ** (-0.5)
        p = _softmax(scores, axis=-1)
        o = torch.einsum('bhqk,bhkd->bhqd', p, v_cache)
        return (lambda x, *d: x.permute(*d))(o, 0, 2, 1, 3).reshape(B, Sq, H * Dh) @ wo

    def _B_ref(x, w1, b1, w2, b2):
        y = _layernorm(x, axis=-1)
        h = _gelu(torch.einsum('bsd,df->bsf', y, w1) + b1)
        return torch.einsum('bsf,fd->bsd', h, w2) + b2
    x = A_x
    mod = (_silu(c) @ w_mod).reshape(c.shape[0], 6, 1, -1)
    sa, ca, ga, sb, cb, gb = (mod[:, 0].clone(), mod[:, 1].clone(), mod[:, 2].clone(), mod[:, 3].clone(), mod[:, 4].clone(), mod[:, 5].clone())
    h = _layernorm(x, axis=-1) * (1 + ca) + sa
    x = x + ga * _A_ref(h, A_k_cache, A_v_cache, A_wq, A_wo)
    h = _layernorm(x, axis=-1) * (1 + cb) + sb
    x = x + gb * _B_ref(h, B_w1, B_b1, B_w2, B_b2)
    return x
```

```cpp
#include <hip/hip_runtime.h>
#include <hip/hip_bf16.h>
typedef unsigned short u16;
__device__ __forceinline__ float bf2f(u16 x){ return __uint_as_float(((unsigned)x)<<16); }
__device__ __forceinline__ u16 f2bf(float f){ unsigned u=__float_as_uint(f); u+=0x7fffu+((u>>16)&1u); return (u16)(u>>16); }
constexpr int S=2048,D=1024,H=32,DH=128,F=3584;

__global__ void k_mod(const u16* c,const u16* w,float* mod){
  int n=blockIdx.x*blockDim.x+threadIdx.x; float a=0;
  for(int k=0;k<D;k++){ float x=bf2f(c[k]); float s=bf2f(f2bf(x/(1.f+__expf(-x)))); a+=s*bf2f(w[(size_t)k*6144+n]); }
  mod[n]=bf2f(f2bf(a));
}
__device__ float bsum(float v,float* sh){
  for(int o=32;o;o>>=1) v+=__shfl_xor(v,o);
  __syncthreads(); if((threadIdx.x&63)==0) sh[threadIdx.x>>6]=v; __syncthreads();
  return sh[0]+sh[1]+sh[2]+sh[3];
}
__global__ void k_ln(const float* xf,const u16* xb,const float* sh_,const float* sc,u16* out,int mode){
  __shared__ float sh[4]; int r=blockIdx.x,t=threadIdx.x; float v[4];
  for(int i=0;i<4;i++) v[i]= xf? xf[(size_t)r*D+t*4+i] : bf2f(xb[(size_t)r*D+t*4+i]);
  float m=bsum(v[0]+v[1]+v[2]+v[3],sh)/D; float q=0; for(int i=0;i<4;i++) q+=(v[i]-m)*(v[i]-m);
  float rs=rsqrtf(bsum(q,sh)/D+1e-5f);
  for(int i=0;i<4;i++) v[i]=(v[i]-m)*rs*(1.f+sc[t*4+i])+sh_[t*4+i];
  if(mode){ m=bsum(v[0]+v[1]+v[2]+v[3],sh)/D; q=0; for(int i=0;i<4;i++) q+=(v[i]-m)*(v[i]-m);
    rs=rsqrtf(bsum(q,sh)/D+1e-5f); for(int i=0;i<4;i++) v[i]=(v[i]-m)*rs; }
  for(int i=0;i<4;i++) out[(size_t)r*D+t*4+i]=f2bf(v[i]);
}
__global__ void k_gemm(const u16* A,const u16* B,int M,int N,int K,int epi,u16* ob,float* of,const u16* xb,const float* xf,const float* g,const u16* bias){
  __shared__ float As[16][65],Bs[16][64];
  int tx=threadIdx.x&15,ty=threadIdx.x>>4; int bm=blockIdx.y*64,bn=blockIdx.x*64; float acc[4][4]={};
  for(int k0=0;k0<K;k0+=16){
    for(int i=threadIdx.x;i<1024;i+=256){ int r=i>>4,k=i&15; As[k][r]=bf2f(A[(size_t)(bm+r)*K+k0+k]); int kk=i>>6,n=i&63; Bs[kk][n]=bf2f(B[(size_t)(k0+kk)*N+bn+n]); }
    __syncthreads();
    for(int k=0;k<16;k++){ float a[4],b[4]; for(int i=0;i<4;i++){a[i]=As[k][ty*4+i]; b[i]=Bs[k][tx*4+i];}
      for(int i=0;i<4;i++)for(int j=0;j<4;j++) acc[i][j]+=a[i]*b[j]; }
    __syncthreads();
  }
  for(int i=0;i<4;i++)for(int j=0;j<4;j++){ int r=bm+ty*4+i,n=bn+tx*4+j; size_t o=(size_t)r*N+n; float v=acc[i][j];
    if(epi==0) ob[o]=f2bf(v);
    else if(epi==1) of[o]=bf2f(xb[o])+g[n]*v;
    else if(epi==2){ v+=bf2f(bias[n]); float t=tanhf(0.7978845608f*(v+0.044715f*v*v*v)); ob[o]=f2bf(0.5f*v*(1.f+t)); }
    else ob[o]=f2bf(xf[o]+g[n]*(v+bf2f(bias[n])));
  }
}
__global__ void k_attn(const u16* Q,const u16* Kc,const u16* Vc,u16* O){
  __shared__ float qs[16][128]; __shared__ float sc[16][2048]; __shared__ float sh[4];
  int h=blockIdx.y,q0=blockIdx.x*16,t=threadIdx.x;
  for(int i=t;i<16*128;i+=256) qs[i>>7][i&127]=bf2f(Q[(size_t)(q0+(i>>7))*4096+h*128+(i&127)]);
  __syncthreads();
  for(int k=t;k<2048;k+=256){ float a[16]={}; const u16* kp=Kc+((size_t)h*2048+k)*128;
    for(int d=0;d<128;d++){ float kv=bf2f(kp[d]); for(int i=0;i<16;i++) a[i]+=qs[i][d]*kv; }
    for(int i=0;i<16;i++) sc[i][k]=a[i]*0.08838834764f; }
  __syncthreads();
  for(int i=0;i<16;i++){ float m=-1e30f; for(int k=t;k<2048;k+=256) m=fmaxf(m,sc[i][k]);
    for(int o=32;o;o>>=1) m=fmaxf(m,__shfl_xor(m,o)); __syncthreads(); if((t&63)==0) sh[t>>6]=m; __syncthreads();
    m=fmaxf(fmaxf(sh[0],sh[1]),fmaxf(sh[2],sh[3])); float s=0; for(int k=t;k<2048;k+=256){ float e=__expf(sc[i][k]-m); sc[i][k]=e; s+=e; }
    s=bsum(s,sh); for(int k=t;k<2048;k+=256) sc[i][k]/=s; }
  __syncthreads();
  int d=t&127,hf=t>>7; float a[8]={};
  for(int k=0;k<2048;k++){ float v=bf2f(Vc[((size_t)h*2048+k)*128+d]); for(int i=0;i<8;i++) a[i]+=sc[hf*8+i][k]*v; }
  for(int i=0;i<8;i++) O[(size_t)(q0+hf*8+i)*4096+h*128+d]=f2bf(a[i]);
}
extern "C" void kernel_launch(void* const* d_in, const int* in_sizes, int n_in, void* d_out, int out_size, void* d_ws, size_t ws_size, hipStream_t stream) {
  const u16 *x=(const u16*)d_in[0],*kc=(const u16*)d_in[1],*vc=(const u16*)d_in[2],*wq=(const u16*)d_in[3],*wo=(const u16*)d_in[4];
  const u16 *w1=(const u16*)d_in[6],*b1=(const u16*)d_in[7],*w2=(const u16*)d_in[8],*b2=(const u16*)d_in[9],*c=(const u16*)d_in[10],*wm=(const u16*)d_in[11];
  char* ws=(char*)d_ws; float* mod=(float*)ws; u16* h=(u16*)(ws+(1<<20)); u16* q=(u16*)(ws+(8<<20)); u16* o=(u16*)(ws+(32<<20));
  float* x1=(float*)(ws+(64<<20)); u16* y=(u16*)(ws+(80<<20)); u16* h1=(u16*)(ws+(96<<20));
  k_mod<<<24,256,0,stream>>>(c,wm,mod);
  k_ln<<<S,256,0,stream>>>(nullptr,x,mod,mod+1024,h,0);
  k_gemm<<<dim3(4096/64,S/64),256,0,stream>>>(h,wq,S,4096,D,0,q,nullptr,nullptr,nullptr,nullptr,nullptr);
  k_attn<<<dim3(S/16,H),256,0,stream>>>(q,kc,vc,o);
  k_gemm<<<dim3(D/64,S/64),256,0,stream>>>(o,wo,S,D,4096,1,nullptr,x1,x,nullptr,mod+2048,nullptr);
  k_ln<<<S,256,0,stream>>>(x1,nullptr,mod+3072,mod+4096,y,1);
  k_gemm<<<dim3(F/64,S/64),256,0,stream>>>(y,w1,S,F,D,2,h1,nullptr,nullptr,nullptr,nullptr,b1);
  k_gemm<<<dim3(D/64,S/64),256,0,stream>>>(h1,w2,S,D,F,3,(u16*)d_out,nullptr,nullptr,x1,mod+5120,b2);
}
```

```cpp
#define SLP 2

#include <hip/hip_runtime.h>
typedef unsigned short u16;
typedef unsigned u32x2 __attribute__((ext_vector_type(2)));
typedef unsigned u32x4 __attribute__((ext_vector_type(4)));
typedef float f32x2 __attribute__((ext_vector_type(2)));
typedef float f32x4 __attribute__((ext_vector_type(4)));
typedef float f32x8 __attribute__((ext_vector_type(8)));
typedef float f32x16 __attribute__((ext_vector_type(16)));
typedef unsigned u32x16 __attribute__((ext_vector_type(16)));
typedef __bf16 bf16x2 __attribute__((ext_vector_type(2)));
typedef __attribute__((address_space(1))) unsigned gu32;
typedef __attribute__((address_space(1))) unsigned long long gu64;
#define DI __device__ __forceinline__
#define RLX_AGENT __ATOMIC_RELAXED, __HIP_MEMORY_SCOPE_AGENT
#define SRD(p, n) __builtin_amdgcn_make_buffer_rsrc((void*)(p), (short)0, (int)(n), 0x00020000)
DI float bflo(unsigned x) { return __uint_as_float(x << 16); }
DI float bfhi(unsigned x) { return __uint_as_float(x & 0xffff0000u); }
DI unsigned cvtpk(float lo, float hi) { f32x2 v = {lo, hi}; bf16x2 b = __builtin_convertvector(v, bf16x2); return __builtin_bit_cast(unsigned, b); }
DI u32x4 pack44(f32x4 a, f32x4 b) { return (u32x4){cvtpk(a[0], a[1]), cvtpk(a[2], a[3]), cvtpk(b[0], b[1]), cvtpk(b[2], b[3])}; }
DI u32x4 ld_sc1(__amdgpu_buffer_rsrc_t r, unsigned off, unsigned soff) { return __builtin_amdgcn_raw_buffer_load_b128(r, off, soff, 16); }
DI void st_sc1(u32x4 v, __amdgpu_buffer_rsrc_t r, unsigned off) { __builtin_amdgcn_raw_buffer_store_b128(v, r, off, 0, 16); }
#define BARRIER() asm volatile("s_waitcnt lgkmcnt(0)\n s_barrier" ::: "memory")
#define PUBLISH(flag) do { asm volatile("s_waitcnt vmcnt(0)\n s_barrier" ::: "memory"); \
    if (threadIdx.x < 4u) __hip_atomic_store((gu32*)(flag) + threadIdx.x * 32u, 1u, RLX_AGENT); } while (0)
#define PIN(x) asm volatile("" : "+v"(x))
DI f32x2 up2(unsigned x) { return (f32x2){bflo(x), bfhi(x)}; }
DI f32x2 fma2(f32x2 a, f32x2 b, f32x2 c) { return __builtin_elementwise_fma(a, b, c); }
DI f32x2 bc2(float a) { return (f32x2){a, a}; }
DI unsigned pk2(f32x2 v) { bf16x2 b = __builtin_convertvector(v, bf16x2); return __builtin_bit_cast(unsigned, b); }
DI float dot2(unsigned a, unsigned b, float c) { return __builtin_amdgcn_fdot2_f32_bf16(__builtin_bit_cast(bf16x2, a), __builtin_bit_cast(bf16x2, b), c, false); }
DI f32x2 add2(f32x2 acc, unsigned x) {
  unsigned s0 = 0x00003f80u, s1 = 0x3f800000u;
  asm("" : "+s"(s0), "+s"(s1));
  const bf16x2 xb = __builtin_bit_cast(bf16x2, x);
  return (f32x2){__builtin_amdgcn_fdot2_f32_bf16(xb, __builtin_bit_cast(bf16x2, s0), acc[0], false),
                 __builtin_amdgcn_fdot2_f32_bf16(xb, __builtin_bit_cast(bf16x2, s1), acc[1], false)};
}
constexpr unsigned WS_MOD = 0, WS_CNT = 131072, WS_M2 = 196608, WS_TAG = 262144, WS_EP = 327680, WS_H = 2u << 20, WS_O = 8u << 20, WS_P1 = 24u << 20, WS_Y = WS_H, WS_X1 = 40u << 20, WS_H1 = WS_O, WS_P2 = WS_P1;
constexpr unsigned L_ST = 131072 + 64, L_X = 132096, L_PAR = 148480, L_END = 162816;

DI float xor8(float x) { return __uint_as_float(__builtin_amdgcn_update_dpp(0u, __float_as_uint(x), 0x128  , 0xf, 0xf, false)); }
DI void row_stats(const f32x2 (&v)[16], f32x2* sh, unsigned t, float& mean, float& rstd) {
  f32x2 s0 = v[0], s1 = v[1];
#pragma unroll
  for (int i = 2; i < 16; i += 2) { s0 += v[i]; s1 += v[i + 1]; }
  s0 += s1;
  float m = (s0[0] + s0[1]) * (1.f / 32.f);
  f32x2 q0 = {0.f, 0.f}, q1 = {0.f, 0.f};
#pragma unroll
  for (int i = 0; i < 16; i += 2) { const f32x2 d0 = v[i] - m, d1 = v[i + 1] - m; q0 = fma2(d0, d0, q0); q1 = fma2(d1, d1, q1); }
  q0 += q1;
  float q = q0[0] + q0[1];
  {
    const float mo = xor8(m), qo = xor8(q), d = mo - m;
    q = q + qo + d * d * 16.f; m = 0.5f * (m + mo);
  }
  {
    auto rm = __builtin_amdgcn_permlane16_swap(__float_as_uint(m), __float_as_uint(m), false, false);
    auto rq = __builtin_amdgcn_permlane16_swap(__float_as_uint(q), __float_as_uint(q), false, false);
    const float a = __uint_as_float(rm[0]), b = __uint_as_float(rm[1]), d = a - b;
    q = __uint_as_float(rq[0]) + __uint_as_float(rq[1]) + d * d * 32.f; m = 0.5f * (a + b);
  }
  {
    auto rm = __builtin_amdgcn_permlane32_swap(__float_as_uint(m), __float_as_uint(m), false, false);
    auto rq = __builtin_amdgcn_permlane32_swap(__float_as_uint(q), __float_as_uint(q), false, false);
    const float a = __uint_as_float(rm[0]), b = __uint_as_float(rm[1]), d = a - b;
    q = __uint_as_float(rq[0]) + __uint_as_float(rq[1]) + d * d * 64.f; m = 0.5f * (a + b);
  }
  if ((t & 63u) < 8u) sh[(t & 7u) * 4u + (t >> 6)] = (f32x2){m, q};
  BARRIER();
  const f32x4 pa = *(const f32x4*)(sh + (t & 7u) * 4u), pb = *(const f32x4*)(sh + (t & 7u) * 4u + 2u);
  mean = 0.25f * ((pa[0] + pa[2]) + (pb[0] + pb[2]));
  const float d0 = pa[0] - mean, d1 = pa[2] - mean, d2 = pb[0] - mean, d3 = pb[2] - mean;
  const float m2 = (pa[1] + pa[3]) + (pb[1] + pb[3]) + 256.f * ((d0 * d0 + d1 * d1) + (d2 * d2 + d3 * d3));
  rstd = __builtin_amdgcn_rsqf(m2 * (1.f / 1024.f) + 1e-5f);
}

__global__ __launch_bounds__(256, 1) void k_main(const u16* __restrict__ x, const char* __restrict__ kc, const char* __restrict__ vc, const char* __restrict__ wq,
                                                 const char* __restrict__ wo, const char* __restrict__ w1, const u16* __restrict__ b1, const char* __restrict__ w2,
                                                 const u16* __restrict__ b2, const u16* __restrict__ cin, const char* __restrict__ wm, char* ws, u16* __restrict__ out) {
  __shared__ __attribute__((aligned(1024))) char lds[L_END];
  const unsigned w = __builtin_amdgcn_readfirstlane(threadIdx.x >> 6);
  const unsigned xcd = blockIdx.x & 7u, idx = blockIdx.x >> 3;
  const unsigned sk = xcd & 3u, qblk = (xcd >> 2) * 4u + (idx >> 3), jj = idx & 7u, head = sk * 8u + jj;
  const unsigned mb0 = qblk * 8u + 2u * w;
  const unsigned row0 = qblk * 256u + head * 8u;
  unsigned* cq = (unsigned*)(ws + WS_CNT) + qblk * 1024u;
  const unsigned w4k = w * 4096u + (unsigned)(size_t)lds;
  const __amdgpu_buffer_rsrc_t srdW1 = SRD(w1 + head * 224u - (head & 1u) * 32u, 1024u * 7168u - head * 224u + (head & 1u) * 32u);
  asm volatile("" :: "s"(x), "s"(kc), "s"(vc), "s"(wq), "s"(wo), "s"(w1), "s"(b1), "s"(w2), "s"(b2), "s"(cin), "s"(wm), "s"(ws), "s"(out));
  {
    const unsigned t = threadIdx.x, lane = t & 63u;
    unsigned long long pc0; unsigned clen;
    asm volatile("s_getpc_b64 %0\nK_MAIN_CODE_BEGIN:\ns_mov_b32 %1, K_MAIN_CODE_END-K_MAIN_CODE_BEGIN" : "=s"(pc0), "=s"(clen));
    const unsigned coff = ((idx * 4u + w) * 6u + (lane % 6u)) * 128u;
    const unsigned long long vCA = pc0 + (coff < clen ? coff : 0u);
    const unsigned gq = lane >> 4, q = (lane >> 2) & 3u, p = lane & 3u, rr = 4u * gq + q, c16 = lane & 15u;
    const __amdgpu_buffer_rsrc_t srdW = SRD(wq + head * 256u, 1024u * 8192u - head * 256u);
    const __amdgpu_buffer_rsrc_t srdK = SRD(kc + (size_t)head * 524288u, 524288);
    const unsigned vWD = 8192u * gq + 16u * (c16 ^ (gq << 1)), vKD = 256u * gq + 16u * (c16 ^ gq);
    const unsigned row = row0 + (t & 7u), c0 = t >> 3;
    const unsigned gb_ = xcd * 24u + idx, gks = gb_ / 48u, gstrip = gb_ % 48u;
    const bool gact = idx < 24u;
    u32x16 xr, g0, g1, g2, g3;
    u32x2 epv, b2v;
    unsigned cvv;
    {
      const __amdgpu_buffer_rsrc_t srdXi = SRD(x + (size_t)row0 * 1024u, 16384);
      const __amdgpu_buffer_rsrc_t srdG0 = SRD(wm + (gact ? (size_t)gks * (256u * 12288u) + gstrip * 256u : 0), gact ? 256u * 12288u - gstrip * 256u : 0u);
      const unsigned vXo = (t & 7u) * 2048u + (c0 >> 2) * 64u + (c0 & 3u) * 8u, vG = (t >> 4) * 12288u + (t & 15u) * 16u;
      const unsigned vB2 = (t >> 3) * 64u + (t & 1u) * 32u + ((t >> 1) & 3u) * 8u, vC = ((gact ? gks : 0u) * 256u + t) * 2u;
      const char* ep = ws + WS_EP;
  asm volatile(
"global_load_dword v228, %12, off\n"
"v_mov_b32 v83, 0\n"
"global_load_dwordx2 v[80:81], v83, %15 sc1\n"
"global_load_ushort v82, %16, %17\n"
"s_mov_b32 s65, 0\n"
"buffer_load_dwordx4 v[16:19], %11, %10, s65 offen nt\n"
"s_mov_b32 s65, 196608\n"
"buffer_load_dwordx4 v[20:23], %11, %10, s65 offen nt\n"
"s_mov_b32 s65, 393216\n"
"buffer_load_dwordx4 v[24:27], %11, %10, s65 offen nt\n"
"s_mov_b32 s65, 589824\n"
"buffer_load_dwordx4 v[28:31], %11, %10, s65 offen nt\n"
"s_mov_b32 s65, 786432\n"
"buffer_load_dwordx4 v[32:35], %11, %10, s65 offen nt\n"
"s_mov_b32 s65, 983040\n"
"buffer_load_dwordx4 v[36:39], %11, %10, s65 offen nt\n"
"s_mov_b32 s65, 1179648\n"
"buffer_load_dwordx4 v[40:43], %11, %10, s65 offen nt\n"
"s_mov_b32 s65, 1376256\n"
"buffer_load_dwordx4 v[44:47], %11, %10, s65 offen nt\n"
"s_mov_b32 s65, 1572864\n"
"buffer_load_dwordx4 v[48:51], %11, %10, s65 offen nt\n"
"s_mov_b32 s65, 1769472\n"
"buffer_load_dwordx4 v[52:55], %11, %10, s65 offen nt\n"
"s_mov_b32 s65, 1966080\n"
"buffer_load_dwordx4 v[56:59], %11, %10, s65 offen nt\n"
"s_mov_b32 s65, 2162688\n"
"buffer_load_dwordx4 v[60:63], %11, %10, s65 offen nt\n"
"s_mov_b32 s65, 2359296\n"
"buffer_load_dwordx4 v[64:67], %11, %10, s65 offen nt\n"
"s_mov_b32 s65, 2555904\n"
"buffer_load_dwordx4 v[68:71], %11, %10, s65 offen nt\n"
"s_mov_b32 s65, 2752512\n"
"buffer_load_dwordx4 v[72:75], %11, %10, s65 offen nt\n"
"s_mov_b32 s65, 2949120\n"
"buffer_load_dwordx4 v[76:79], %11, %10, s65 offen nt\n"
"buffer_load_dwordx2 v[0:1], %9, %8, 0 offen offset:0 nt\n"
"buffer_load_dwordx2 v[2:3], %9, %8, 0 offen offset:32 nt\n"
"buffer_load_dwordx2 v[4:5], %9, %8, 0 offen offset:512 nt\n"
"buffer_load_dwordx2 v[6:7], %9, %8, 0 offen offset:544 nt\n"
"buffer_load_dwordx2 v[8:9], %9, %8, 0 offen offset:1024 nt\n"
"buffer_load_dwordx2 v[10:11], %9, %8, 0 offen offset:1056 nt\n"
"buffer_load_dwordx2 v[12:13], %9, %8, 0 offen offset:1536 nt\n"
"buffer_load_dwordx2 v[14:15], %9, %8, 0 offen offset:1568 nt\n"
"global_load_dwordx2 v[84:85], %13, %14\n"
    : "=&{v[0:15]}"(xr), "=&{v[16:31]}"(g0), "=&{v[32:47]}"(g1), "=&{v[48:63]}"(g2), "=&{v[64:79]}"(g3), "=&{v[80:81]}"(epv), "=&{v82}"(cvv), "=&{v[84:85]}"(b2v)
    : "s"(srdXi), "v"(vXo), "s"(srdG0), "v"(vG), "v"(vCA), "v"(vB2), "s"(b2), "s"(ep), "v"(vC), "s"(cin)
    : "v83", "v228", "s65", "scc", "memory");
    }
#pragma unroll
    for (unsigned k = 0; k < 9; ++k) {
      const unsigned ln = (k * 128u + (xcd * 8u + idx - 24u) * 2u + w - 2u) * 64u + lane, lv = ln - 12288u;
      const bool on = w >= 2u && !gact && ln < 81920u;
      const unsigned long long ta = (unsigned long long)(!on ? (const char*)cin : ln >= 16384u ? wq + (size_t)(ln - 16384u) * 128u
                                                         : ln < 12288u ? kc + (size_t)(ln / 384u) * 524288u + (ln % 384u) * 128u : vc + (size_t)(lv >> 7) * 524288u + (lv & 127u) * 128u);
      asm volatile("global_load_dword v228, %0, off" :: "v"(ta) : "v228", "memory");
    }
    asm volatile("s_waitcnt vmcnt(34)" : "+{v[80:81]}"(epv), "+{v82}"(cvv) :: "memory");
    unsigned* gsc = (unsigned*)(lds + L_PAR + 4096u);
    float* gpart = (float*)(lds + L_PAR + 6144u);
    {
      const float cx = bflo(cvv);
      gsc[(t & 15u) * 16u + (t >> 4)] = cvtpk(cx * __builtin_amdgcn_rcpf(1.f + __expf(-cx)), 0.f);
    }
    const unsigned long long tag = (((unsigned long long)epv[1] << 32) | epv[0]) + 1ull;
    if (t < 20u) __hip_atomic_store((gu32*)(cq + (t < 16u ? (((t >> 2) * 3u + 1u) >> 1) * 128u + (t & 3u) * 32u + head : ((t & 1u) ? 128u : 512u) + (t & 2u) * 4u + sk * 32u + jj)), 0u, RLX_AGENT);
    BARRIER();
    {
      u32x4 sl[4], sh[4];
#pragma unroll
      for (int i = 0; i < 4; ++i) sl[i] = *(const u32x4*)(gsc + (t >> 4) * 16u + 4 * i);
#pragma unroll
      for (int i = 0; i < 4; ++i) { sh[i] = sl[i] << 16; PIN(sh[i]); }
      f32x2 a[4] = {};
#define GSTEP(G, R, N, I) asm volatile("s_waitcnt vmcnt(" #N ")" : "+{" R "}"(G) :: "memory");       _Pragma("unroll") for (int i = 0; i < 4; ++i) _Pragma("unroll") for (int e = 0; e < 4; ++e) {         a[e][0] = dot2(G[4 * i + e], sl[I][i], a[e][0]);         a[e][1] = dot2(G[4 * i + e], sh[I][i], a[e][1]); }       PIN(a[0]); PIN(a[1]); PIN(a[2]); PIN(a[3]);
      GSTEP(g0, "v[16:31]", 30, 0) GSTEP(g1, "v[32:47]", 26, 1) GSTEP(g2, "v[48:63]", 22, 2) GSTEP(g3, "v[64:79]", 18, 3)
      float r8[8];
#pragma unroll
      for (int e = 0; e < 8; ++e) {
        const float ae = a[e >> 1][e & 1];
        auto r = __builtin_amdgcn_permlane16_swap(__float_as_uint(ae), __float_as_uint(ae), false, false);
        const float u = __uint_as_float(r[0]) + __uint_as_float(r[1]);
        auto r2 = __builtin_amdgcn_permlane32_swap(__float_as_uint(u), __float_as_uint(u), false, false);
        r8[e] = __uint_as_float(r2[0]) + __uint_as_float(r2[1]);
      }
      if (lane < 16u) { *(f32x4*)(gpart + w * 128u + lane * 8u) = (f32x4){r8[0], r8[1], r8[2], r8[3]}; *(f32x4*)(gpart + w * 128u + lane * 8u + 4u) = (f32x4){r8[4], r8[5], r8[6], r8[7]}; }
    }
    BARRIER();
    if (w == 0u) {
      if (gact) {
        const unsigned col = 2u * lane;
        const f32x2 sum = (*(const f32x2*)(gpart + col) + *(const f32x2*)(gpart + 128u + col)) + (*(const f32x2*)(gpart + 256u + col) + *(const f32x2*)(gpart + 384u + col));
        __hip_atomic_store((gu64*)(ws + WS_MOD + (gks * 6144u + gstrip * 128u + (col & 96u) + ((col >> 2) & 3u) * 8u + (col & 3u) + 4u * ((col >> 4) & 1u)) * 4u),
                           __builtin_bit_cast(unsigned long long, sum), RLX_AGENT);
      }
      asm volatile("s_waitcnt vmcnt(0)" ::: "memory");
      if (lane < 8u) __hip_atomic_store((gu64*)(ws + WS_TAG + lane * 2048u + blockIdx.x * 8u), tag, RLX_AGENT);
    }
    asm volatile("s_waitcnt vmcnt(10)" : "+{v[0:15]}"(xr) :: "memory");
    f32x2 v[16];
#pragma unroll
    for (int i = 0; i < 16; ++i) v[i] = up2(xr[i]);
    float mean, rstd;
    row_stats(v, (f32x2*)(lds + L_ST), t, mean, rstd);
    const float QS = 0.12751743082f;
    {
      const float ra = rstd * QS, rb = -mean * ra;
#pragma unroll
      for (int i = 0; i < 16; ++i) { v[i] = fma2(v[i], bc2(ra), bc2(rb)); PIN(v[i]); }
    }
#pragma unroll
    for (int i = 0; i < 4; ++i) *(u32x4*)(lds + L_X + (i * 256u + t) * 16u) = (u32x4){xr[4 * i], xr[4 * i + 1], xr[4 * i + 2], xr[4 * i + 3]};
    f32x8 mq = {};
    if (w < 2u) {
      const __amdgpu_buffer_rsrc_t srdT = SRD(ws + WS_TAG + xcd * 2048u, 2048), srdM = SRD(ws + WS_MOD, 4u * 24576u);
#pragma nounroll
      for (unsigned spins = 0; spins < (1u << 18); ++spins) {
        const u32x4 f0 = ld_sc1(srdT, lane * 16u, 0), f1 = ld_sc1(srdT, lane * 16u, 1024);
        const unsigned lo = (unsigned)tag, hi = (unsigned)(tag >> 32);
        const unsigned d = ((f0[0] ^ lo) | (f0[1] ^ hi) | (f0[2] ^ lo) | (f0[3] ^ hi)) | ((f1[0] ^ lo) | (f1[1] ^ hi) | (f1[2] ^ lo) | (f1[3] ^ hi));
        if (__builtin_amdgcn_ballot_w64(d != 0u) == 0ull) break;
        __builtin_amdgcn_s_sleep(SLP);
      }
      asm volatile("" ::: "memory");
      if (blockIdx.x == 0u && t == 0u) __hip_atomic_store((gu64*)(ws + WS_EP), tag, RLX_AGENT);
      constexpr int NH = 2;
      u32x4 mp[NH][8];
#pragma unroll
      for (int h = 0; h < NH; ++h)
#pragma unroll
        for (int k = 0; k < 8; ++k) mp[h][k] = ld_sc1(srdM, t * 32u + h * 4096u + (k & 1) * 16u, (k >> 1) * 24576u);
#pragma unroll
      for (int k = 0; k < 4; ++k) {
        const u32x2 r = __builtin_amdgcn_raw_buffer_load_b64(srdM, 8192u + head * 512u + lane * 8u, k * 24576u, 16);
        mq[2 * k] = __uint_as_float(r[0]); mq[2 * k + 1] = __uint_as_float(r[1]);
      }
#pragma unroll
      for (int h = 0; h < NH; ++h) {
        f32x4 s[2];
#pragma unroll
        for (int k = 0; k < 2; ++k)
          s[k] = (__builtin_bit_cast(f32x4, mp[h][k]) + __builtin_bit_cast(f32x4, mp[h][2 + k])) + (__builtin_bit_cast(f32x4, mp[h][4 + k]) + __builtin_bit_cast(f32x4, mp[h][6 + k]));
        *(u32x4*)(lds + L_PAR + h * 2048u + t * 16u) = pack44(s[0], s[1]);
      }
    }
    BARRIER();
    const __amdgpu_buffer_rsrc_t srdHw = SRD(ws + WS_H + (size_t)(row0 >> 5) * 65536u, 65536);
    const unsigned poff = ((row >> 4) & 1u) * 1024u + (row & 15u) * 16u + (c0 >> 2) * 2048u + (c0 & 3u) * 256u;
    u32x4 sa[4], ca[4];
#pragma unroll
    for (int i = 0; i < 4; ++i) { sa[i] = *(const u32x4*)(lds + L_PAR + (c0 + 32u * i) * 16u); ca[i] = *(const u32x4*)(lds + L_PAR + 2048u + (c0 + 32u * i) * 16u); }
#pragma unroll
    for (int i = 0; i < 4; ++i) {
      u32x4 o;
#pragma unroll
      for (int e = 0; e < 4; ++e) { const f32x2 n = v[4 * i + e]; o[e] = pk2(fma2(up2(sa[i][e]), bc2(QS), fma2(n, up2(ca[i][e]), n))); }
      st_sc1(o, srdHw, poff + i * 16384u);
    }
    if (t < 64u) {
      const float s2 = (mq[0] + mq[2]) + (mq[4] + mq[6]), s3 = (mq[1] + mq[3]) + (mq[5] + mq[7]);
      __hip_atomic_store((gu32*)(ws + WS_M2 + qblk * 8192u + head * 256u + t * 4u), cvtpk(s2, s3), RLX_AGENT);
    }
    asm volatile("s_waitcnt vmcnt(0)" : "+{v[84:85]}"(b2v) :: "memory");
    *(u32x2*)(lds + L_PAR + 12288u + t * 8u) = b2v;
    PUBLISH(cq + head);
  asm volatile(
"v_xor_b32 v221, 128, %2\n"
"v_add_u32 v221, 31744, v221\n"
"v_xor_b32 v223, 128, %2\n"
"v_add_u32 v223, 95232, v223\n"
"v_add_u32 v222, 63488, %2\n"
"v_add_u32 v220, 0, %2\n"
"v_xor_b32 v204, 0, %4\n"
"v_xor_b32 v205, 64, %4\n"
"v_xor_b32 v206, 128, %4\n"
"v_xor_b32 v207, 192, %4\n"
"s_lshl_b32 s64, %1, 5\n"
"s_add_u32 m0, %1, 65536\n"
"s_nop 0\n"
"buffer_load_dwordx4 v220, %0, s64 offen offset:0 lds\n"
"buffer_load_dwordx4 v221, %0, s64 offen offset:1024 lds\n"
"buffer_load_dwordx4 v222, %0, s64 offen offset:2048 lds\n"
"buffer_load_dwordx4 v223, %0, s64 offen offset:3072 lds\n"
"s_add_u32 s64, s64, 524288\n"
"s_add_u32 m0, %1, 81920\n"
"s_nop 0\n"
"buffer_load_dwordx4 v220, %0, s64 offen offset:0 lds\n"
"buffer_load_dwordx4 v221, %0, s64 offen offset:1024 lds\n"
"buffer_load_dwordx4 v222, %0, s64 offen offset:2048 lds\n"
"buffer_load_dwordx4 v223, %0, s64 offen offset:3072 lds\n"
    :
    : "s"(srdW), "s"(w4k), "v"(vWD), "s"(srdK), "v"(vKD)
    : "v204", "v205", "v206", "v207", "v220", "v221", "v222", "v223", "s64", "s72", "s74", "scc", "memory");
    const __amdgpu_buffer_rsrc_t srdH = SRD(ws + WS_H + (size_t)mb0 * 65536u, 131072);
    const __amdgpu_buffer_rsrc_t srdV = SRD(vc + (size_t)head * 524288u - 32768, 524288 + 32768);
    const __amdgpu_buffer_rsrc_t srdO = SRD(ws + WS_O + (size_t)mb0 * 262144u + head * 8192u, 262144 + 8192);
    const __amdgpu_buffer_rsrc_t srdOX = SRD(ws + WS_O + (size_t)mb0 * 262144u + sk * 65536u, 262144 + 65536);
    const __amdgpu_buffer_rsrc_t srdWO = SRD(wo + (size_t)sk * (1024u * 2048u) + jj * 256u, 1024u * 2048u - jj * 256u);
    const __amdgpu_buffer_rsrc_t srdP = SRD(ws + WS_P1 + (size_t)sk * (4u << 20) + (size_t)mb0 * 65536u + jj * 8192u, 65536 + 8192);
    unsigned* cH = cq + sk * 32u + w * 8u;
    unsigned* cO = cq + 128u + sk * 32u;
    const unsigned vK0 = c16 * 256u + 16u * (gq ^ c16);
    const unsigned vV0 = 65536u + rr * 256u + 32u * (rr & 7u) + 8u * p, vW0 = 65536u + rr * 256u + 32u * ((p >> 1) ^ (rr & 7u)) + 16u * (p & 1u);
    const unsigned vVD = 256u * gq + 16u * (c16 ^ (gq << 1)), vL16 = lane * 16u;
    const unsigned gw = blockIdx.x * 4u + w;
    const unsigned long long vPS = lane >= 12u ? 0ull : lane >= 4u ? 112ull : 128ull;
    const unsigned long long vPA = lane >= 32u ? vCA : (unsigned long long)(lane < 4u ? wo + (size_t)(gw * 4u + lane) * 2048u : lane < 12u ? (lane < 8u ? w1 : w2) + (size_t)(gw * 4u + (lane & 3u)) * 1792u
                                                       : (const char*)b1 + head * 224u + (lane & 1u) * 128u + ((lane & 2u) ? 92u : 0u));
    {
      u32x4 pd;
#pragma unroll
      for (int i = 0; i < 4; ++i) {
        const unsigned drow = 4u * i + gq, ch = c16 ^ ((drow & 7u) << 1), blk = ch >> 1, sb_ = (head & 1u) ? (blk < 6u ? blk + 2u : 7u - blk) : blk;
        pd[i] = (w * 16u + drow) * 7168u + 32u * sb_ + 16u * (ch & 1u) - 1024u * i;
      }
      *(u32x4*)(lds + L_PAR + t * 16u) = pd;
    }
  asm volatile(
"s_waitcnt lgkmcnt(0)\n"
"v_xor_b32 v221, 128, %18\n"
"v_add_u32 v221, 31744, v221\n"
"v_xor_b32 v223, 128, %18\n"
"v_add_u32 v223, 95232, v223\n"
"v_add_u32 v222, 63488, %18\n"
"v_add_u32 v220, 0, %18\n"
"v_xor_b32 v204, 0, %16\n"
"v_xor_b32 v205, 64, %16\n"
"v_xor_b32 v206, 128, %16\n"
"v_xor_b32 v207, 192, %16\n"
"s_lshl_b32 s64, %11, 5\n"
"s_mov_b32 s65, 0\n"
"s_mov_b32 s69, 0\n"
"s_mov_b32 s70, 4096\n"
"s_add_u32 s64, s64, 524288\n"
"v_xor_b32 v210, 0, %15\n"
"v_xor_b32 v211, 64, %15\n"
"v_xor_b32 v212, 128, %15\n"
"v_xor_b32 v213, 192, %15\n"
"v_mov_b32 v218, %19\n"
"v_add_u32 v219, 65536, %19\n"
"v_mov_b32 v208, %17\n"
"v_xor_b32 v209, 128, %17\n"
"v_xor_b32 v192, 0, %13\n"
"v_xor_b32 v193, 64, %13\n"
"v_xor_b32 v194, 128, %13\n"
"v_xor_b32 v195, 192, %13\n"
"v_xor_b32 v196, 0, %14\n"
"v_xor_b32 v197, 32, %14\n"
"v_xor_b32 v198, 64, %14\n"
"v_xor_b32 v199, 96, %14\n"
"v_xor_b32 v200, 128, %14\n"
"v_xor_b32 v201, 160, %14\n"
"v_xor_b32 v202, 192, %14\n"
"v_xor_b32 v203, 224, %14\n"
"v_and_b32 v237, 32, %19\n"
"v_cmp_eq_u32 vcc, 0, v237\n"
"v_mov_b32 v237, 0x3f803f80\n"
"v_and_b32 v238, 16, %19\n"
"v_mov_b32 v239, 0x44444444\n"
"v_cndmask_b32 v236, 0, v237, vcc\n"
"v_cmp_eq_u32 vcc, 0, v238\n"
"v_mov_b32 v229, 0xeeeeeeee\n"
"s_nop 0\n"
"v_cndmask_b32 v229, v229, v239, vcc\n"
"v_accvgpr_write_b32 a128, 0\n"
"v_accvgpr_write_b32 a129, 0\n"
"v_accvgpr_write_b32 a130, 0\n"
"v_accvgpr_write_b32 a131, 0\n"
"v_accvgpr_write_b32 a132, 0\n"
"v_accvgpr_write_b32 a133, 0\n"
"v_accvgpr_write_b32 a134, 0\n"
"v_accvgpr_write_b32 a135, 0\n"
"v_accvgpr_write_b32 a136, 0\n"
"v_accvgpr_write_b32 a137, 0\n"
"v_accvgpr_write_b32 a138, 0\n"
"v_accvgpr_write_b32 a139, 0\n"
"v_accvgpr_write_b32 a140, 0\n"
"v_accvgpr_write_b32 a141, 0\n"
"v_accvgpr_write_b32 a142, 0\n"
"v_accvgpr_write_b32 a143, 0\n"
"v_accvgpr_write_b32 a144, v236\n"
"v_accvgpr_write_b32 a145, v236\n"
"v_accvgpr_write_b32 a146, v236\n"
"v_accvgpr_write_b32 a147, v236\n"
"s_mov_b32 s71, 0x40000\n"
"v_bfe_u32 v233, %19, 2, 5\n"
"PH%=:\n"
"global_load_dword v232, v233, %9 offset:0 sc1\n"
"s_waitcnt vmcnt(0)\n"
"v_cmp_eq_u32 vcc, 0, v232\n"
"s_sub_u32 s71, s71, 1\n"
"s_cbranch_scc1 PHX%=\n"
"s_cmp_eq_u64 vcc, 0\n"
"s_cbranch_scc1 PHX%=\n"
"s_sleep 2\n"
"s_branch PH%=\n"
"PHX%=:\n"
"buffer_load_dwordx4 a[0:3], v218, %0, s65 offen offset:0 sc1\n"
"buffer_load_dwordx4 a[4:7], v218, %0, s65 offen offset:1024 sc1\n"
"buffer_load_dwordx4 a[8:11], v219, %0, s65 offen offset:0 sc1\n"
"buffer_load_dwordx4 a[12:15], v219, %0, s65 offen offset:1024 sc1\n"
"buffer_load_dwordx4 a[16:19], v218, %0, s65 offen offset:2048 sc1\n"
"buffer_load_dwordx4 a[20:23], v218, %0, s65 offen offset:3072 sc1\n"
"buffer_load_dwordx4 a[24:27], v219, %0, s65 offen offset:2048 sc1\n"
"buffer_load_dwordx4 a[28:31], v219, %0, s65 offen offset:3072 sc1\n"
"s_add_u32 s65, s65, 4096\n"
"buffer_load_dwordx4 a[32:35], v218, %0, s65 offen offset:0 sc1\n"
"buffer_load_dwordx4 a[36:39], v218, %0, s65 offen offset:1024 sc1\n"
"buffer_load_dwordx4 a[40:43], v219, %0, s65 offen offset:0 sc1\n"
"buffer_load_dwordx4 a[44:47], v219, %0, s65 offen offset:1024 sc1\n"
"buffer_load_dwordx4 a[48:51], v218, %0, s65 offen offset:2048 sc1\n"
"buffer_load_dwordx4 a[52:55], v218, %0, s65 offen offset:3072 sc1\n"
"buffer_load_dwordx4 a[56:59], v219, %0, s65 offen offset:2048 sc1\n"
"buffer_load_dwordx4 a[60:63], v219, %0, s65 offen offset:3072 sc1\n"
"s_waitcnt vmcnt(22)\n"
"s_barrier\n"
"ds_read_b64_tr_b16 a[224:225], v210 offset:0\n"
"ds_read_b64_tr_b16 a[226:227], v210 offset:4096\n"
"ds_read_b64_tr_b16 a[228:229], v210 offset:8192\n"
"ds_read_b64_tr_b16 a[230:231], v210 offset:12288\n"
"ds_read_b64_tr_b16 a[232:233], v210 offset:8\n"
"ds_read_b64_tr_b16 a[234:235], v210 offset:4104\n"
"ds_read_b64_tr_b16 a[236:237], v210 offset:8200\n"
"ds_read_b64_tr_b16 a[238:239], v210 offset:12296\n"
"v_mov_b64 v[0:1], 0\n"
"v_mov_b64 v[2:3], 0\n"
"v_mov_b64 v[4:5], 0\n"
"v_mov_b64 v[6:7], 0\n"
"v_mov_b64 v[8:9], 0\n"
"v_mov_b64 v[10:11], 0\n"
"v_mov_b64 v[12:13], 0\n"
"v_mov_b64 v[14:15], 0\n"
"v_mov_b64 v[16:17], 0\n"
"v_mov_b64 v[18:19], 0\n"
"v_mov_b64 v[20:21], 0\n"
"v_mov_b64 v[22:23], 0\n"
"v_mov_b64 v[24:25], 0\n"
"v_mov_b64 v[26:27], 0\n"
"v_mov_b64 v[28:29], 0\n"
"v_mov_b64 v[30:31], 0\n"
"v_mov_b64 v[32:33], 0\n"
"v_mov_b64 v[34:35], 0\n"
"v_mov_b64 v[36:37], 0\n"
"v_mov_b64 v[38:39], 0\n"
"v_mov_b64 v[40:41], 0\n"
"v_mov_b64 v[42:43], 0\n"
"v_mov_b64 v[44:45], 0\n"
"v_mov_b64 v[46:47], 0\n"
"v_mov_b64 v[48:49], 0\n"
"v_mov_b64 v[50:51], 0\n"
"v_mov_b64 v[52:53], 0\n"
"v_mov_b64 v[54:55], 0\n"
"v_mov_b64 v[56:57], 0\n"
"v_mov_b64 v[58:59], 0\n"
"v_mov_b64 v[60:61], 0\n"
"v_mov_b64 v[62:63], 0\n"
"v_mov_b64 v[64:65], 0\n"
"v_mov_b64 v[66:67], 0\n"
"v_mov_b64 v[68:69], 0\n"
"v_mov_b64 v[70:71], 0\n"
"v_mov_b64 v[72:73], 0\n"
"v_mov_b64 v[74:75], 0\n"
"v_mov_b64 v[76:77], 0\n"
"v_mov_b64 v[78:79], 0\n"
"v_mov_b64 v[80:81], 0\n"
"v_mov_b64 v[82:83], 0\n"
"v_mov_b64 v[84:85], 0\n"
"v_mov_b64 v[86:87], 0\n"
"v_mov_b64 v[88:89], 0\n"
"v_mov_b64 v[90:91], 0\n"
"v_mov_b64 v[92:93], 0\n"
"v_mov_b64 v[94:95], 0\n"
"v_mov_b64 v[96:97], 0\n"
"v_mov_b64 v[98:99], 0\n"
"v_mov_b64 v[100:101], 0\n"
"v_mov_b64 v[102:103], 0\n"
"v_mov_b64 v[104:105], 0\n"
"v_mov_b64 v[106:107], 0\n"
"v_mov_b64 v[108:109], 0\n"
"v_mov_b64 v[110:111], 0\n"
"v_mov_b64 v[112:113], 0\n"
"v_mov_b64 v[114:115], 0\n"
"v_mov_b64 v[116:117], 0\n"
"v_mov_b64 v[118:119], 0\n"
"v_mov_b64 v[120:121], 0\n"
"v_mov_b64 v[122:123], 0\n"
"v_mov_b64 v[124:125], 0\n"
"v_mov_b64 v[126:127], 0\n"
"s_mov_b32 s66, 3\n"
"LOOP_Q%=:\n"
"s_waitcnt vmcnt(8) lgkmcnt(4)\n"
"v_mfma_f32_16x16x32_bf16 v[0:3], a[224:227], a[0:3], v[0:3]\n"
"ds_read_b64_tr_b16 a[240:241], v211 offset:0\n"
"v_mfma_f32_16x16x32_bf16 v[0:3], a[228:231], a[16:19], v[0:3]\n"
"ds_read_b64_tr_b16 a[242:243], v211 offset:4096\n"
"v_mfma_f32_16x16x32_bf16 v[8:11], a[228:231], a[20:23], v[8:11]\n"
"v_mfma_f32_16x16x32_bf16 v[8:11], a[224:227], a[4:7], v[8:11]\n"
"v_mfma_f32_16x16x32_bf16 v[16:19], a[224:227], a[8:11], v[16:19]\n"
"ds_read_b64_tr_b16 a[244:245], v211 offset:8192\n"
"s_add_u32 s64, s64, 524288\n"
"v_mfma_f32_16x16x32_bf16 v[16:19], a[228:231], a[24:27], v[16:19]\n"
"ds_read_b64_tr_b16 a[246:247], v211 offset:12288\n"
"s_add_u32 m0, %11, 98304\n"
"v_mfma_f32_16x16x32_bf16 v[24:27], a[228:231], a[28:31], v[24:27]\n"
"buffer_load_dwordx4 v220, %1, s64 offen offset:0 lds\n"
"v_mfma_f32_16x16x32_bf16 v[24:27], a[224:227], a[12:15], v[24:27]\n"
"s_waitcnt lgkmcnt(4)\n"
"v_mfma_f32_16x16x32_bf16 v[28:31], a[232:235], a[12:15], v[28:31]\n"
"ds_read_b64_tr_b16 a[248:249], v211 offset:8\n"
"v_mfma_f32_16x16x32_bf16 v[28:31], a[236:239], a[28:31], v[28:31]\n"
"ds_read_b64_tr_b16 a[250:251], v211 offset:4104\n"
"v_mfma_f32_16x16x32_bf16 v[20:23], a[236:239], a[24:27], v[20:23]\n"
"buffer_load_dwordx4 v221, %1, s64 offen offset:1024 lds\n"
"v_mfma_f32_16x16x32_bf16 v[20:23], a[232:235], a[8:11], v[20:23]\n"
"v_mfma_f32_16x16x32_bf16 v[12:15], a[232:235], a[4:7], v[12:15]\n"
"ds_read_b64_tr_b16 a[252:253], v211 offset:8200\n"
"v_mfma_f32_16x16x32_bf16 v[12:15], a[236:239], a[20:23], v[12:15]\n"
"ds_read_b64_tr_b16 a[254:255], v211 offset:12296\n"
"v_mfma_f32_16x16x32_bf16 v[4:7], a[236:239], a[16:19], v[4:7]\n"
"buffer_load_dwordx4 v222, %1, s64 offen offset:2048 lds\n"
"v_mfma_f32_16x16x32_bf16 v[4:7], a[232:235], a[0:3], v[4:7]\n"
"s_waitcnt lgkmcnt(4)\n"
"v_mfma_f32_16x16x32_bf16 v[32:35], a[240:243], a[0:3], v[32:35]\n"
"ds_read_b64_tr_b16 a[224:225], v212 offset:0\n"
"v_mfma_f32_16x16x32_bf16 v[32:35], a[244:247], a[16:19], v[32:35]\n"
"ds_read_b64_tr_b16 a[226:227], v212 offset:4096\n"
"v_mfma_f32_16x16x32_bf16 v[40:43], a[244:247], a[20:23], v[40:43]\n"
"buffer_load_dwordx4 v223, %1, s64 offen offset:3072 lds\n"
"v_mfma_f32_16x16x32_bf16 v[40:43], a[240:243], a[4:7], v[40:43]\n"
"v_mfma_f32_16x16x32_bf16 v[48:51], a[240:243], a[8:11], v[48:51]\n"
"ds_read_b64_tr_b16 a[228:229], v212 offset:8192\n"
"v_mfma_f32_16x16x32_bf16 v[48:51], a[244:247], a[24:27], v[48:51]\n"
"ds_read_b64_tr_b16 a[230:231], v212 offset:12288\n"
"v_mfma_f32_16x16x32_bf16 v[56:59], a[244:247], a[28:31], v[56:59]\n"
"v_mfma_f32_16x16x32_bf16 v[56:59], a[240:243], a[12:15], v[56:59]\n"
"s_waitcnt lgkmcnt(4)\n"
"v_mfma_f32_16x16x32_bf16 v[60:63], a[248:251], a[12:15], v[60:63]\n"
"ds_read_b64_tr_b16 a[232:233], v212 offset:8\n"
"v_mfma_f32_16x16x32_bf16 v[60:63], a[252:255], a[28:31], v[60:63]\n"
"ds_read_b64_tr_b16 a[234:235], v212 offset:4104\n"
"s_add_u32 s65, s65, 4096\n"
"v_mfma_f32_16x16x32_bf16 v[52:55], a[252:255], a[24:27], v[52:55]\n"
"buffer_load_dwordx4 a[64:67], v218, %0, s65 offen offset:0 sc1\n"
"v_mfma_f32_16x16x32_bf16 v[52:55], a[248:251], a[8:11], v[52:55]\n"
"v_mfma_f32_16x16x32_bf16 v[44:47], a[248:251], a[4:7], v[44:47]\n"
"ds_read_b64_tr_b16 a[236:237], v212 offset:8200\n"
"v_mfma_f32_16x16x32_bf16 v[44:47], a[252:255], a[20:23], v[44:47]\n"
"ds_read_b64_tr_b16 a[238:239], v212 offset:12296\n"
"v_mfma_f32_16x16x32_bf16 v[36:39], a[252:255], a[16:19], v[36:39]\n"
"buffer_load_dwordx4 a[68:71], v218, %0, s65 offen offset:1024 sc1\n"
"v_mfma_f32_16x16x32_bf16 v[36:39], a[248:251], a[0:3], v[36:39]\n"
"s_waitcnt lgkmcnt(4)\n"
"v_mfma_f32_16x16x32_bf16 v[64:67], a[224:227], a[0:3], v[64:67]\n"
"ds_read_b64_tr_b16 a[240:241], v213 offset:0\n"
"v_mfma_f32_16x16x32_bf16 v[64:67], a[228:231], a[16:19], v[64:67]\n"
"ds_read_b64_tr_b16 a[242:243], v213 offset:4096\n"
"v_mfma_f32_16x16x32_bf16 v[72:75], a[228:231], a[20:23], v[72:75]\n"
"buffer_load_dwordx4 a[72:75], v219, %0, s65 offen offset:0 sc1\n"
"v_mfma_f32_16x16x32_bf16 v[72:75], a[224:227], a[4:7], v[72:75]\n"
"v_mfma_f32_16x16x32_bf16 v[80:83], a[224:227], a[8:11], v[80:83]\n"
"ds_read_b64_tr_b16 a[244:245], v213 offset:8192\n"
"v_mfma_f32_16x16x32_bf16 v[80:83], a[228:231], a[24:27], v[80:83]\n"
"ds_read_b64_tr_b16 a[246:247], v213 offset:12288\n"
"v_mfma_f32_16x16x32_bf16 v[88:91], a[228:231], a[28:31], v[88:91]\n"
"buffer_load_dwordx4 a[76:79], v219, %0, s65 offen offset:1024 sc1\n"
"v_mfma_f32_16x16x32_bf16 v[88:91], a[224:227], a[12:15], v[88:91]\n"
"s_waitcnt lgkmcnt(4)\n"
"v_mfma_f32_16x16x32_bf16 v[92:95], a[232:235], a[12:15], v[92:95]\n"
"ds_read_b64_tr_b16 a[248:249], v213 offset:8\n"
"v_mfma_f32_16x16x32_bf16 v[92:95], a[236:239], a[28:31], v[92:95]\n"
"ds_read_b64_tr_b16 a[250:251], v213 offset:4104\n"
"v_mfma_f32_16x16x32_bf16 v[84:87], a[236:239], a[24:27], v[84:87]\n"
"v_mfma_f32_16x16x32_bf16 v[84:87], a[232:235], a[8:11], v[84:87]\n"
"v_mfma_f32_16x16x32_bf16 v[76:79], a[232:235], a[4:7], v[76:79]\n"
"ds_read_b64_tr_b16 a[252:253], v213 offset:8200\n"
"v_mfma_f32_16x16x32_bf16 v[76:79], a[236:239], a[20:23], v[76:79]\n"
"ds_read_b64_tr_b16 a[254:255], v213 offset:12296\n"
"v_mfma_f32_16x16x32_bf16 v[68:71], a[236:239], a[16:19], v[68:71]\n"
"buffer_load_dwordx4 a[80:83], v218, %0, s65 offen offset:2048 sc1\n"
"v_mfma_f32_16x16x32_bf16 v[68:71], a[232:235], a[0:3], v[68:71]\n"
"s_waitcnt lgkmcnt(4)\n"
"v_mfma_f32_16x16x32_bf16 v[96:99], a[240:243], a[0:3], v[96:99]\n"
"s_waitcnt vmcnt(17)\n"
"s_barrier\n"
"ds_read_b64_tr_b16 a[224:225], v210 offset:16384\n"
"v_mfma_f32_16x16x32_bf16 v[96:99], a[244:247], a[16:19], v[96:99]\n"
"ds_read_b64_tr_b16 a[226:227], v210 offset:20480\n"
"v_mfma_f32_16x16x32_bf16 v[104:107], a[244:247], a[20:23], v[104:107]\n"
"buffer_load_dwordx4 a[84:87], v218, %0, s65 offen offset:3072 sc1\n"
"v_mfma_f32_16x16x32_bf16 v[104:107], a[240:243], a[4:7], v[104:107]\n"
"v_mfma_f32_16x16x32_bf16 v[112:115], a[240:243], a[8:11], v[112:115]\n"
"ds_read_b64_tr_b16 a[228:229], v210 offset:24576\n"
"v_mfma_f32_16x16x32_bf16 v[112:115], a[244:247], a[24:27], v[112:115]\n"
"ds_read_b64_tr_b16 a[230:231], v210 offset:28672\n"
"v_mfma_f32_16x16x32_bf16 v[120:123], a[244:247], a[28:31], v[120:123]\n"
"buffer_load_dwordx4 a[88:91], v219, %0, s65 offen offset:2048 sc1\n"
"v_mfma_f32_16x16x32_bf16 v[120:123], a[240:243], a[12:15], v[120:123]\n"
"s_waitcnt lgkmcnt(4)\n"
"v_mfma_f32_16x16x32_bf16 v[124:127], a[248:251], a[12:15], v[124:127]\n"
"ds_read_b64_tr_b16 a[232:233], v210 offset:16392\n"
"v_mfma_f32_16x16x32_bf16 v[124:127], a[252:255], a[28:31], v[124:127]\n"
"ds_read_b64_tr_b16 a[234:235], v210 offset:20488\n"
"v_mfma_f32_16x16x32_bf16 v[116:119], a[252:255], a[24:27], v[116:119]\n"
"buffer_load_dwordx4 a[92:95], v219, %0, s65 offen offset:3072 sc1\n"
"v_mfma_f32_16x16x32_bf16 v[116:119], a[248:251], a[8:11], v[116:119]\n"
"v_mfma_f32_16x16x32_bf16 v[108:111], a[248:251], a[4:7], v[108:111]\n"
"ds_read_b64_tr_b16 a[236:237], v210 offset:24584\n"
"v_mfma_f32_16x16x32_bf16 v[108:111], a[252:255], a[20:23], v[108:111]\n"
"ds_read_b64_tr_b16 a[238:239], v210 offset:28680\n"
"v_mfma_f32_16x16x32_bf16 v[100:103], a[252:255], a[16:19], v[100:103]\n"
"v_mfma_f32_16x16x32_bf16 v[100:103], a[248:251], a[0:3], v[100:103]\n"
"s_waitcnt vmcnt(12) lgkmcnt(4)\n"
"v_mfma_f32_16x16x32_bf16 v[0:3], a[224:227], a[32:35], v[0:3]\n"
"ds_read_b64_tr_b16 a[240:241], v211 offset:16384\n"
"v_mfma_f32_16x16x32_bf16 v[0:3], a[228:231], a[48:51], v[0:3]\n"
"ds_read_b64_tr_b16 a[242:243], v211 offset:20480\n"
"v_mfma_f32_16x16x32_bf16 v[8:11], a[228:231], a[52:55], v[8:11]\n"
"v_mfma_f32_16x16x32_bf16 v[8:11], a[224:227], a[36:39], v[8:11]\n"
"v_mfma_f32_16x16x32_bf16 v[16:19], a[224:227], a[40:43], v[16:19]\n"
"ds_read_b64_tr_b16 a[244:245], v211 offset:24576\n"
"s_add_u32 s64, s64, 524288\n"
"v_mfma_f32_16x16x32_bf16 v[16:19], a[228:231], a[56:59], v[16:19]\n"
"ds_read_b64_tr_b16 a[246:247], v211 offset:28672\n"
"s_add_u32 m0, %11, 114688\n"
"v_mfma_f32_16x16x32_bf16 v[24:27], a[228:231], a[60:63], v[24:27]\n"
"buffer_load_dwordx4 v220, %1, s64 offen offset:0 lds\n"
"v_mfma_f32_16x16x32_bf16 v[24:27], a[224:227], a[44:47], v[24:27]\n"
"s_waitcnt lgkmcnt(4)\n"
"v_mfma_f32_16x16x32_bf16 v[28:31], a[232:235], a[44:47], v[28:31]\n"
"ds_read_b64_tr_b16 a[248:249], v211 offset:16392\n"
"v_mfma_f32_16x16x32_bf16 v[28:31], a[236:239], a[60:63], v[28:31]\n"
"ds_read_b64_tr_b16 a[250:251], v211 offset:20488\n"
"v_mfma_f32_16x16x32_bf16 v[20:23], a[236:239], a[56:59], v[20:23]\n"
"buffer_load_dwordx4 v221, %1, s64 offen offset:1024 lds\n"
"v_mfma_f32_16x16x32_bf16 v[20:23], a[232:235], a[40:43], v[20:23]\n"
"v_mfma_f32_16x16x32_bf16 v[12:15], a[232:235], a[36:39], v[12:15]\n"
"ds_read_b64_tr_b16 a[252:253], v211 offset:24584\n"
"v_mfma_f32_16x16x32_bf16 v[12:15], a[236:239], a[52:55], v[12:15]\n"
"ds_read_b64_tr_b16 a[254:255], v211 offset:28680\n"
"v_mfma_f32_16x16x32_bf16 v[4:7], a[236:239], a[48:51], v[4:7]\n"
"buffer_load_dwordx4 v222, %1, s64 offen offset:2048 lds\n"
"v_mfma_f32_16x16x32_bf16 v[4:7], a[232:235], a[32:35], v[4:7]\n"
"s_waitcnt lgkmcnt(4)\n"
"v_mfma_f32_16x16x32_bf16 v[32:35], a[240:243], a[32:35], v[32:35]\n"
"ds_read_b64_tr_b16 a[224:225], v212 offset:16384\n"
"v_mfma_f32_16x16x32_bf16 v[32:35], a[244:247], a[48:51], v[32:35]\n"
"ds_read_b64_tr_b16 a[226:227], v212 offset:20480\n"
"v_mfma_f32_16x16x32_bf16 v[40:43], a[244:247], a[52:55], v[40:43]\n"
"buffer_load_dwordx4 v223, %1, s64 offen offset:3072 lds\n"
"v_mfma_f32_16x16x32_bf16 v[40:43], a[240:243], a[36:39], v[40:43]\n"
"v_mfma_f32_16x16x32_bf16 v[48:51], a[240:243], a[40:43], v[48:51]\n"
"ds_read_b64_tr_b16 a[228:229], v212 offset:24576\n"
"v_mfma_f32_16x16x32_bf16 v[48:51], a[244:247], a[56:59], v[48:51]\n"
"ds_read_b64_tr_b16 a[230:231], v212 offset:28672\n"
"v_mfma_f32_16x16x32_bf16 v[56:59], a[244:247], a[60:63], v[56:59]\n"
"v_mfma_f32_16x16x32_bf16 v[56:59], a[240:243], a[44:47], v[56:59]\n"
"s_waitcnt lgkmcnt(4)\n"
"v_mfma_f32_16x16x32_bf16 v[60:63], a[248:251], a[44:47], v[60:63]\n"
"ds_read_b64_tr_b16 a[232:233], v212 offset:16392\n"
"v_mfma_f32_16x16x32_bf16 v[60:63], a[252:255], a[60:63], v[60:63]\n"
"ds_read_b64_tr_b16 a[234:235], v212 offset:20488\n"
"s_add_u32 s65, s65, 4096\n"
"v_mfma_f32_16x16x32_bf16 v[52:55], a[252:255], a[56:59], v[52:55]\n"
"buffer_load_dwordx4 a[96:99], v218, %0, s65 offen offset:0 sc1\n"
"v_mfma_f32_16x16x32_bf16 v[52:55], a[248:251], a[40:43], v[52:55]\n"
"v_mfma_f32_16x16x32_bf16 v[44:47], a[248:251], a[36:39], v[44:47]\n"
"ds_read_b64_tr_b16 a[236:237], v212 offset:24584\n"
"v_mfma_f32_16x16x32_bf16 v[44:47], a[252:255], a[52:55], v[44:47]\n"
"ds_read_b64_tr_b16 a[238:239], v212 offset:28680\n"
"v_mfma_f32_16x16x32_bf16 v[36:39], a[252:255], a[48:51], v[36:39]\n"
"buffer_load_dwordx4 a[100:103], v218, %0, s65 offen offset:1024 sc1\n"
"v_mfma_f32_16x16x32_bf16 v[36:39], a[248:251], a[32:35], v[36:39]\n"
"s_waitcnt lgkmcnt(4)\n"
"v_mfma_f32_16x16x32_bf16 v[64:67], a[224:227], a[32:35], v[64:67]\n"
"ds_read_b64_tr_b16 a[240:241], v213 offset:16384\n"
"v_mfma_f32_16x16x32_bf16 v[64:67], a[228:231], a[48:51], v[64:67]\n"
"ds_read_b64_tr_b16 a[242:243], v213 offset:20480\n"
"v_mfma_f32_16x16x32_bf16 v[72:75], a[228:231], a[52:55], v[72:75]\n"
"buffer_load_dwordx4 a[104:107], v219, %0, s65 offen offset:0 sc1\n"
"v_mfma_f32_16x16x32_bf16 v[72:75], a[224:227], a[36:39], v[72:75]\n"
"v_mfma_f32_16x16x32_bf16 v[80:83], a[224:227], a[40:43], v[80:83]\n"
"ds_read_b64_tr_b16 a[244:245], v213 offset:24576\n"
"v_mfma_f32_16x16x32_bf16 v[80:83], a[228:231], a[56:59], v[80:83]\n"
"ds_read_b64_tr_b16 a[246:247], v213 offset:28672\n"
"v_mfma_f32_16x16x32_bf16 v[88:91], a[228:231], a[60:63], v[88:91]\n"
"buffer_load_dwordx4 a[108:111], v219, %0, s65 offen offset:1024 sc1\n"
"v_mfma_f32_16x16x32_bf16 v[88:91], a[224:227], a[44:47], v[88:91]\n"
"s_waitcnt lgkmcnt(4)\n"
"v_mfma_f32_16x16x32_bf16 v[92:95], a[232:235], a[44:47], v[92:95]\n"
"ds_read_b64_tr_b16 a[248:249], v213 offset:16392\n"
"v_mfma_f32_16x16x32_bf16 v[92:95], a[236:239], a[60:63], v[92:95]\n"
"ds_read_b64_tr_b16 a[250:251], v213 offset:20488\n"
"v_mfma_f32_16x16x32_bf16 v[84:87], a[236:239], a[56:59], v[84:87]\n"
"v_mfma_f32_16x16x32_bf16 v[84:87], a[232:235], a[40:43], v[84:87]\n"
"v_mfma_f32_16x16x32_bf16 v[76:79], a[232:235], a[36:39], v[76:79]\n"
"ds_read_b64_tr_b16 a[252:253], v213 offset:24584\n"
"v_mfma_f32_16x16x32_bf16 v[76:79], a[236:239], a[52:55], v[76:79]\n"
"ds_read_b64_tr_b16 a[254:255], v213 offset:28680\n"
"v_mfma_f32_16x16x32_bf16 v[68:71], a[236:239], a[48:51], v[68:71]\n"
"buffer_load_dwordx4 a[112:115], v218, %0, s65 offen offset:2048 sc1\n"
"v_mfma_f32_16x16x32_bf16 v[68:71], a[232:235], a[32:35], v[68:71]\n"
"s_waitcnt lgkmcnt(4)\n"
"v_mfma_f32_16x16x32_bf16 v[96:99], a[240:243], a[32:35], v[96:99]\n"
"s_waitcnt vmcnt(17)\n"
"s_barrier\n"
"ds_read_b64_tr_b16 a[224:225], v210 offset:32768\n"
"v_mfma_f32_16x16x32_bf16 v[96:99], a[244:247], a[48:51], v[96:99]\n"
"ds_read_b64_tr_b16 a[226:227], v210 offset:36864\n"
"v_mfma_f32_16x16x32_bf16 v[104:107], a[244:247], a[52:55], v[104:107]\n"
"buffer_load_dwordx4 a[116:119], v218, %0, s65 offen offset:3072 sc1\n"
"v_mfma_f32_16x16x32_bf16 v[104:107], a[240:243], a[36:39], v[104:107]\n"
"v_mfma_f32_16x16x32_bf16 v[112:115], a[240:243], a[40:43], v[112:115]\n"
"ds_read_b64_tr_b16 a[228:229], v210 offset:40960\n"
"v_mfma_f32_16x16x32_bf16 v[112:115], a[244:247], a[56:59], v[112:115]\n"
"ds_read_b64_tr_b16 a[230:231], v210 offset:45056\n"
"v_mfma_f32_16x16x32_bf16 v[120:123], a[244:247], a[60:63], v[120:123]\n"
"buffer_load_dwordx4 a[120:123], v219, %0, s65 offen offset:2048 sc1\n"
"v_mfma_f32_16x16x32_bf16 v[120:123], a[240:243], a[44:47], v[120:123]\n"
"s_waitcnt lgkmcnt(4)\n"
"v_mfma_f32_16x16x32_bf16 v[124:127], a[248:251], a[44:47], v[124:127]\n"
"ds_read_b64_tr_b16 a[232:233], v210 offset:32776\n"
"v_mfma_f32_16x16x32_bf16 v[124:127], a[252:255], a[60:63], v[124:127]\n"
"ds_read_b64_tr_b16 a[234:235], v210 offset:36872\n"
"v_mfma_f32_16x16x32_bf16 v[116:119], a[252:255], a[56:59], v[116:119]\n"
"buffer_load_dwordx4 a[124:127], v219, %0, s65 offen offset:3072 sc1\n"
"v_mfma_f32_16x16x32_bf16 v[116:119], a[248:251], a[40:43], v[116:119]\n"
"v_mfma_f32_16x16x32_bf16 v[108:111], a[248:251], a[36:39], v[108:111]\n"
"ds_read_b64_tr_b16 a[236:237], v210 offset:40968\n"
"v_mfma_f32_16x16x32_bf16 v[108:111], a[252:255], a[52:55], v[108:111]\n"
"ds_read_b64_tr_b16 a[238:239], v210 offset:45064\n"
"v_mfma_f32_16x16x32_bf16 v[100:103], a[252:255], a[48:51], v[100:103]\n"
"v_mfma_f32_16x16x32_bf16 v[100:103], a[248:251], a[32:35], v[100:103]\n"
"s_waitcnt vmcnt(12) lgkmcnt(4)\n"
"v_mfma_f32_16x16x32_bf16 v[0:3], a[224:227], a[64:67], v[0:3]\n"
"ds_read_b64_tr_b16 a[240:241], v211 offset:32768\n"
"v_mfma_f32_16x16x32_bf16 v[0:3], a[228:231], a[80:83], v[0:3]\n"
"ds_read_b64_tr_b16 a[242:243], v211 offset:36864\n"
"v_mfma_f32_16x16x32_bf16 v[8:11], a[228:231], a[84:87], v[8:11]\n"
"v_mfma_f32_16x16x32_bf16 v[8:11], a[224:227], a[68:71], v[8:11]\n"
"v_mfma_f32_16x16x32_bf16 v[16:19], a[224:227], a[72:75], v[16:19]\n"
"ds_read_b64_tr_b16 a[244:245], v211 offset:40960\n"
"s_add_u32 s64, s64, 524288\n"
"v_mfma_f32_16x16x32_bf16 v[16:19], a[228:231], a[88:91], v[16:19]\n"
"ds_read_b64_tr_b16 a[246:247], v211 offset:45056\n"
"s_add_u32 m0, %11, 65536\n"
"v_mfma_f32_16x16x32_bf16 v[24:27], a[228:231], a[92:95], v[24:27]\n"
"buffer_load_dwordx4 v220, %1, s64 offen offset:0 lds\n"
"v_mfma_f32_16x16x32_bf16 v[24:27], a[224:227], a[76:79], v[24:27]\n"
"s_waitcnt lgkmcnt(4)\n"
"v_mfma_f32_16x16x32_bf16 v[28:31], a[232:235], a[76:79], v[28:31]\n"
"ds_read_b64_tr_b16 a[248:249], v211 offset:32776\n"
"v_mfma_f32_16x16x32_bf16 v[28:31], a[236:239], a[92:95], v[28:31]\n"
"ds_read_b64_tr_b16 a[250:251], v211 offset:36872\n"
"v_mfma_f32_16x16x32_bf16 v[20:23], a[236:239], a[88:91], v[20:23]\n"
"buffer_load_dwordx4 v221, %1, s64 offen offset:1024 lds\n"
"v_mfma_f32_16x16x32_bf16 v[20:23], a[232:235], a[72:75], v[20:23]\n"
"v_mfma_f32_16x16x32_bf16 v[12:15], a[232:235], a[68:71], v[12:15]\n"
"ds_read_b64_tr_b16 a[252:253], v211 offset:40968\n"
"v_mfma_f32_16x16x32_bf16 v[12:15], a[236:239], a[84:87], v[12:15]\n"
"ds_read_b64_tr_b16 a[254:255], v211 offset:45064\n"
"v_mfma_f32_16x16x32_bf16 v[4:7], a[236:239], a[80:83], v[4:7]\n"
"buffer_load_dwordx4 v222, %1, s64 offen offset:2048 lds\n"
"v_mfma_f32_16x16x32_bf16 v[4:7], a[232:235], a[64:67], v[4:7]\n"
"s_waitcnt lgkmcnt(4)\n"
"v_mfma_f32_16x16x32_bf16 v[32:35], a[240:243], a[64:67], v[32:35]\n"
"ds_read_b64_tr_b16 a[224:225], v212 offset:32768\n"
"v_mfma_f32_16x16x32_bf16 v[32:35], a[244:247], a[80:83], v[32:35]\n"
"ds_read_b64_tr_b16 a[226:227], v212 offset:36864\n"
"v_mfma_f32_16x16x32_bf16 v[40:43], a[244:247], a[84:87], v[40:43]\n"
"buffer_load_dwordx4 v223, %1, s64 offen offset:3072 lds\n"
"v_mfma_f32_16x16x32_bf16 v[40:43], a[240:243], a[68:71], v[40:43]\n"
"v_mfma_f32_16x16x32_bf16 v[48:51], a[240:243], a[72:75], v[48:51]\n"
"ds_read_b64_tr_b16 a[228:229], v212 offset:40960\n"
"v_mfma_f32_16x16x32_bf16 v[48:51], a[244:247], a[88:91], v[48:51]\n"
"ds_read_b64_tr_b16 a[230:231], v212 offset:45056\n"
"v_mfma_f32_16x16x32_bf16 v[56:59], a[244:247], a[92:95], v[56:59]\n"
"v_mfma_f32_16x16x32_bf16 v[56:59], a[240:243], a[76:79], v[56:59]\n"
"s_waitcnt lgkmcnt(4)\n"
"v_mfma_f32_16x16x32_bf16 v[60:63], a[248:251], a[76:79], v[60:63]\n"
"ds_read_b64_tr_b16 a[232:233], v212 offset:32776\n"
"v_mfma_f32_16x16x32_bf16 v[60:63], a[252:255], a[92:95], v[60:63]\n"
"ds_read_b64_tr_b16 a[234:235], v212 offset:36872\n"
"s_add_u32 s65, s65, 4096\n"
"v_mfma_f32_16x16x32_bf16 v[52:55], a[252:255], a[88:91], v[52:55]\n"
"buffer_load_dwordx4 a[0:3], v218, %0, s65 offen offset:0 sc1\n"
"v_mfma_f32_16x16x32_bf16 v[52:55], a[248:251], a[72:75], v[52:55]\n"
"v_mfma_f32_16x16x32_bf16 v[44:47], a[248:251], a[68:71], v[44:47]\n"
"ds_read_b64_tr_b16 a[236:237], v212 offset:40968\n"
"v_mfma_f32_16x16x32_bf16 v[44:47], a[252:255], a[84:87], v[44:47]\n"
"ds_read_b64_tr_b16 a[238:239], v212 offset:45064\n"
"v_mfma_f32_16x16x32_bf16 v[36:39], a[252:255], a[80:83], v[36:39]\n"
"buffer_load_dwordx4 a[4:7], v218, %0, s65 offen offset:1024 sc1\n"
"v_mfma_f32_16x16x32_bf16 v[36:39], a[248:251], a[64:67], v[36:39]\n"
"s_waitcnt lgkmcnt(4)\n"
"v_mfma_f32_16x16x32_bf16 v[64:67], a[224:227], a[64:67], v[64:67]\n"
"ds_read_b64_tr_b16 a[240:241], v213 offset:32768\n"
"v_mfma_f32_16x16x32_bf16 v[64:67], a[228:231], a[80:83], v[64:67]\n"
"ds_read_b64_tr_b16 a[242:243], v213 offset:36864\n"
"v_mfma_f32_16x16x32_bf16 v[72:75], a[228:231], a[84:87], v[72:75]\n"
"buffer_load_dwordx4 a[8:11], v219, %0, s65 offen offset:0 sc1\n"
"v_mfma_f32_16x16x32_bf16 v[72:75], a[224:227], a[68:71], v[72:75]\n"
"v_mfma_f32_16x16x32_bf16 v[80:83], a[224:227], a[72:75], v[80:83]\n"
"ds_read_b64_tr_b16 a[244:245], v213 offset:40960\n"
"v_mfma_f32_16x16x32_bf16 v[80:83], a[228:231], a[88:91], v[80:83]\n"
"ds_read_b64_tr_b16 a[246:247], v213 offset:45056\n"
"v_mfma_f32_16x16x32_bf16 v[88:91], a[228:231], a[92:95], v[88:91]\n"
"buffer_load_dwordx4 a[12:15], v219, %0, s65 offen offset:1024 sc1\n"
"v_mfma_f32_16x16x32_bf16 v[88:91], a[224:227], a[76:79], v[88:91]\n"
"s_waitcnt lgkmcnt(4)\n"
"v_mfma_f32_16x16x32_bf16 v[92:95], a[232:235], a[76:79], v[92:95]\n"
"ds_read_b64_tr_b16 a[248:249], v213 offset:32776\n"
"v_mfma_f32_16x16x32_bf16 v[92:95], a[236:239], a[92:95], v[92:95]\n"
"ds_read_b64_tr_b16 a[250:251], v213 offset:36872\n"
"v_mfma_f32_16x16x32_bf16 v[84:87], a[236:239], a[88:91], v[84:87]\n"
"v_mfma_f32_16x16x32_bf16 v[84:87], a[232:235], a[72:75], v[84:87]\n"
"v_mfma_f32_16x16x32_bf16 v[76:79], a[232:235], a[68:71], v[76:79]\n"
"ds_read_b64_tr_b16 a[252:253], v213 offset:40968\n"
"v_mfma_f32_16x16x32_bf16 v[76:79], a[236:239], a[84:87], v[76:79]\n"
"ds_read_b64_tr_b16 a[254:255], v213 offset:45064\n"
"v_mfma_f32_16x16x32_bf16 v[68:71], a[236:239], a[80:83], v[68:71]\n"
"buffer_load_dwordx4 a[16:19], v218, %0, s65 offen offset:2048 sc1\n"
"v_mfma_f32_16x16x32_bf16 v[68:71], a[232:235], a[64:67], v[68:71]\n"
"s_waitcnt lgkmcnt(4)\n"
"v_mfma_f32_16x16x32_bf16 v[96:99], a[240:243], a[64:67], v[96:99]\n"
"s_waitcnt vmcnt(17)\n"
"s_barrier\n"
"ds_read_b64_tr_b16 a[224:225], v210 offset:49152\n"
"v_mfma_f32_16x16x32_bf16 v[96:99], a[244:247], a[80:83], v[96:99]\n"
"ds_read_b64_tr_b16 a[226:227], v210 offset:53248\n"
"v_mfma_f32_16x16x32_bf16 v[104:107], a[244:247], a[84:87], v[104:107]\n"
"buffer_load_dwordx4 a[20:23], v218, %0, s65 offen offset:3072 sc1\n"
"v_mfma_f32_16x16x32_bf16 v[104:107], a[240:243], a[68:71], v[104:107]\n"
"v_mfma_f32_16x16x32_bf16 v[112:115], a[240:243], a[72:75], v[112:115]\n"
"ds_read_b64_tr_b16 a[228:229], v210 offset:57344\n"
"v_mfma_f32_16x16x32_bf16 v[112:115], a[244:247], a[88:91], v[112:115]\n"
"ds_read_b64_tr_b16 a[230:231], v210 offset:61440\n"
"v_mfma_f32_16x16x32_bf16 v[120:123], a[244:247], a[92:95], v[120:123]\n"
"buffer_load_dwordx4 a[24:27], v219, %0, s65 offen offset:2048 sc1\n"
"v_mfma_f32_16x16x32_bf16 v[120:123], a[240:243], a[76:79], v[120:123]\n"
"s_waitcnt lgkmcnt(4)\n"
"v_mfma_f32_16x16x32_bf16 v[124:127], a[248:251], a[76:79], v[124:127]\n"
"ds_read_b64_tr_b16 a[232:233], v210 offset:49160\n"
"v_mfma_f32_16x16x32_bf16 v[124:127], a[252:255], a[92:95], v[124:127]\n"
"ds_read_b64_tr_b16 a[234:235], v210 offset:53256\n"
"v_mfma_f32_16x16x32_bf16 v[116:119], a[252:255], a[88:91], v[116:119]\n"
"buffer_load_dwordx4 a[28:31], v219, %0, s65 offen offset:3072 sc1\n"
"v_mfma_f32_16x16x32_bf16 v[116:119], a[248:251], a[72:75], v[116:119]\n"
"v_mfma_f32_16x16x32_bf16 v[108:111], a[248:251], a[68:71], v[108:111]\n"
"ds_read_b64_tr_b16 a[236:237], v210 offset:57352\n"
"v_mfma_f32_16x16x32_bf16 v[108:111], a[252:255], a[84:87], v[108:111]\n"
"ds_read_b64_tr_b16 a[238:239], v210 offset:61448\n"
"v_mfma_f32_16x16x32_bf16 v[100:103], a[252:255], a[80:83], v[100:103]\n"
"v_mfma_f32_16x16x32_bf16 v[100:103], a[248:251], a[64:67], v[100:103]\n"
"s_waitcnt vmcnt(12) lgkmcnt(4)\n"
"v_mfma_f32_16x16x32_bf16 v[0:3], a[224:227], a[96:99], v[0:3]\n"
"ds_read_b64_tr_b16 a[240:241], v211 offset:49152\n"
"v_mfma_f32_16x16x32_bf16 v[0:3], a[228:231], a[112:115], v[0:3]\n"
"ds_read_b64_tr_b16 a[242:243], v211 offset:53248\n"
"v_mfma_f32_16x16x32_bf16 v[8:11], a[228:231], a[116:119], v[8:11]\n"
"v_mfma_f32_16x16x32_bf16 v[8:11], a[224:227], a[100:103], v[8:11]\n"
"v_mfma_f32_16x16x32_bf16 v[16:19], a[224:227], a[104:107], v[16:19]\n"
"ds_read_b64_tr_b16 a[244:245], v211 offset:57344\n"
"s_add_u32 s64, s64, 524288\n"
"v_mfma_f32_16x16x32_bf16 v[16:19], a[228:231], a[120:123], v[16:19]\n"
"ds_read_b64_tr_b16 a[246:247], v211 offset:61440\n"
"s_add_u32 m0, %11, 81920\n"
"v_mfma_f32_16x16x32_bf16 v[24:27], a[228:231], a[124:127], v[24:27]\n"
"buffer_load_dwordx4 v220, %1, s64 offen offset:0 lds\n"
"v_mfma_f32_16x16x32_bf16 v[24:27], a[224:227], a[108:111], v[24:27]\n"
"s_waitcnt lgkmcnt(4)\n"
"v_mfma_f32_16x16x32_bf16 v[28:31], a[232:235], a[108:111], v[28:31]\n"
"ds_read_b64_tr_b16 a[248:249], v211 offset:49160\n"
"v_mfma_f32_16x16x32_bf16 v[28:31], a[236:239], a[124:127], v[28:31]\n"
"ds_read_b64_tr_b16 a[250:251], v211 offset:53256\n"
"v_mfma_f32_16x16x32_bf16 v[20:23], a[236:239], a[120:123], v[20:23]\n"
"buffer_load_dwordx4 v221, %1, s64 offen offset:1024 lds\n"
"v_mfma_f32_16x16x32_bf16 v[20:23], a[232:235], a[104:107], v[20:23]\n"
"v_mfma_f32_16x16x32_bf16 v[12:15], a[232:235], a[100:103], v[12:15]\n"
"ds_read_b64_tr_b16 a[252:253], v211 offset:57352\n"
"v_mfma_f32_16x16x32_bf16 v[12:15], a[236:239], a[116:119], v[12:15]\n"
"ds_read_b64_tr_b16 a[254:255], v211 offset:61448\n"
"v_mfma_f32_16x16x32_bf16 v[4:7], a[236:239], a[112:115], v[4:7]\n"
"buffer_load_dwordx4 v222, %1, s64 offen offset:2048 lds\n"
"v_mfma_f32_16x16x32_bf16 v[4:7], a[232:235], a[96:99], v[4:7]\n"
"s_waitcnt lgkmcnt(4)\n"
"v_mfma_f32_16x16x32_bf16 v[32:35], a[240:243], a[96:99], v[32:35]\n"
"ds_read_b64_tr_b16 a[224:225], v212 offset:49152\n"
"v_mfma_f32_16x16x32_bf16 v[32:35], a[244:247], a[112:115], v[32:35]\n"
"ds_read_b64_tr_b16 a[226:227], v212 offset:53248\n"
"v_mfma_f32_16x16x32_bf16 v[40:43], a[244:247], a[116:119], v[40:43]\n"
"buffer_load_dwordx4 v223, %1, s64 offen offset:3072 lds\n"
"v_mfma_f32_16x16x32_bf16 v[40:43], a[240:243], a[100:103], v[40:43]\n"
"v_mfma_f32_16x16x32_bf16 v[48:51], a[240:243], a[104:107], v[48:51]\n"
"ds_read_b64_tr_b16 a[228:229], v212 offset:57344\n"
"v_mfma_f32_16x16x32_bf16 v[48:51], a[244:247], a[120:123], v[48:51]\n"
"ds_read_b64_tr_b16 a[230:231], v212 offset:61440\n"
"v_mfma_f32_16x16x32_bf16 v[56:59], a[244:247], a[124:127], v[56:59]\n"
"v_mfma_f32_16x16x32_bf16 v[56:59], a[240:243], a[108:111], v[56:59]\n"
"s_waitcnt lgkmcnt(4)\n"
"v_mfma_f32_16x16x32_bf16 v[60:63], a[248:251], a[108:111], v[60:63]\n"
"ds_read_b64_tr_b16 a[232:233], v212 offset:49160\n"
"v_mfma_f32_16x16x32_bf16 v[60:63], a[252:255], a[124:127], v[60:63]\n"
"ds_read_b64_tr_b16 a[234:235], v212 offset:53256\n"
"s_add_u32 s65, s65, 4096\n"
"v_mfma_f32_16x16x32_bf16 v[52:55], a[252:255], a[120:123], v[52:55]\n"
"buffer_load_dwordx4 a[32:35], v218, %0, s65 offen offset:0 sc1\n"
"v_mfma_f32_16x16x32_bf16 v[52:55], a[248:251], a[104:107], v[52:55]\n"
"v_mfma_f32_16x16x32_bf16 v[44:47], a[248:251], a[100:103], v[44:47]\n"
"ds_read_b64_tr_b16 a[236:237], v212 offset:57352\n"
"v_mfma_f32_16x16x32_bf16 v[44:47], a[252:255], a[116:119], v[44:47]\n"
"ds_read_b64_tr_b16 a[238:239], v212 offset:61448\n"
"v_mfma_f32_16x16x32_bf16 v[36:39], a[252:255], a[112:115], v[36:39]\n"
"buffer_load_dwordx4 a[36:39], v218, %0, s65 offen offset:1024 sc1\n"
"v_mfma_f32_16x16x32_bf16 v[36:39], a[248:251], a[96:99], v[36:39]\n"
"s_waitcnt lgkmcnt(4)\n"
"v_mfma_f32_16x16x32_bf16 v[64:67], a[224:227], a[96:99], v[64:67]\n"
"ds_read_b64_tr_b16 a[240:241], v213 offset:49152\n"
"v_mfma_f32_16x16x32_bf16 v[64:67], a[228:231], a[112:115], v[64:67]\n"
"ds_read_b64_tr_b16 a[242:243], v213 offset:53248\n"
"v_mfma_f32_16x16x32_bf16 v[72:75], a[228:231], a[116:119], v[72:75]\n"
"buffer_load_dwordx4 a[40:43], v219, %0, s65 offen offset:0 sc1\n"
"v_mfma_f32_16x16x32_bf16 v[72:75], a[224:227], a[100:103], v[72:75]\n"
"v_mfma_f32_16x16x32_bf16 v[80:83], a[224:227], a[104:107], v[80:83]\n"
"ds_read_b64_tr_b16 a[244:245], v213 offset:57344\n"
"v_mfma_f32_16x16x32_bf16 v[80:83], a[228:231], a[120:123], v[80:83]\n"
"ds_read_b64_tr_b16 a[246:247], v213 offset:61440\n"
"v_mfma_f32_16x16x32_bf16 v[88:91], a[228:231], a[124:127], v[88:91]\n"
"buffer_load_dwordx4 a[44:47], v219, %0, s65 offen offset:1024 sc1\n"
"v_mfma_f32_16x16x32_bf16 v[88:91], a[224:227], a[108:111], v[88:91]\n"
"s_waitcnt lgkmcnt(4)\n"
"v_mfma_f32_16x16x32_bf16 v[92:95], a[232:235], a[108:111], v[92:95]\n"
"ds_read_b64_tr_b16 a[248:249], v213 offset:49160\n"
"v_mfma_f32_16x16x32_bf16 v[92:95], a[236:239], a[124:127], v[92:95]\n"
"ds_read_b64_tr_b16 a[250:251], v213 offset:53256\n"
"v_mfma_f32_16x16x32_bf16 v[84:87], a[236:239], a[120:123], v[84:87]\n"
"v_mfma_f32_16x16x32_bf16 v[84:87], a[232:235], a[104:107], v[84:87]\n"
"v_mfma_f32_16x16x32_bf16 v[76:79], a[232:235], a[100:103], v[76:79]\n"
"ds_read_b64_tr_b16 a[252:253], v213 offset:57352\n"
"v_mfma_f32_16x16x32_bf16 v[76:79], a[236:239], a[116:119], v[76:79]\n"
"ds_read_b64_tr_b16 a[254:255], v213 offset:61448\n"
"v_mfma_f32_16x16x32_bf16 v[68:71], a[236:239], a[112:115], v[68:71]\n"
"buffer_load_dwordx4 a[48:51], v218, %0, s65 offen offset:2048 sc1\n"
"v_mfma_f32_16x16x32_bf16 v[68:71], a[232:235], a[96:99], v[68:71]\n"
"s_waitcnt lgkmcnt(4)\n"
"v_mfma_f32_16x16x32_bf16 v[96:99], a[240:243], a[96:99], v[96:99]\n"
"s_waitcnt vmcnt(17)\n"
"s_barrier\n"
"ds_read_b64_tr_b16 a[224:225], v210 offset:0\n"
"v_mfma_f32_16x16x32_bf16 v[96:99], a[244:247], a[112:115], v[96:99]\n"
"ds_read_b64_tr_b16 a[226:227], v210 offset:4096\n"
"v_mfma_f32_16x16x32_bf16 v[104:107], a[244:247], a[116:119], v[104:107]\n"
"buffer_load_dwordx4 a[52:55], v218, %0, s65 offen offset:3072 sc1\n"
"v_mfma_f32_16x16x32_bf16 v[104:107], a[240:243], a[100:103], v[104:107]\n"
"v_mfma_f32_16x16x32_bf16 v[112:115], a[240:243], a[104:107], v[112:115]\n"
"ds_read_b64_tr_b16 a[228:229], v210 offset:8192\n"
"v_mfma_f32_16x16x32_bf16 v[112:115], a[244:247], a[120:123], v[112:115]\n"
"ds_read_b64_tr_b16 a[230:231], v210 offset:12288\n"
"v_mfma_f32_16x16x32_bf16 v[120:123], a[244:247], a[124:127], v[120:123]\n"
"buffer_load_dwordx4 a[56:59], v219, %0, s65 offen offset:2048 sc1\n"
"v_mfma_f32_16x16x32_bf16 v[120:123], a[240:243], a[108:111], v[120:123]\n"
"s_waitcnt lgkmcnt(4)\n"
"v_mfma_f32_16x16x32_bf16 v[124:127], a[248:251], a[108:111], v[124:127]\n"
"ds_read_b64_tr_b16 a[232:233], v210 offset:8\n"
"v_mfma_f32_16x16x32_bf16 v[124:127], a[252:255], a[124:127], v[124:127]\n"
"ds_read_b64_tr_b16 a[234:235], v210 offset:4104\n"
"v_mfma_f32_16x16x32_bf16 v[116:119], a[252:255], a[120:123], v[116:119]\n"
"buffer_load_dwordx4 a[60:63], v219, %0, s65 offen offset:3072 sc1\n"
"v_mfma_f32_16x16x32_bf16 v[116:119], a[248:251], a[104:107], v[116:119]\n"
"s_sub_u32 s66, s66, 1\n"
"v_mfma_f32_16x16x32_bf16 v[108:111], a[248:251], a[100:103], v[108:111]\n"
"ds_read_b64_tr_b16 a[236:237], v210 offset:8200\n"
"v_mfma_f32_16x16x32_bf16 v[108:111], a[252:255], a[116:119], v[108:111]\n"
"ds_read_b64_tr_b16 a[238:239], v210 offset:12296\n"
"v_mfma_f32_16x16x32_bf16 v[100:103], a[252:255], a[112:115], v[100:103]\n"
"v_mfma_f32_16x16x32_bf16 v[100:103], a[248:251], a[96:99], v[100:103]\n"
"s_cmp_lg_u32 s66, 0\n"
"s_cbranch_scc1 LOOP_Q%=\n"
"s_waitcnt vmcnt(12) lgkmcnt(4)\n"
"v_mfma_f32_16x16x32_bf16 v[0:3], a[224:227], a[0:3], v[0:3]\n"
"ds_read_b64_tr_b16 a[240:241], v211 offset:0\n"
"v_mfma_f32_16x16x32_bf16 v[0:3], a[228:231], a[16:19], v[0:3]\n"
"ds_read_b64_tr_b16 a[242:243], v211 offset:4096\n"
"v_mfma_f32_16x16x32_bf16 v[8:11], a[228:231], a[20:23], v[8:11]\n"
"v_mfma_f32_16x16x32_bf16 v[8:11], a[224:227], a[4:7], v[8:11]\n"
"v_mfma_f32_16x16x32_bf16 v[16:19], a[224:227], a[8:11], v[16:19]\n"
"ds_read_b64_tr_b16 a[244:245], v211 offset:8192\n"
"s_add_u32 s64, s64, 524288\n"
"v_mfma_f32_16x16x32_bf16 v[16:19], a[228:231], a[24:27], v[16:19]\n"
"ds_read_b64_tr_b16 a[246:247], v211 offset:12288\n"
"s_add_u32 m0, %11, 98304\n"
"v_mfma_f32_16x16x32_bf16 v[24:27], a[228:231], a[28:31], v[24:27]\n"
"buffer_load_dwordx4 v220, %1, s64 offen offset:0 lds\n"
"v_mfma_f32_16x16x32_bf16 v[24:27], a[224:227], a[12:15], v[24:27]\n"
"s_waitcnt lgkmcnt(4)\n"
"v_mfma_f32_16x16x32_bf16 v[28:31], a[232:235], a[12:15], v[28:31]\n"
"ds_read_b64_tr_b16 a[248:249], v211 offset:8\n"
"v_mfma_f32_16x16x32_bf16 v[28:31], a[236:239], a[28:31], v[28:31]\n"
"ds_read_b64_tr_b16 a[250:251], v211 offset:4104\n"
"v_mfma_f32_16x16x32_bf16 v[20:23], a[236:239], a[24:27], v[20:23]\n"
"buffer_load_dwordx4 v221, %1, s64 offen offset:1024 lds\n"
"v_mfma_f32_16x16x32_bf16 v[20:23], a[232:235], a[8:11], v[20:23]\n"
"v_mfma_f32_16x16x32_bf16 v[12:15], a[232:235], a[4:7], v[12:15]\n"
"ds_read_b64_tr_b16 a[252:253], v211 offset:8200\n"
"v_mfma_f32_16x16x32_bf16 v[12:15], a[236:239], a[20:23], v[12:15]\n"
"ds_read_b64_tr_b16 a[254:255], v211 offset:12296\n"
"v_mfma_f32_16x16x32_bf16 v[4:7], a[236:239], a[16:19], v[4:7]\n"
"buffer_load_dwordx4 v222, %1, s64 offen offset:2048 lds\n"
"v_mfma_f32_16x16x32_bf16 v[4:7], a[232:235], a[0:3], v[4:7]\n"
"s_waitcnt lgkmcnt(4)\n"
"v_mfma_f32_16x16x32_bf16 v[32:35], a[240:243], a[0:3], v[32:35]\n"
"ds_read_b64_tr_b16 a[224:225], v212 offset:0\n"
"v_mfma_f32_16x16x32_bf16 v[32:35], a[244:247], a[16:19], v[32:35]\n"
"ds_read_b64_tr_b16 a[226:227], v212 offset:4096\n"
"v_mfma_f32_16x16x32_bf16 v[40:43], a[244:247], a[20:23], v[40:43]\n"
"buffer_load_dwordx4 v223, %1, s64 offen offset:3072 lds\n"
"v_mfma_f32_16x16x32_bf16 v[40:43], a[240:243], a[4:7], v[40:43]\n"
"v_mfma_f32_16x16x32_bf16 v[48:51], a[240:243], a[8:11], v[48:51]\n"
"ds_read_b64_tr_b16 a[228:229], v212 offset:8192\n"
"v_mfma_f32_16x16x32_bf16 v[48:51], a[244:247], a[24:27], v[48:51]\n"
"ds_read_b64_tr_b16 a[230:231], v212 offset:12288\n"
"v_mfma_f32_16x16x32_bf16 v[56:59], a[244:247], a[28:31], v[56:59]\n"
"v_mfma_f32_16x16x32_bf16 v[56:59], a[240:243], a[12:15], v[56:59]\n"
"s_waitcnt lgkmcnt(4)\n"
"v_mfma_f32_16x16x32_bf16 v[60:63], a[248:251], a[12:15], v[60:63]\n"
"ds_read_b64_tr_b16 a[232:233], v212 offset:8\n"
"v_mfma_f32_16x16x32_bf16 v[60:63], a[252:255], a[28:31], v[60:63]\n"
"ds_read_b64_tr_b16 a[234:235], v212 offset:4104\n"
"s_add_u32 s65, s65, 4096\n"
"v_mfma_f32_16x16x32_bf16 v[52:55], a[252:255], a[24:27], v[52:55]\n"
"buffer_load_dwordx4 a[64:67], v218, %0, s65 offen offset:0 sc1\n"
"v_mfma_f32_16x16x32_bf16 v[52:55], a[248:251], a[8:11], v[52:55]\n"
"v_mfma_f32_16x16x32_bf16 v[44:47], a[248:251], a[4:7], v[44:47]\n"
"ds_read_b64_tr_b16 a[236:237], v212 offset:8200\n"
"v_mfma_f32_16x16x32_bf16 v[44:47], a[252:255], a[20:23], v[44:47]\n"
"ds_read_b64_tr_b16 a[238:239], v212 offset:12296\n"
"v_mfma_f32_16x16x32_bf16 v[36:39], a[252:255], a[16:19], v[36:39]\n"
"buffer_load_dwordx4 a[68:71], v218, %0, s65 offen offset:1024 sc1\n"
"v_mfma_f32_16x16x32_bf16 v[36:39], a[248:251], a[0:3], v[36:39]\n"
"s_waitcnt lgkmcnt(4)\n"
"v_mfma_f32_16x16x32_bf16 v[64:67], a[224:227], a[0:3], v[64:67]\n"
"ds_read_b64_tr_b16 a[240:241], v213 offset:0\n"
"v_mfma_f32_16x16x32_bf16 v[64:67], a[228:231], a[16:19], v[64:67]\n"
"ds_read_b64_tr_b16 a[242:243], v213 offset:4096\n"
"v_mfma_f32_16x16x32_bf16 v[72:75], a[228:231], a[20:23], v[72:75]\n"
"buffer_load_dwordx4 a[72:75], v219, %0, s65 offen offset:0 sc1\n"
"v_mfma_f32_16x16x32_bf16 v[72:75], a[224:227], a[4:7], v[72:75]\n"
"v_mfma_f32_16x16x32_bf16 v[80:83], a[224:227], a[8:11], v[80:83]\n"
"ds_read_b64_tr_b16 a[244:245], v213 offset:8192\n"
"v_mfma_f32_16x16x32_bf16 v[80:83], a[228:231], a[24:27], v[80:83]\n"
"ds_read_b64_tr_b16 a[246:247], v213 offset:12288\n"
"v_mfma_f32_16x16x32_bf16 v[88:91], a[228:231], a[28:31], v[88:91]\n"
"buffer_load_dwordx4 a[76:79], v219, %0, s65 offen offset:1024 sc1\n"
"v_mfma_f32_16x16x32_bf16 v[88:91], a[224:227], a[12:15], v[88:91]\n"
"s_waitcnt lgkmcnt(4)\n"
"v_mfma_f32_16x16x32_bf16 v[92:95], a[232:235], a[12:15], v[92:95]\n"
"ds_read_b64_tr_b16 a[248:249], v213 offset:8\n"
"v_mfma_f32_16x16x32_bf16 v[92:95], a[236:239], a[28:31], v[92:95]\n"
"ds_read_b64_tr_b16 a[250:251], v213 offset:4104\n"
"v_mfma_f32_16x16x32_bf16 v[84:87], a[236:239], a[24:27], v[84:87]\n"
"v_mfma_f32_16x16x32_bf16 v[84:87], a[232:235], a[8:11], v[84:87]\n"
"v_mfma_f32_16x16x32_bf16 v[76:79], a[232:235], a[4:7], v[76:79]\n"
"ds_read_b64_tr_b16 a[252:253], v213 offset:8200\n"
"v_mfma_f32_16x16x32_bf16 v[76:79], a[236:239], a[20:23], v[76:79]\n"
"ds_read_b64_tr_b16 a[254:255], v213 offset:12296\n"
"v_mfma_f32_16x16x32_bf16 v[68:71], a[236:239], a[16:19], v[68:71]\n"
"buffer_load_dwordx4 a[80:83], v218, %0, s65 offen offset:2048 sc1\n"
"v_mfma_f32_16x16x32_bf16 v[68:71], a[232:235], a[0:3], v[68:71]\n"
"s_waitcnt lgkmcnt(4)\n"
"v_mfma_f32_16x16x32_bf16 v[96:99], a[240:243], a[0:3], v[96:99]\n"
"s_waitcnt vmcnt(17)\n"
"s_barrier\n"
"ds_read_b64_tr_b16 a[224:225], v210 offset:16384\n"
"v_mfma_f32_16x16x32_bf16 v[96:99], a[244:247], a[16:19], v[96:99]\n"
"ds_read_b64_tr_b16 a[226:227], v210 offset:20480\n"
"v_mfma_f32_16x16x32_bf16 v[104:107], a[244:247], a[20:23], v[104:107]\n"
"buffer_load_dwordx4 a[84:87], v218, %0, s65 offen offset:3072 sc1\n"
"v_mfma_f32_16x16x32_bf16 v[104:107], a[240:243], a[4:7], v[104:107]\n"
"v_mfma_f32_16x16x32_bf16 v[112:115], a[240:243], a[8:11], v[112:115]\n"
"ds_read_b64_tr_b16 a[228:229], v210 offset:24576\n"
"v_mfma_f32_16x16x32_bf16 v[112:115], a[244:247], a[24:27], v[112:115]\n"
"ds_read_b64_tr_b16 a[230:231], v210 offset:28672\n"
"v_mfma_f32_16x16x32_bf16 v[120:123], a[244:247], a[28:31], v[120:123]\n"
"buffer_load_dwordx4 a[88:91], v219, %0, s65 offen offset:2048 sc1\n"
"v_mfma_f32_16x16x32_bf16 v[120:123], a[240:243], a[12:15], v[120:123]\n"
"s_waitcnt lgkmcnt(4)\n"
"v_mfma_f32_16x16x32_bf16 v[124:127], a[248:251], a[12:15], v[124:127]\n"
"ds_read_b64_tr_b16 a[232:233], v210 offset:16392\n"
"v_mfma_f32_16x16x32_bf16 v[124:127], a[252:255], a[28:31], v[124:127]\n"
"ds_read_b64_tr_b16 a[234:235], v210 offset:20488\n"
"v_mfma_f32_16x16x32_bf16 v[116:119], a[252:255], a[24:27], v[116:119]\n"
"buffer_load_dwordx4 a[92:95], v219, %0, s65 offen offset:3072 sc1\n"
"v_mfma_f32_16x16x32_bf16 v[116:119], a[248:251], a[8:11], v[116:119]\n"
"v_mfma_f32_16x16x32_bf16 v[108:111], a[248:251], a[4:7], v[108:111]\n"
"ds_read_b64_tr_b16 a[236:237], v210 offset:24584\n"
"v_mfma_f32_16x16x32_bf16 v[108:111], a[252:255], a[20:23], v[108:111]\n"
"ds_read_b64_tr_b16 a[238:239], v210 offset:28680\n"
"v_mfma_f32_16x16x32_bf16 v[100:103], a[252:255], a[16:19], v[100:103]\n"
"v_mfma_f32_16x16x32_bf16 v[100:103], a[248:251], a[0:3], v[100:103]\n"
"s_waitcnt vmcnt(12) lgkmcnt(4)\n"
"v_mfma_f32_16x16x32_bf16 v[0:3], a[224:227], a[32:35], v[0:3]\n"
"ds_read_b64_tr_b16 a[240:241], v211 offset:16384\n"
"v_mfma_f32_16x16x32_bf16 v[0:3], a[228:231], a[48:51], v[0:3]\n"
"ds_read_b64_tr_b16 a[242:243], v211 offset:20480\n"
"v_mfma_f32_16x16x32_bf16 v[8:11], a[228:231], a[52:55], v[8:11]\n"
"v_mfma_f32_16x16x32_bf16 v[8:11], a[224:227], a[36:39], v[8:11]\n"
"v_accvgpr_write_b32 a0, 0\n"
"v_accvgpr_write_b32 a1, 0\n"
"v_mfma_f32_16x16x32_bf16 v[16:19], a[224:227], a[40:43], v[16:19]\n"
"ds_read_b64_tr_b16 a[244:245], v211 offset:24576\n"
"s_add_u32 s64, s64, 524288\n"
"v_mfma_f32_16x16x32_bf16 v[16:19], a[228:231], a[56:59], v[16:19]\n"
"ds_read_b64_tr_b16 a[246:247], v211 offset:28672\n"
"s_add_u32 m0, %11, 114688\n"
"v_mfma_f32_16x16x32_bf16 v[24:27], a[228:231], a[60:63], v[24:27]\n"
"buffer_load_dwordx4 v220, %1, s64 offen offset:0 lds\n"
"v_mfma_f32_16x16x32_bf16 v[24:27], a[224:227], a[44:47], v[24:27]\n"
"v_accvgpr_write_b32 a2, 0\n"
"v_accvgpr_write_b32 a3, 0\n"
"s_waitcnt lgkmcnt(4)\n"
"v_mfma_f32_16x16x32_bf16 v[28:31], a[232:235], a[44:47], v[28:31]\n"
"ds_read_b64_tr_b16 a[248:249], v211 offset:16392\n"
"v_mfma_f32_16x16x32_bf16 v[28:31], a[236:239], a[60:63], v[28:31]\n"
"ds_read_b64_tr_b16 a[250:251], v211 offset:20488\n"
"v_mfma_f32_16x16x32_bf16 v[20:23], a[236:239], a[56:59], v[20:23]\n"
"buffer_load_dwordx4 v221, %1, s64 offen offset:1024 lds\n"
"v_mfma_f32_16x16x32_bf16 v[20:23], a[232:235], a[40:43], v[20:23]\n"
"v_accvgpr_write_b32 a4, 0\n"
"v_accvgpr_write_b32 a5, 0\n"
"v_mfma_f32_16x16x32_bf16 v[12:15], a[232:235], a[36:39], v[12:15]\n"
"ds_read_b64_tr_b16 a[252:253], v211 offset:24584\n"
"v_mfma_f32_16x16x32_bf16 v[12:15], a[236:239], a[52:55], v[12:15]\n"
"ds_read_b64_tr_b16 a[254:255], v211 offset:28680\n"
"v_mfma_f32_16x16x32_bf16 v[4:7], a[236:239], a[48:51], v[4:7]\n"
"buffer_load_dwordx4 v222, %1, s64 offen offset:2048 lds\n"
"v_mfma_f32_16x16x32_bf16 v[4:7], a[232:235], a[32:35], v[4:7]\n"
"v_accvgpr_write_b32 a6, 0\n"
"v_accvgpr_write_b32 a7, 0\n"
"s_waitcnt lgkmcnt(4)\n"
"v_mfma_f32_16x16x32_bf16 v[32:35], a[240:243], a[32:35], v[32:35]\n"
"ds_read_b64_tr_b16 a[224:225], v212 offset:16384\n"
"v_mfma_f32_16x16x32_bf16 v[32:35], a[244:247], a[48:51], v[32:35]\n"
"ds_read_b64_tr_b16 a[226:227], v212 offset:20480\n"
"v_mfma_f32_16x16x32_bf16 v[40:43], a[244:247], a[52:55], v[40:43]\n"
"buffer_load_dwordx4 v223, %1, s64 offen offset:3072 lds\n"
"v_mfma_f32_16x16x32_bf16 v[40:43], a[240:243], a[36:39], v[40:43]\n"
"v_accvgpr_write_b32 a8, 0\n"
"v_accvgpr_write_b32 a9, 0\n"
"v_mfma_f32_16x16x32_bf16 v[48:51], a[240:243], a[40:43], v[48:51]\n"
"ds_read_b64_tr_b16 a[228:229], v212 offset:24576\n"
"v_mfma_f32_16x16x32_bf16 v[48:51], a[244:247], a[56:59], v[48:51]\n"
"ds_read_b64_tr_b16 a[230:231], v212 offset:28672\n"
"v_mfma_f32_16x16x32_bf16 v[56:59], a[244:247], a[60:63], v[56:59]\n"
"v_mfma_f32_16x16x32_bf16 v[56:59], a[240:243], a[44:47], v[56:59]\n"
"v_accvgpr_write_b32 a10, 0\n"
"v_accvgpr_write_b32 a11, 0\n"
"s_waitcnt lgkmcnt(4)\n"
"v_mfma_f32_16x16x32_bf16 v[60:63], a[248:251], a[44:47], v[60:63]\n"
"ds_read_b64_tr_b16 a[232:233], v212 offset:16392\n"
"v_mfma_f32_16x16x32_bf16 v[60:63], a[252:255], a[60:63], v[60:63]\n"
"ds_read_b64_tr_b16 a[234:235], v212 offset:20488\n"
"s_add_u32 s65, s65, 4096\n"
"v_mfma_f32_16x16x32_bf16 v[52:55], a[252:255], a[56:59], v[52:55]\n"
"buffer_load_dwordx4 a[96:99], v218, %0, s65 offen offset:0 sc1\n"
"v_mfma_f32_16x16x32_bf16 v[52:55], a[248:251], a[40:43], v[52:55]\n"
"v_accvgpr_write_b32 a12, 0\n"
"v_accvgpr_write_b32 a13, 0\n"
"v_mfma_f32_16x16x32_bf16 v[44:47], a[248:251], a[36:39], v[44:47]\n"
"ds_read_b64_tr_b16 a[236:237], v212 offset:24584\n"
"v_mfma_f32_16x16x32_bf16 v[44:47], a[252:255], a[52:55], v[44:47]\n"
"ds_read_b64_tr_b16 a[238:239], v212 offset:28680\n"
"v_mfma_f32_16x16x32_bf16 v[36:39], a[252:255], a[48:51], v[36:39]\n"
"buffer_load_dwordx4 a[100:103], v218, %0, s65 offen offset:1024 sc1\n"
"v_mfma_f32_16x16x32_bf16 v[36:39], a[248:251], a[32:35], v[36:39]\n"
"v_accvgpr_write_b32 a14, 0\n"
"v_accvgpr_write_b32 a15, 0\n"
"s_waitcnt lgkmcnt(4)\n"
"v_mfma_f32_16x16x32_bf16 v[64:67], a[224:227], a[32:35], v[64:67]\n"
"ds_read_b64_tr_b16 a[240:241], v213 offset:16384\n"
"v_mfma_f32_16x16x32_bf16 v[64:67], a[228:231], a[48:51], v[64:67]\n"
"ds_read_b64_tr_b16 a[242:243], v213 offset:20480\n"
"v_mfma_f32_16x16x32_bf16 v[72:75], a[228:231], a[52:55], v[72:75]\n"
"buffer_load_dwordx4 a[104:107], v219, %0, s65 offen offset:0 sc1\n"
"v_mfma_f32_16x16x32_bf16 v[72:75], a[224:227], a[36:39], v[72:75]\n"
"v_accvgpr_write_b32 a16, 0\n"
"v_accvgpr_write_b32 a17, 0\n"
"v_mfma_f32_16x16x32_bf16 v[80:83], a[224:227], a[40:43], v[80:83]\n"
"ds_read_b64_tr_b16 a[244:245], v213 offset:24576\n"
"v_mfma_f32_16x16x32_bf16 v[80:83], a[228:231], a[56:59], v[80:83]\n"
"ds_read_b64_tr_b16 a[246:247], v213 offset:28672\n"
"v_mfma_f32_16x16x32_bf16 v[88:91], a[228:231], a[60:63], v[88:91]\n"
"buffer_load_dwordx4 a[108:111], v219, %0, s65 offen offset:1024 sc1\n"
"v_mfma_f32_16x16x32_bf16 v[88:91], a[224:227], a[44:47], v[88:91]\n"
"v_accvgpr_write_b32 a18, 0\n"
"v_accvgpr_write_b32 a19, 0\n"
"s_waitcnt lgkmcnt(4)\n"
"v_mfma_f32_16x16x32_bf16 v[92:95], a[232:235], a[44:47], v[92:95]\n"
"ds_read_b64_tr_b16 a[248:249], v213 offset:16392\n"
"v_mfma_f32_16x16x32_bf16 v[92:95], a[236:239], a[60:63], v[92:95]\n"
"ds_read_b64_tr_b16 a[250:251], v213 offset:20488\n"
"v_mfma_f32_16x16x32_bf16 v[84:87], a[236:239], a[56:59], v[84:87]\n"
"v_mfma_f32_16x16x32_bf16 v[84:87], a[232:235], a[40:43], v[84:87]\n"
"v_accvgpr_write_b32 a20, 0\n"
"v_accvgpr_write_b32 a21, 0\n"
"v_mfma_f32_16x16x32_bf16 v[76:79], a[232:235], a[36:39], v[76:79]\n"
"ds_read_b64_tr_b16 a[252:253], v213 offset:24584\n"
"v_mfma_f32_16x16x32_bf16 v[76:79], a[236:239], a[52:55], v[76:79]\n"
"ds_read_b64_tr_b16 a[254:255], v213 offset:28680\n"
"v_mfma_f32_16x16x32_bf16 v[68:71], a[236:239], a[48:51], v[68:71]\n"
"buffer_load_dwordx4 a[112:115], v218, %0, s65 offen offset:2048 sc1\n"
"v_mfma_f32_16x16x32_bf16 v[68:71], a[232:235], a[32:35], v[68:71]\n"
"v_accvgpr_write_b32 a22, 0\n"
"v_accvgpr_write_b32 a23, 0\n"
"s_waitcnt lgkmcnt(4)\n"
"v_mfma_f32_16x16x32_bf16 v[96:99], a[240:243], a[32:35], v[96:99]\n"
"s_waitcnt vmcnt(17)\n"
"s_barrier\n"
"ds_read_b64_tr_b16 a[224:225], v210 offset:32768\n"
"v_mfma_f32_16x16x32_bf16 v[96:99], a[244:247], a[48:51], v[96:99]\n"
"ds_read_b64_tr_b16 a[226:227], v210 offset:36864\n"
"v_mfma_f32_16x16x32_bf16 v[104:107], a[244:247], a[52:55], v[104:107]\n"
"buffer_load_dwordx4 a[116:119], v218, %0, s65 offen offset:3072 sc1\n"
"v_mfma_f32_16x16x32_bf16 v[104:107], a[240:243], a[36:39], v[104:107]\n"
"v_accvgpr_write_b32 a24, 0\n"
"v_accvgpr_write_b32 a25, 0\n"
"v_mfma_f32_16x16x32_bf16 v[112:115], a[240:243], a[40:43], v[112:115]\n"
"ds_read_b64_tr_b16 a[228:229], v210 offset:40960\n"
"v_mfma_f32_16x16x32_bf16 v[112:115], a[244:247], a[56:59], v[112:115]\n"
"ds_read_b64_tr_b16 a[230:231], v210 offset:45056\n"
"v_mfma_f32_16x16x32_bf16 v[120:123], a[244:247], a[60:63], v[120:123]\n"
"buffer_load_dwordx4 a[120:123], v219, %0, s65 offen offset:2048 sc1\n"
"v_mfma_f32_16x16x32_bf16 v[120:123], a[240:243], a[44:47], v[120:123]\n"
"v_accvgpr_write_b32 a26, 0\n"
"v_accvgpr_write_b32 a27, 0\n"
"s_waitcnt lgkmcnt(4)\n"
"v_mfma_f32_16x16x32_bf16 v[124:127], a[248:251], a[44:47], v[124:127]\n"
"ds_read_b64_tr_b16 a[232:233], v210 offset:32776\n"
"v_mfma_f32_16x16x32_bf16 v[124:127], a[252:255], a[60:63], v[124:127]\n"
"ds_read_b64_tr_b16 a[234:235], v210 offset:36872\n"
"v_mfma_f32_16x16x32_bf16 v[116:119], a[252:255], a[56:59], v[116:119]\n"
"buffer_load_dwordx4 a[124:127], v219, %0, s65 offen offset:3072 sc1\n"
"v_mfma_f32_16x16x32_bf16 v[116:119], a[248:251], a[40:43], v[116:119]\n"
"v_accvgpr_write_b32 a28, 0\n"
"v_accvgpr_write_b32 a29, 0\n"
"v_mfma_f32_16x16x32_bf16 v[108:111], a[248:251], a[36:39], v[108:111]\n"
"ds_read_b64_tr_b16 a[236:237], v210 offset:40968\n"
"v_mfma_f32_16x16x32_bf16 v[108:111], a[252:255], a[52:55], v[108:111]\n"
"ds_read_b64_tr_b16 a[238:239], v210 offset:45064\n"
"v_mfma_f32_16x16x32_bf16 v[100:103], a[252:255], a[48:51], v[100:103]\n"
"v_mfma_f32_16x16x32_bf16 v[100:103], a[248:251], a[32:35], v[100:103]\n"
"v_accvgpr_write_b32 a30, 0\n"
"v_accvgpr_write_b32 a31, 0\n"
"s_waitcnt vmcnt(12) lgkmcnt(4)\n"
"v_mfma_f32_16x16x32_bf16 v[0:3], a[224:227], a[64:67], v[0:3]\n"
"ds_read_b64_tr_b16 a[240:241], v211 offset:32768\n"
"s_add_u32 s72, %11, 0\n"
"v_mfma_f32_16x16x32_bf16 v[0:3], a[228:231], a[80:83], v[0:3]\n"
"ds_read_b64_tr_b16 a[242:243], v211 offset:36864\n"
"s_add_u32 m0, %11, 0\n"
"v_mfma_f32_16x16x32_bf16 v[8:11], a[228:231], a[84:87], v[8:11]\n"
"buffer_load_dwordx4 v204, %2, s72 offen offset:0 lds\n"
"v_mfma_f32_16x16x32_bf16 v[8:11], a[224:227], a[68:71], v[8:11]\n"
"v_accvgpr_write_b32 a32, 0\n"
"v_accvgpr_write_b32 a33, 0\n"
"v_mfma_f32_16x16x32_bf16 v[16:19], a[224:227], a[72:75], v[16:19]\n"
"ds_read_b64_tr_b16 a[244:245], v211 offset:40960\n"
"v_mfma_f32_16x16x32_bf16 v[16:19], a[228:231], a[88:91], v[16:19]\n"
"ds_read_b64_tr_b16 a[246:247], v211 offset:45056\n"
"v_mfma_f32_16x16x32_bf16 v[24:27], a[228:231], a[92:95], v[24:27]\n"
"buffer_load_dwordx4 v205, %2, s72 offen offset:1024 lds\n"
"v_mfma_f32_16x16x32_bf16 v[24:27], a[224:227], a[76:79], v[24:27]\n"
"v_accvgpr_write_b32 a34, 0\n"
"v_accvgpr_write_b32 a35, 0\n"
"s_waitcnt lgkmcnt(4)\n"
"v_mfma_f32_16x16x32_bf16 v[28:31], a[232:235], a[76:79], v[28:31]\n"
"ds_read_b64_tr_b16 a[248:249], v211 offset:32776\n"
"v_mfma_f32_16x16x32_bf16 v[28:31], a[236:239], a[92:95], v[28:31]\n"
"ds_read_b64_tr_b16 a[250:251], v211 offset:36872\n"
"v_mfma_f32_16x16x32_bf16 v[20:23], a[236:239], a[88:91], v[20:23]\n"
"buffer_load_dwordx4 v206, %2, s72 offen offset:2048 lds\n"
"v_mfma_f32_16x16x32_bf16 v[20:23], a[232:235], a[72:75], v[20:23]\n"
"v_accvgpr_write_b32 a36, 0\n"
"v_accvgpr_write_b32 a37, 0\n"
"v_mfma_f32_16x16x32_bf16 v[12:15], a[232:235], a[68:71], v[12:15]\n"
"ds_read_b64_tr_b16 a[252:253], v211 offset:40968\n"
"v_mfma_f32_16x16x32_bf16 v[12:15], a[236:239], a[84:87], v[12:15]\n"
"ds_read_b64_tr_b16 a[254:255], v211 offset:45064\n"
"v_mfma_f32_16x16x32_bf16 v[4:7], a[236:239], a[80:83], v[4:7]\n"
"buffer_load_dwordx4 v207, %2, s72 offen offset:3072 lds\n"
"v_mfma_f32_16x16x32_bf16 v[4:7], a[232:235], a[64:67], v[4:7]\n"
"v_accvgpr_write_b32 a38, 0\n"
"v_accvgpr_write_b32 a39, 0\n"
"s_waitcnt lgkmcnt(4)\n"
"v_mfma_f32_16x16x32_bf16 v[32:35], a[240:243], a[64:67], v[32:35]\n"
"ds_read_b64_tr_b16 a[224:225], v212 offset:32768\n"
"s_add_u32 s72, %11, 16384\n"
"v_mfma_f32_16x16x32_bf16 v[32:35], a[244:247], a[80:83], v[32:35]\n"
"ds_read_b64_tr_b16 a[226:227], v212 offset:36864\n"
"s_add_u32 m0, %11, 16384\n"
"v_mfma_f32_16x16x32_bf16 v[40:43], a[244:247], a[84:87], v[40:43]\n"
"buffer_load_dwordx4 v204, %2, s72 offen offset:0 lds\n"
"v_mfma_f32_16x16x32_bf16 v[40:43], a[240:243], a[68:71], v[40:43]\n"
"v_accvgpr_write_b32 a40, 0\n"
"v_accvgpr_write_b32 a41, 0\n"
"v_mfma_f32_16x16x32_bf16 v[48:51], a[240:243], a[72:75], v[48:51]\n"
"ds_read_b64_tr_b16 a[228:229], v212 offset:40960\n"
"v_mfma_f32_16x16x32_bf16 v[48:51], a[244:247], a[88:91], v[48:51]\n"
"ds_read_b64_tr_b16 a[230:231], v212 offset:45056\n"
"v_mfma_f32_16x16x32_bf16 v[56:59], a[244:247], a[92:95], v[56:59]\n"
"buffer_load_dwordx4 v205, %2, s72 offen offset:1024 lds\n"
"v_mfma_f32_16x16x32_bf16 v[56:59], a[240:243], a[76:79], v[56:59]\n"
"v_accvgpr_write_b32 a42, 0\n"
"v_accvgpr_write_b32 a43, 0\n"
"s_waitcnt lgkmcnt(4)\n"
"v_mfma_f32_16x16x32_bf16 v[60:63], a[248:251], a[76:79], v[60:63]\n"
"ds_read_b64_tr_b16 a[232:233], v212 offset:32776\n"
"v_mfma_f32_16x16x32_bf16 v[60:63], a[252:255], a[92:95], v[60:63]\n"
"ds_read_b64_tr_b16 a[234:235], v212 offset:36872\n"
"v_mfma_f32_16x16x32_bf16 v[52:55], a[252:255], a[88:91], v[52:55]\n"
"buffer_load_dwordx4 v206, %2, s72 offen offset:2048 lds\n"
"v_mfma_f32_16x16x32_bf16 v[52:55], a[248:251], a[72:75], v[52:55]\n"
"v_accvgpr_write_b32 a44, 0\n"
"v_accvgpr_write_b32 a45, 0\n"
"v_mfma_f32_16x16x32_bf16 v[44:47], a[248:251], a[68:71], v[44:47]\n"
"ds_read_b64_tr_b16 a[236:237], v212 offset:40968\n"
"v_mfma_f32_16x16x32_bf16 v[44:47], a[252:255], a[84:87], v[44:47]\n"
"ds_read_b64_tr_b16 a[238:239], v212 offset:45064\n"
"v_mfma_f32_16x16x32_bf16 v[36:39], a[252:255], a[80:83], v[36:39]\n"
"buffer_load_dwordx4 v207, %2, s72 offen offset:3072 lds\n"
"v_mfma_f32_16x16x32_bf16 v[36:39], a[248:251], a[64:67], v[36:39]\n"
"v_accvgpr_write_b32 a46, 0\n"
"v_accvgpr_write_b32 a47, 0\n"
"s_waitcnt lgkmcnt(4)\n"
"v_mfma_f32_16x16x32_bf16 v[64:67], a[224:227], a[64:67], v[64:67]\n"
"ds_read_b64_tr_b16 a[240:241], v213 offset:32768\n"
"s_add_u32 s72, %11, 32768\n"
"v_mfma_f32_16x16x32_bf16 v[64:67], a[228:231], a[80:83], v[64:67]\n"
"ds_read_b64_tr_b16 a[242:243], v213 offset:36864\n"
"s_add_u32 m0, %11, 65536\n"
"v_mfma_f32_16x16x32_bf16 v[72:75], a[228:231], a[84:87], v[72:75]\n"
"buffer_load_dwordx4 v208, %3, s72 offen offset:0 lds\n"
"v_mfma_f32_16x16x32_bf16 v[72:75], a[224:227], a[68:71], v[72:75]\n"
"v_accvgpr_write_b32 a48, 0\n"
"v_accvgpr_write_b32 a49, 0\n"
"v_mfma_f32_16x16x32_bf16 v[80:83], a[224:227], a[72:75], v[80:83]\n"
"ds_read_b64_tr_b16 a[244:245], v213 offset:40960\n"
"v_mfma_f32_16x16x32_bf16 v[80:83], a[228:231], a[88:91], v[80:83]\n"
"ds_read_b64_tr_b16 a[246:247], v213 offset:45056\n"
"v_mfma_f32_16x16x32_bf16 v[88:91], a[228:231], a[92:95], v[88:91]\n"
"buffer_load_dwordx4 v209, %3, s72 offen offset:1024 lds\n"
"v_mfma_f32_16x16x32_bf16 v[88:91], a[224:227], a[76:79], v[88:91]\n"
"v_accvgpr_write_b32 a50, 0\n"
"v_accvgpr_write_b32 a51, 0\n"
"s_waitcnt lgkmcnt(4)\n"
"v_mfma_f32_16x16x32_bf16 v[92:95], a[232:235], a[76:79], v[92:95]\n"
"ds_read_b64_tr_b16 a[248:249], v213 offset:32776\n"
"v_mfma_f32_16x16x32_bf16 v[92:95], a[236:239], a[92:95], v[92:95]\n"
"ds_read_b64_tr_b16 a[250:251], v213 offset:36872\n"
"v_mfma_f32_16x16x32_bf16 v[84:87], a[236:239], a[88:91], v[84:87]\n"
"buffer_load_dwordx4 v208, %3, s72 offen offset:2048 lds\n"
"v_mfma_f32_16x16x32_bf16 v[84:87], a[232:235], a[72:75], v[84:87]\n"
"v_accvgpr_write_b32 a52, 0\n"
"v_accvgpr_write_b32 a53, 0\n"
"v_mfma_f32_16x16x32_bf16 v[76:79], a[232:235], a[68:71], v[76:79]\n"
"ds_read_b64_tr_b16 a[252:253], v213 offset:40968\n"
"v_mfma_f32_16x16x32_bf16 v[76:79], a[236:239], a[84:87], v[76:79]\n"
"ds_read_b64_tr_b16 a[254:255], v213 offset:45064\n"
"v_mfma_f32_16x16x32_bf16 v[68:71], a[236:239], a[80:83], v[68:71]\n"
"buffer_load_dwordx4 v209, %3, s72 offen offset:3072 lds\n"
"v_mfma_f32_16x16x32_bf16 v[68:71], a[232:235], a[64:67], v[68:71]\n"
"v_accvgpr_write_b32 a54, 0\n"
"v_accvgpr_write_b32 a55, 0\n"
"s_waitcnt lgkmcnt(4)\n"
"v_mfma_f32_16x16x32_bf16 v[96:99], a[240:243], a[64:67], v[96:99]\n"
"s_waitcnt vmcnt(20)\n"
"s_barrier\n"
"ds_read_b64_tr_b16 a[224:225], v210 offset:49152\n"
"s_add_u32 s72, %11, 32768\n"
"v_mfma_f32_16x16x32_bf16 v[96:99], a[244:247], a[80:83], v[96:99]\n"
"ds_read_b64_tr_b16 a[226:227], v210 offset:53248\n"
"s_add_u32 m0, %11, 32768\n"
"v_mfma_f32_16x16x32_bf16 v[104:107], a[244:247], a[84:87], v[104:107]\n"
"buffer_load_dwordx4 v204, %2, s72 offen offset:0 lds\n"
"v_mfma_f32_16x16x32_bf16 v[104:107], a[240:243], a[68:71], v[104:107]\n"
"v_accvgpr_write_b32 a56, 0\n"
"v_accvgpr_write_b32 a57, 0\n"
"v_mfma_f32_16x16x32_bf16 v[112:115], a[240:243], a[72:75], v[112:115]\n"
"ds_read_b64_tr_b16 a[228:229], v210 offset:57344\n"
"v_mfma_f32_16x16x32_bf16 v[112:115], a[244:247], a[88:91], v[112:115]\n"
"ds_read_b64_tr_b16 a[230:231], v210 offset:61440\n"
"v_mfma_f32_16x16x32_bf16 v[120:123], a[244:247], a[92:95], v[120:123]\n"
"buffer_load_dwordx4 v205, %2, s72 offen offset:1024 lds\n"
"v_mfma_f32_16x16x32_bf16 v[120:123], a[240:243], a[76:79], v[120:123]\n"
"v_accvgpr_write_b32 a58, 0\n"
"v_accvgpr_write_b32 a59, 0\n"
"s_waitcnt lgkmcnt(4)\n"
"v_mfma_f32_16x16x32_bf16 v[124:127], a[248:251], a[76:79], v[124:127]\n"
"ds_read_b64_tr_b16 a[232:233], v210 offset:49160\n"
"v_mfma_f32_16x16x32_bf16 v[124:127], a[252:255], a[92:95], v[124:127]\n"
"ds_read_b64_tr_b16 a[234:235], v210 offset:53256\n"
"v_mfma_f32_16x16x32_bf16 v[116:119], a[252:255], a[88:91], v[116:119]\n"
"buffer_load_dwordx4 v206, %2, s72 offen offset:2048 lds\n"
"v_mfma_f32_16x16x32_bf16 v[116:119], a[248:251], a[72:75], v[116:119]\n"
"v_accvgpr_write_b32 a60, 0\n"
"v_accvgpr_write_b32 a61, 0\n"
"v_mfma_f32_16x16x32_bf16 v[108:111], a[248:251], a[68:71], v[108:111]\n"
"ds_read_b64_tr_b16 a[236:237], v210 offset:57352\n"
"v_mfma_f32_16x16x32_bf16 v[108:111], a[252:255], a[84:87], v[108:111]\n"
"ds_read_b64_tr_b16 a[238:239], v210 offset:61448\n"
"v_mfma_f32_16x16x32_bf16 v[100:103], a[252:255], a[80:83], v[100:103]\n"
"buffer_load_dwordx4 v207, %2, s72 offen offset:3072 lds\n"
"v_mfma_f32_16x16x32_bf16 v[100:103], a[248:251], a[64:67], v[100:103]\n"
"v_accvgpr_write_b32 a62, 0\n"
"v_accvgpr_write_b32 a63, 0\n"
"s_waitcnt vmcnt(16) lgkmcnt(4)\n"
"v_mfma_f32_16x16x32_bf16 v[0:3], a[224:227], a[96:99], v[0:3]\n"
"ds_read_b64_tr_b16 a[240:241], v211 offset:49152\n"
"s_add_u32 s72, %11, 49152\n"
"v_mfma_f32_16x16x32_bf16 v[0:3], a[228:231], a[112:115], v[0:3]\n"
"ds_read_b64_tr_b16 a[242:243], v211 offset:53248\n"
"s_add_u32 m0, %11, 81920\n"
"v_mfma_f32_16x16x32_bf16 v[8:11], a[228:231], a[116:119], v[8:11]\n"
"v_accvgpr_write_b32 a64, 0\n"
"v_accvgpr_write_b32 a65, 0\n"
"buffer_load_dwordx4 v208, %3, s72 offen offset:0 lds\n"
"v_mfma_f32_16x16x32_bf16 v[8:11], a[224:227], a[100:103], v[8:11]\n"
"v_accvgpr_write_b32 a66, 0\n"
"v_accvgpr_write_b32 a67, 0\n"
"v_mfma_f32_16x16x32_bf16 v[16:19], a[224:227], a[104:107], v[16:19]\n"
"ds_read_b64_tr_b16 a[244:245], v211 offset:57344\n"
"v_mfma_f32_16x16x32_bf16 v[16:19], a[228:231], a[120:123], v[16:19]\n"
"ds_read_b64_tr_b16 a[246:247], v211 offset:61440\n"
"v_mfma_f32_16x16x32_bf16 v[24:27], a[228:231], a[124:127], v[24:27]\n"
"v_accvgpr_write_b32 a68, 0\n"
"v_accvgpr_write_b32 a69, 0\n"
"buffer_load_dwordx4 v209, %3, s72 offen offset:1024 lds\n"
"v_mfma_f32_16x16x32_bf16 v[24:27], a[224:227], a[108:111], v[24:27]\n"
"v_accvgpr_write_b32 a70, 0\n"
"v_accvgpr_write_b32 a71, 0\n"
"s_waitcnt lgkmcnt(4)\n"
"v_mfma_f32_16x16x32_bf16 v[28:31], a[232:235], a[108:111], v[28:31]\n"
"ds_read_b64_tr_b16 a[248:249], v211 offset:49160\n"
"v_mfma_f32_16x16x32_bf16 v[28:31], a[236:239], a[124:127], v[28:31]\n"
"ds_read_b64_tr_b16 a[250:251], v211 offset:53256\n"
"v_mfma_f32_16x16x32_bf16 v[20:23], a[236:239], a[120:123], v[20:23]\n"
"v_accvgpr_write_b32 a72, 0\n"
"v_accvgpr_write_b32 a73, 0\n"
"buffer_load_dwordx4 v208, %3, s72 offen offset:2048 lds\n"
"v_mfma_f32_16x16x32_bf16 v[20:23], a[232:235], a[104:107], v[20:23]\n"
"v_accvgpr_write_b32 a74, 0\n"
"v_accvgpr_write_b32 a75, 0\n"
"v_mfma_f32_16x16x32_bf16 v[12:15], a[232:235], a[100:103], v[12:15]\n"
"ds_read_b64_tr_b16 a[252:253], v211 offset:57352\n"
"v_mfma_f32_16x16x32_bf16 v[12:15], a[236:239], a[116:119], v[12:15]\n"
"ds_read_b64_tr_b16 a[254:255], v211 offset:61448\n"
"v_mfma_f32_16x16x32_bf16 v[4:7], a[236:239], a[112:115], v[4:7]\n"
"v_accvgpr_write_b32 a76, 0\n"
"v_accvgpr_write_b32 a77, 0\n"
"buffer_load_dwordx4 v209, %3, s72 offen offset:3072 lds\n"
"v_mfma_f32_16x16x32_bf16 v[4:7], a[232:235], a[96:99], v[4:7]\n"
"v_accvgpr_write_b32 a78, 0\n"
"v_accvgpr_write_b32 a79, 0\n"
"s_waitcnt lgkmcnt(4)\n"
"v_mfma_f32_16x16x32_bf16 v[32:35], a[240:243], a[96:99], v[32:35]\n"
"ds_read_b64_tr_b16 a[224:225], v212 offset:49152\n"
"s_add_u32 s72, %11, 49152\n"
"v_mfma_f32_16x16x32_bf16 v[32:35], a[244:247], a[112:115], v[32:35]\n"
"ds_read_b64_tr_b16 a[226:227], v212 offset:53248\n"
"s_add_u32 m0, %11, 49152\n"
"v_mfma_f32_16x16x32_bf16 v[40:43], a[244:247], a[116:119], v[40:43]\n"
"v_accvgpr_write_b32 a80, 0\n"
"v_accvgpr_write_b32 a81, 0\n"
"buffer_load_dwordx4 v204, %2, s72 offen offset:0 lds\n"
"v_mfma_f32_16x16x32_bf16 v[40:43], a[240:243], a[100:103], v[40:43]\n"
"v_accvgpr_write_b32 a82, 0\n"
"v_accvgpr_write_b32 a83, 0\n"
"v_mfma_f32_16x16x32_bf16 v[48:51], a[240:243], a[104:107], v[48:51]\n"
"ds_read_b64_tr_b16 a[228:229], v212 offset:57344\n"
"v_mfma_f32_16x16x32_bf16 v[48:51], a[244:247], a[120:123], v[48:51]\n"
"ds_read_b64_tr_b16 a[230:231], v212 offset:61440\n"
"v_mfma_f32_16x16x32_bf16 v[56:59], a[244:247], a[124:127], v[56:59]\n"
"v_accvgpr_write_b32 a84, 0\n"
"v_accvgpr_write_b32 a85, 0\n"
"buffer_load_dwordx4 v205, %2, s72 offen offset:1024 lds\n"
"v_mfma_f32_16x16x32_bf16 v[56:59], a[240:243], a[108:111], v[56:59]\n"
"v_accvgpr_write_b32 a86, 0\n"
"v_accvgpr_write_b32 a87, 0\n"
"s_waitcnt lgkmcnt(4)\n"
"v_mfma_f32_16x16x32_bf16 v[60:63], a[248:251], a[108:111], v[60:63]\n"
"ds_read_b64_tr_b16 a[232:233], v212 offset:49160\n"
"v_mfma_f32_16x16x32_bf16 v[60:63], a[252:255], a[124:127], v[60:63]\n"
"ds_read_b64_tr_b16 a[234:235], v212 offset:53256\n"
"v_mfma_f32_16x16x32_bf16 v[52:55], a[252:255], a[120:123], v[52:55]\n"
"v_accvgpr_write_b32 a88, 0\n"
"v_accvgpr_write_b32 a89, 0\n"
"buffer_load_dwordx4 v206, %2, s72 offen offset:2048 lds\n"
"v_mfma_f32_16x16x32_bf16 v[52:55], a[248:251], a[104:107], v[52:55]\n"
"v_accvgpr_write_b32 a90, 0\n"
"v_accvgpr_write_b32 a91, 0\n"
"v_mfma_f32_16x16x32_bf16 v[44:47], a[248:251], a[100:103], v[44:47]\n"
"ds_read_b64_tr_b16 a[236:237], v212 offset:57352\n"
"v_mfma_f32_16x16x32_bf16 v[44:47], a[252:255], a[116:119], v[44:47]\n"
"ds_read_b64_tr_b16 a[238:239], v212 offset:61448\n"
"v_mfma_f32_16x16x32_bf16 v[36:39], a[252:255], a[112:115], v[36:39]\n"
"v_accvgpr_write_b32 a92, 0\n"
"v_accvgpr_write_b32 a93, 0\n"
"buffer_load_dwordx4 v207, %2, s72 offen offset:3072 lds\n"
"v_mfma_f32_16x16x32_bf16 v[36:39], a[248:251], a[96:99], v[36:39]\n"
"v_accvgpr_write_b32 a94, 0\n"
"v_accvgpr_write_b32 a95, 0\n"
"s_waitcnt lgkmcnt(4)\n"
"v_mfma_f32_16x16x32_bf16 v[64:67], a[224:227], a[96:99], v[64:67]\n"
"ds_read_b64_tr_b16 a[240:241], v213 offset:49152\n"
"s_waitcnt vmcnt(20)\n"
"s_barrier\n"
"v_mfma_f32_16x16x32_bf16 v[64:67], a[228:231], a[112:115], v[64:67]\n"
"ds_read_b64_tr_b16 a[242:243], v213 offset:53248\n"
"v_mfma_f32_16x16x32_bf16 v[72:75], a[228:231], a[116:119], v[72:75]\n"
"ds_read_b128 a[192:195], v192 offset:0\n"
"v_mfma_f32_16x16x32_bf16 v[72:75], a[224:227], a[100:103], v[72:75]\n"
"ds_read_b128 a[196:199], v192 offset:4096\n"
"v_mfma_f32_16x16x32_bf16 v[80:83], a[224:227], a[104:107], v[80:83]\n"
"ds_read_b64_tr_b16 a[244:245], v213 offset:57344\n"
"v_mfma_f32_16x16x32_bf16 v[80:83], a[228:231], a[120:123], v[80:83]\n"
"ds_read_b64_tr_b16 a[246:247], v213 offset:61440\n"
"v_mfma_f32_16x16x32_bf16 v[88:91], a[228:231], a[124:127], v[88:91]\n"
"ds_read_b128 a[200:203], v193 offset:0\n"
"v_mfma_f32_16x16x32_bf16 v[88:91], a[224:227], a[108:111], v[88:91]\n"
"ds_read_b128 a[204:207], v193 offset:4096\n"
"s_waitcnt lgkmcnt(8)\n"
"v_mfma_f32_16x16x32_bf16 v[92:95], a[232:235], a[108:111], v[92:95]\n"
"ds_read_b64_tr_b16 a[248:249], v213 offset:49160\n"
"v_mfma_f32_16x16x32_bf16 v[92:95], a[236:239], a[124:127], v[92:95]\n"
"ds_read_b64_tr_b16 a[250:251], v213 offset:53256\n"
"v_mfma_f32_16x16x32_bf16 v[84:87], a[236:239], a[120:123], v[84:87]\n"
"ds_read_b128 a[208:211], v194 offset:0\n"
"v_mfma_f32_16x16x32_bf16 v[84:87], a[232:235], a[104:107], v[84:87]\n"
"ds_read_b128 a[212:215], v194 offset:4096\n"
"v_mfma_f32_16x16x32_bf16 v[76:79], a[232:235], a[100:103], v[76:79]\n"
"ds_read_b64_tr_b16 a[252:253], v213 offset:57352\n"
"v_mfma_f32_16x16x32_bf16 v[76:79], a[236:239], a[116:119], v[76:79]\n"
"ds_read_b64_tr_b16 a[254:255], v213 offset:61448\n"
"v_mfma_f32_16x16x32_bf16 v[68:71], a[236:239], a[112:115], v[68:71]\n"
"ds_read_b128 a[216:219], v195 offset:0\n"
"v_mfma_f32_16x16x32_bf16 v[68:71], a[232:235], a[96:99], v[68:71]\n"
"ds_read_b128 a[220:223], v195 offset:4096\n"
"s_waitcnt lgkmcnt(10)\n"
"v_mfma_f32_16x16x32_bf16 v[96:99], a[240:243], a[96:99], v[96:99]\n"
"s_add_u32 s72, %11, 65536\n"
"v_cvt_pk_bf16_f32 v128, v0, v1\n"
"v_mfma_f32_16x16x32_bf16 v[96:99], a[244:247], a[112:115], v[96:99]\n"
"s_add_u32 m0, %11, 98304\n"
"v_cvt_pk_bf16_f32 v129, v2, v3\n"
"v_mfma_f32_16x16x32_bf16 v[104:107], a[244:247], a[116:119], v[104:107]\n"
"buffer_load_dwordx4 v208, %3, s72 offen offset:0 lds\n"
"v_cvt_pk_bf16_f32 v130, v4, v5\n"
"v_mfma_f32_16x16x32_bf16 v[104:107], a[240:243], a[100:103], v[104:107]\n"
"v_cvt_pk_bf16_f32 v131, v6, v7\n"
"v_mfma_f32_16x16x32_bf16 v[112:115], a[240:243], a[104:107], v[112:115]\n"
"v_cvt_pk_bf16_f32 v144, v8, v9\n"
"v_mfma_f32_16x16x32_bf16 v[112:115], a[244:247], a[120:123], v[112:115]\n"
"v_cvt_pk_bf16_f32 v145, v10, v11\n"
"v_mfma_f32_16x16x32_bf16 v[120:123], a[244:247], a[124:127], v[120:123]\n"
"buffer_load_dwordx4 v209, %3, s72 offen offset:1024 lds\n"
"v_cvt_pk_bf16_f32 v146, v12, v13\n"
"v_mfma_f32_16x16x32_bf16 v[120:123], a[240:243], a[108:111], v[120:123]\n"
"v_cvt_pk_bf16_f32 v147, v14, v15\n"
"s_waitcnt lgkmcnt(2)\n"
"v_mfma_f32_16x16x32_bf16 v[124:127], a[248:251], a[108:111], v[124:127]\n"
"v_cvt_pk_bf16_f32 v160, v16, v17\n"
"v_mfma_f32_16x16x32_bf16 v[124:127], a[252:255], a[124:127], v[124:127]\n"
"v_cvt_pk_bf16_f32 v161, v18, v19\n"
"v_mfma_f32_16x16x32_bf16 v[116:119], a[252:255], a[120:123], v[116:119]\n"
"buffer_load_dwordx4 v208, %3, s72 offen offset:2048 lds\n"
"v_cvt_pk_bf16_f32 v162, v20, v21\n"
"v_mfma_f32_16x16x32_bf16 v[116:119], a[248:251], a[104:107], v[116:119]\n"
"v_cvt_pk_bf16_f32 v163, v22, v23\n"
"v_mfma_f32_16x16x32_bf16 v[108:111], a[248:251], a[100:103], v[108:111]\n"
"v_cvt_pk_bf16_f32 v176, v24, v25\n"
"v_mfma_f32_16x16x32_bf16 v[108:111], a[252:255], a[116:119], v[108:111]\n"
"v_cvt_pk_bf16_f32 v177, v26, v27\n"
"v_mfma_f32_16x16x32_bf16 v[100:103], a[252:255], a[112:115], v[100:103]\n"
"buffer_load_dwordx4 v209, %3, s72 offen offset:3072 lds\n"
"v_cvt_pk_bf16_f32 v178, v28, v29\n"
"v_mfma_f32_16x16x32_bf16 v[100:103], a[248:251], a[96:99], v[100:103]\n"
"v_cvt_pk_bf16_f32 v179, v30, v31\n"
"s_waitcnt lgkmcnt(0)\n"
"v_mfma_f32_16x16x32_bf16 v[0:3], a[192:195], v[128:131], 0\n"
"v_cvt_pk_bf16_f32 v132, v32, v33\n"
"v_cvt_pk_bf16_f32 v133, v34, v35\n"
"v_accvgpr_write_b32 a96, 0\n"
"v_mfma_f32_16x16x32_bf16 v[8:11], a[192:195], v[144:147], 0\n"
"v_cvt_pk_bf16_f32 v134, v36, v37\n"
"v_cvt_pk_bf16_f32 v135, v38, v39\n"
"ds_read_b128 a[224:227], v192 offset:8192\n"
"v_accvgpr_write_b32 a97, 0\n"
"v_mfma_f32_16x16x32_bf16 v[16:19], a[192:195], v[160:163], 0\n"
"v_cvt_pk_bf16_f32 v148, v40, v41\n"
"v_cvt_pk_bf16_f32 v149, v42, v43\n"
"v_accvgpr_write_b32 a98, 0\n"
"v_mfma_f32_16x16x32_bf16 v[24:27], a[192:195], v[176:179], 0\n"
"v_cvt_pk_bf16_f32 v150, v44, v45\n"
"v_cvt_pk_bf16_f32 v151, v46, v47\n"
"ds_read_b128 a[228:231], v192 offset:12288\n"
"v_accvgpr_write_b32 a99, 0\n"
"v_mfma_f32_16x16x32_bf16 v[28:31], a[196:199], v[176:179], 0\n"
"v_cvt_pk_bf16_f32 v164, v48, v49\n"
"v_cvt_pk_bf16_f32 v165, v50, v51\n"
"v_accvgpr_write_b32 a100, 0\n"
"v_mfma_f32_16x16x32_bf16 v[20:23], a[196:199], v[160:163], 0\n"
"v_cvt_pk_bf16_f32 v166, v52, v53\n"
"v_cvt_pk_bf16_f32 v167, v54, v55\n"
"ds_read_b128 a[232:235], v193 offset:8192\n"
"v_accvgpr_write_b32 a101, 0\n"
"v_mfma_f32_16x16x32_bf16 v[12:15], a[196:199], v[144:147], 0\n"
"v_cvt_pk_bf16_f32 v180, v56, v57\n"
"v_cvt_pk_bf16_f32 v181, v58, v59\n"
"v_accvgpr_write_b32 a102, 0\n"
"v_mfma_f32_16x16x32_bf16 v[4:7], a[196:199], v[128:131], 0\n"
"v_cvt_pk_bf16_f32 v182, v60, v61\n"
"v_cvt_pk_bf16_f32 v183, v62, v63\n"
"ds_read_b128 a[236:239], v193 offset:12288\n"
"v_accvgpr_write_b32 a103, 0\n"
"v_mfma_f32_16x16x32_bf16 v[0:3], a[200:203], v[132:135], v[0:3]\n"
"v_cvt_pk_bf16_f32 v136, v64, v65\n"
"v_cvt_pk_bf16_f32 v137, v66, v67\n"
"v_accvgpr_write_b32 a104, 0\n"
"v_mfma_f32_16x16x32_bf16 v[8:11], a[200:203], v[148:151], v[8:11]\n"
"v_cvt_pk_bf16_f32 v138, v68, v69\n"
"v_cvt_pk_bf16_f32 v139, v70, v71\n"
"ds_read_b128 a[240:243], v194 offset:8192\n"
"v_accvgpr_write_b32 a105, 0\n"
"v_mfma_f32_16x16x32_bf16 v[16:19], a[200:203], v[164:167], v[16:19]\n"
"v_cvt_pk_bf16_f32 v152, v72, v73\n"
"v_cvt_pk_bf16_f32 v153, v74, v75\n"
"v_accvgpr_write_b32 a106, 0\n"
"v_mfma_f32_16x16x32_bf16 v[24:27], a[200:203], v[180:183], v[24:27]\n"
"v_cvt_pk_bf16_f32 v154, v76, v77\n"
"v_cvt_pk_bf16_f32 v155, v78, v79\n"
"ds_read_b128 a[244:247], v194 offset:12288\n"
"v_accvgpr_write_b32 a107, 0\n"
"v_mfma_f32_16x16x32_bf16 v[28:31], a[204:207], v[180:183], v[28:31]\n"
"v_cvt_pk_bf16_f32 v168, v80, v81\n"
"v_cvt_pk_bf16_f32 v169, v82, v83\n"
"v_accvgpr_write_b32 a108, 0\n"
"v_mfma_f32_16x16x32_bf16 v[20:23], a[204:207], v[164:167], v[20:23]\n"
"v_cvt_pk_bf16_f32 v170, v84, v85\n"
"v_cvt_pk_bf16_f32 v171, v86, v87\n"
"ds_read_b128 a[248:251], v195 offset:8192\n"
"v_accvgpr_write_b32 a109, 0\n"
"v_mfma_f32_16x16x32_bf16 v[12:15], a[204:207], v[148:151], v[12:15]\n"
"v_cvt_pk_bf16_f32 v184, v88, v89\n"
"v_cvt_pk_bf16_f32 v185, v90, v91\n"
"v_accvgpr_write_b32 a110, 0\n"
"v_mfma_f32_16x16x32_bf16 v[4:7], a[204:207], v[132:135], v[4:7]\n"
"v_cvt_pk_bf16_f32 v186, v92, v93\n"
"v_cvt_pk_bf16_f32 v187, v94, v95\n"
"ds_read_b128 a[252:255], v195 offset:12288\n"
"v_accvgpr_write_b32 a111, 0\n"
"v_mfma_f32_16x16x32_bf16 v[0:3], a[208:211], v[136:139], v[0:3]\n"
"v_cvt_pk_bf16_f32 v140, v96, v97\n"
"v_cvt_pk_bf16_f32 v141, v98, v99\n"
"v_accvgpr_write_b32 a112, 0\n"
"v_mfma_f32_16x16x32_bf16 v[8:11], a[208:211], v[152:155], v[8:11]\n"
"v_cvt_pk_bf16_f32 v142, v100, v101\n"
"v_cvt_pk_bf16_f32 v143, v102, v103\n"
"v_accvgpr_write_b32 a113, 0\n"
"v_mfma_f32_16x16x32_bf16 v[16:19], a[208:211], v[168:171], v[16:19]\n"
"v_cvt_pk_bf16_f32 v156, v104, v105\n"
"v_cvt_pk_bf16_f32 v157, v106, v107\n"
"v_accvgpr_write_b32 a114, 0\n"
"v_mfma_f32_16x16x32_bf16 v[24:27], a[208:211], v[184:187], v[24:27]\n"
"v_cvt_pk_bf16_f32 v158, v108, v109\n"
"v_cvt_pk_bf16_f32 v159, v110, v111\n"
"v_accvgpr_write_b32 a115, 0\n"
"v_mfma_f32_16x16x32_bf16 v[28:31], a[212:215], v[184:187], v[28:31]\n"
"v_cvt_pk_bf16_f32 v172, v112, v113\n"
"v_cvt_pk_bf16_f32 v173, v114, v115\n"
"v_accvgpr_write_b32 a116, 0\n"
"v_mfma_f32_16x16x32_bf16 v[20:23], a[212:215], v[168:171], v[20:23]\n"
"v_cvt_pk_bf16_f32 v174, v116, v117\n"
"v_cvt_pk_bf16_f32 v175, v118, v119\n"
"v_accvgpr_write_b32 a117, 0\n"
"v_mfma_f32_16x16x32_bf16 v[12:15], a[212:215], v[152:155], v[12:15]\n"
"v_cvt_pk_bf16_f32 v188, v120, v121\n"
"v_cvt_pk_bf16_f32 v189, v122, v123\n"
"v_accvgpr_write_b32 a118, 0\n"
"v_mfma_f32_16x16x32_bf16 v[4:7], a[212:215], v[136:139], v[4:7]\n"
"v_cvt_pk_bf16_f32 v190, v124, v125\n"
"v_cvt_pk_bf16_f32 v191, v126, v127\n"
"v_accvgpr_write_b32 a119, 0\n"
"v_mfma_f32_16x16x32_bf16 v[0:3], a[216:219], v[140:143], v[0:3]\n"
"v_accvgpr_write_b32 a120, 0\n"
"v_mfma_f32_16x16x32_bf16 v[8:11], a[216:219], v[156:159], v[8:11]\n"
"v_accvgpr_write_b32 a121, 0\n"
"v_mfma_f32_16x16x32_bf16 v[16:19], a[216:219], v[172:175], v[16:19]\n"
"v_accvgpr_write_b32 a122, 0\n"
"v_mfma_f32_16x16x32_bf16 v[24:27], a[216:219], v[188:191], v[24:27]\n"
"v_accvgpr_write_b32 a123, 0\n"
"v_mfma_f32_16x16x32_bf16 v[28:31], a[220:223], v[188:191], v[28:31]\n"
"v_accvgpr_write_b32 a124, 0\n"
"v_mfma_f32_16x16x32_bf16 v[20:23], a[220:223], v[172:175], v[20:23]\n"
"v_accvgpr_write_b32 a125, 0\n"
"v_mfma_f32_16x16x32_bf16 v[12:15], a[220:223], v[156:159], v[12:15]\n"
"v_accvgpr_write_b32 a126, 0\n"
"v_mfma_f32_16x16x32_bf16 v[4:7], a[220:223], v[140:143], v[4:7]\n"
"v_accvgpr_write_b32 a127, 0\n"
"s_waitcnt vmcnt(20) lgkmcnt(0)\n"
"v_mfma_f32_16x16x32_bf16 v[32:35], a[224:227], v[128:131], 0\n"
"s_barrier\n"
"v_mfma_f32_16x16x32_bf16 v[32:35], a[232:235], v[132:135], v[32:35]\n"
"ds_read_b128 a[192:195], v192 offset:16384\n"
"v_mfma_f32_16x16x32_bf16 v[32:35], a[240:243], v[136:139], v[32:35]\n"
"ds_read_b128 a[196:199], v192 offset:20480\n"
"v_mfma_f32_16x16x32_bf16 v[32:35], a[248:251], v[140:143], v[32:35]\n"
"ds_read_b128 a[200:203], v193 offset:16384\n"
"v_mfma_f32_16x16x32_bf16 v[40:43], a[248:251], v[156:159], 0\n"
"ds_read_b128 a[204:207], v193 offset:20480\n"
"v_mfma_f32_16x16x32_bf16 v[40:43], a[240:243], v[152:155], v[40:43]\n"
"ds_read_b128 a[208:211], v194 offset:16384\n"
"v_mfma_f32_16x16x32_bf16 v[40:43], a[232:235], v[148:151], v[40:43]\n"
"ds_read_b128 a[212:215], v194 offset:20480\n"
"v_max_f32 v232, v0, v1\n"
"v_max_f32 v233, v8, v9\n"
"v_mfma_f32_16x16x32_bf16 v[40:43], a[224:227], v[144:147], v[40:43]\n"
"ds_read_b128 a[216:219], v195 offset:16384\n"
"v_max_f32 v234, v16, v17\n"
"v_max_f32 v235, v24, v25\n"
"v_mfma_f32_16x16x32_bf16 v[48:51], a[224:227], v[160:163], 0\n"
"ds_read_b128 a[220:223], v195 offset:20480\n"
"v_max3_f32 v232, v2, v3, v232\n"
"v_max3_f32 v233, v10, v11, v233\n"
"v_mfma_f32_16x16x32_bf16 v[48:51], a[232:235], v[164:167], v[48:51]\n"
"v_max3_f32 v234, v18, v19, v234\n"
"v_max3_f32 v235, v26, v27, v235\n"
"v_mfma_f32_16x16x32_bf16 v[48:51], a[240:243], v[168:171], v[48:51]\n"
"v_max3_f32 v232, v4, v5, v232\n"
"v_max3_f32 v233, v12, v13, v233\n"
"v_mfma_f32_16x16x32_bf16 v[48:51], a[248:251], v[172:175], v[48:51]\n"
"v_max3_f32 v234, v20, v21, v234\n"
"v_max3_f32 v235, v28, v29, v235\n"
"v_mfma_f32_16x16x32_bf16 v[56:59], a[248:251], v[188:191], 0\n"
"v_max3_f32 v232, v6, v7, v232\n"
"v_max3_f32 v233, v14, v15, v233\n"
"v_mfma_f32_16x16x32_bf16 v[56:59], a[240:243], v[184:187], v[56:59]\n"
"v_max3_f32 v234, v22, v23, v234\n"
"v_max3_f32 v235, v30, v31, v235\n"
"v_mfma_f32_16x16x32_bf16 v[56:59], a[232:235], v[180:183], v[56:59]\n"
"v_lshrrev_b32 v236, 8, %19\n"
"v_mov_b32 v237, 1792\n"
"v_mfma_f32_16x16x32_bf16 v[56:59], a[224:227], v[176:179], v[56:59]\n"
"v_mad_u32_u24 v220, v236, v237, %17\n"
"v_xor_b32 v221, 128, v220\n"
"v_mfma_f32_16x16x32_bf16 v[60:63], a[228:231], v[176:179], 0\n"
"v_add_u32 v221, 7168, v221\n"
"v_xor_b32 v223, 128, v220\n"
"v_mfma_f32_16x16x32_bf16 v[60:63], a[236:239], v[180:183], v[60:63]\n"
"v_add_u32 v223, 21504, v223\n"
"v_add_u32 v222, 14336, v220\n"
"v_mfma_f32_16x16x32_bf16 v[60:63], a[244:247], v[184:187], v[60:63]\n"
"v_add_u32 v220, 0, v220\n"
"v_mov_b32 v218, %19\n"
"v_mfma_f32_16x16x32_bf16 v[60:63], a[252:255], v[188:191], v[60:63]\n"
"v_add_u32 v219, 262144, %19\n"
"v_mfma_f32_16x16x32_bf16 v[52:55], a[252:255], v[172:175], 0\n"
"v_mfma_f32_16x16x32_bf16 v[52:55], a[244:247], v[168:171], v[52:55]\n"
"v_mfma_f32_16x16x32_bf16 v[52:55], a[236:239], v[164:167], v[52:55]\n"
"s_add_u32 s67, %11, 65536\n"
"s_mov_b32 m0, %11\n"
"v_mfma_f32_16x16x32_bf16 v[52:55], a[228:231], v[160:163], v[52:55]\n"
"v_mfma_f32_16x16x32_bf16 v[44:47], a[228:231], v[144:147], 0\n"
"buffer_load_dwordx4 v204, %2, s67 offen offset:0 lds\n"
"v_mfma_f32_16x16x32_bf16 v[44:47], a[236:239], v[148:151], v[44:47]\n"
"v_mfma_f32_16x16x32_bf16 v[44:47], a[244:247], v[152:155], v[44:47]\n"
"buffer_load_dwordx4 v205, %2, s67 offen offset:1024 lds\n"
"v_mfma_f32_16x16x32_bf16 v[44:47], a[252:255], v[156:159], v[44:47]\n"
"v_mfma_f32_16x16x32_bf16 v[36:39], a[252:255], v[140:143], 0\n"
"buffer_load_dwordx4 v206, %2, s67 offen offset:2048 lds\n"
"v_mfma_f32_16x16x32_bf16 v[36:39], a[244:247], v[136:139], v[36:39]\n"
"v_mfma_f32_16x16x32_bf16 v[36:39], a[236:239], v[132:135], v[36:39]\n"
"buffer_load_dwordx4 v207, %2, s67 offen offset:3072 lds\n"
"v_mfma_f32_16x16x32_bf16 v[36:39], a[228:231], v[128:131], v[36:39]\n"
"s_nop 7\n"
"v_max3_f32 v232, v32, v33, v232\n"
"v_max3_f32 v233, v40, v41, v233\n"
"v_max3_f32 v234, v48, v49, v234\n"
"v_max3_f32 v235, v56, v57, v235\n"
"v_max3_f32 v232, v34, v35, v232\n"
"v_max3_f32 v233, v42, v43, v233\n"
"v_max3_f32 v234, v50, v51, v234\n"
"v_max3_f32 v235, v58, v59, v235\n"
"v_max3_f32 v232, v36, v37, v232\n"
"v_max3_f32 v233, v44, v45, v233\n"
"v_max3_f32 v234, v52, v53, v234\n"
"v_max3_f32 v235, v60, v61, v235\n"
"v_max3_f32 v232, v38, v39, v232\n"
"v_max3_f32 v233, v46, v47, v233\n"
"v_max3_f32 v234, v54, v55, v234\n"
"v_max3_f32 v235, v62, v63, v235\n"
"v_mov_b32 v236, v232\n"
"v_mov_b32 v237, v233\n"
"v_mov_b32 v238, v234\n"
"v_mov_b32 v239, v235\n"
"s_nop 1\n"
"v_permlane16_swap_b32 v232, v236\n"
"v_permlane16_swap_b32 v233, v237\n"
"v_permlane16_swap_b32 v234, v238\n"
"v_permlane16_swap_b32 v235, v239\n"
"v_max_f32 v232, v232, v236\n"
"v_max_f32 v233, v233, v237\n"
"v_max_f32 v234, v234, v238\n"
"v_max_f32 v235, v235, v239\n"
"v_mov_b32 v236, v232\n"
"v_mov_b32 v237, v233\n"
"v_mov_b32 v238, v234\n"
"v_mov_b32 v239, v235\n"
"s_nop 1\n"
"v_permlane32_swap_b32 v232, v236\n"
"v_permlane32_swap_b32 v233, v237\n"
"v_permlane32_swap_b32 v234, v238\n"
"v_permlane32_swap_b32 v235, v239\n"
"v_max_f32 v232, v232, v236\n"
"v_max_f32 v233, v233, v237\n"
"v_max_f32 v234, v234, v238\n"
"v_max_f32 v235, v235, v239\n"
"v_sub_f32 v224, 0xc2cc0000, v232\n"
"v_sub_f32 v225, 0xc2cc0000, v233\n"
"v_sub_f32 v226, 0xc2cc0000, v234\n"
"v_sub_f32 v227, 0xc2cc0000, v235\n"
"HAVE_NEGM%=:\n"
"v_lshl_add_u64 v[230:231], %20, 0, 0\n"
"global_load_dword v228, v[230:231], off\n"
"v_lshl_add_u64 v[230:231], %21, 0, v[230:231]\n"
"s_add_u32 s67, %11, 65536\n"
"s_mov_b32 s68, %11\n"
"s_mov_b32 s66, 15\n"
"v_mov_b32 v210, v224\n"
"v_mov_b32 v211, v224\n"
"v_mov_b32 v212, v224\n"
"v_mov_b32 v213, v224\n"
"v_mov_b32 v214, v225\n"
"v_mov_b32 v215, v225\n"
"v_mov_b32 v216, v225\n"
"v_mov_b32 v217, v225\n"
"v_mov_b32 v232, v226\n"
"v_mov_b32 v233, v226\n"
"v_mov_b32 v234, v226\n"
"v_mov_b32 v235, v226\n"
"v_mov_b32 v236, v227\n"
"v_mov_b32 v237, v227\n"
"v_mov_b32 v238, v227\n"
"v_mov_b32 v239, v227\n"
"v_pk_add_f32 v[0:1], v[0:1], v[210:211]\n"
"v_pk_add_f32 v[2:3], v[2:3], v[210:211]\n"
"v_pk_add_f32 v[4:5], v[4:5], v[210:211]\n"
"v_pk_add_f32 v[6:7], v[6:7], v[210:211]\n"
"v_pk_add_f32 v[8:9], v[8:9], v[214:215]\n"
"v_pk_add_f32 v[10:11], v[10:11], v[214:215]\n"
"v_pk_add_f32 v[12:13], v[12:13], v[214:215]\n"
"v_pk_add_f32 v[14:15], v[14:15], v[214:215]\n"
"v_pk_add_f32 v[16:17], v[16:17], v[232:233]\n"
"v_pk_add_f32 v[18:19], v[18:19], v[232:233]\n"
"v_pk_add_f32 v[20:21], v[20:21], v[232:233]\n"
"v_pk_add_f32 v[22:23], v[22:23], v[232:233]\n"
"v_pk_add_f32 v[24:25], v[24:25], v[236:237]\n"
"v_pk_add_f32 v[26:27], v[26:27], v[236:237]\n"
"v_pk_add_f32 v[28:29], v[28:29], v[236:237]\n"
"v_pk_add_f32 v[30:31], v[30:31], v[236:237]\n"
"v_pk_add_f32 v[32:33], v[32:33], v[210:211]\n"
"v_pk_add_f32 v[34:35], v[34:35], v[210:211]\n"
"v_pk_add_f32 v[36:37], v[36:37], v[210:211]\n"
"v_pk_add_f32 v[38:39], v[38:39], v[210:211]\n"
"v_pk_add_f32 v[40:41], v[40:41], v[214:215]\n"
"v_pk_add_f32 v[42:43], v[42:43], v[214:215]\n"
"v_pk_add_f32 v[44:45], v[44:45], v[214:215]\n"
"v_pk_add_f32 v[46:47], v[46:47], v[214:215]\n"
"v_pk_add_f32 v[48:49], v[48:49], v[232:233]\n"
"v_pk_add_f32 v[50:51], v[50:51], v[232:233]\n"
"v_pk_add_f32 v[52:53], v[52:53], v[232:233]\n"
"v_pk_add_f32 v[54:55], v[54:55], v[232:233]\n"
"v_pk_add_f32 v[56:57], v[56:57], v[236:237]\n"
"v_pk_add_f32 v[58:59], v[58:59], v[236:237]\n"
"v_pk_add_f32 v[60:61], v[60:61], v[236:237]\n"
"v_pk_add_f32 v[62:63], v[62:63], v[236:237]\n"
"v_exp_f32 v0, v0\n"
"v_exp_f32 v1, v1\n"
"v_exp_f32 v2, v2\n"
"v_exp_f32 v3, v3\n"
"v_exp_f32 v4, v4\n"
"v_exp_f32 v5, v5\n"
"v_exp_f32 v6, v6\n"
"v_exp_f32 v7, v7\n"
"v_exp_f32 v8, v8\n"
"v_exp_f32 v9, v9\n"
"v_exp_f32 v10, v10\n"
"v_exp_f32 v11, v11\n"
"v_exp_f32 v12, v12\n"
"v_exp_f32 v13, v13\n"
"v_exp_f32 v14, v14\n"
"v_exp_f32 v15, v15\n"
"v_exp_f32 v16, v16\n"
"v_exp_f32 v17, v17\n"
"v_exp_f32 v18, v18\n"
"v_exp_f32 v19, v19\n"
"v_exp_f32 v20, v20\n"
"v_exp_f32 v21, v21\n"
"v_exp_f32 v22, v22\n"
"v_exp_f32 v23, v23\n"
"v_exp_f32 v24, v24\n"
"v_exp_f32 v25, v25\n"
"v_exp_f32 v26, v26\n"
"v_exp_f32 v27, v27\n"
"v_exp_f32 v28, v28\n"
"v_exp_f32 v29, v29\n"
"v_exp_f32 v30, v30\n"
"v_exp_f32 v31, v31\n"
"v_cvt_pk_bf16_f32 v64, v0, v1\n"
"v_cvt_pk_bf16_f32 v65, v2, v3\n"
"v_cvt_pk_bf16_f32 v66, v4, v5\n"
"v_cvt_pk_bf16_f32 v67, v6, v7\n"
"v_cvt_pk_bf16_f32 v72, v8, v9\n"
"v_cvt_pk_bf16_f32 v73, v10, v11\n"
"v_cvt_pk_bf16_f32 v74, v12, v13\n"
"v_cvt_pk_bf16_f32 v75, v14, v15\n"
"v_cvt_pk_bf16_f32 v80, v16, v17\n"
"v_cvt_pk_bf16_f32 v81, v18, v19\n"
"v_cvt_pk_bf16_f32 v82, v20, v21\n"
"v_cvt_pk_bf16_f32 v83, v22, v23\n"
"v_cvt_pk_bf16_f32 v88, v24, v25\n"
"v_cvt_pk_bf16_f32 v89, v26, v27\n"
"v_cvt_pk_bf16_f32 v90, v28, v29\n"
"v_cvt_pk_bf16_f32 v91, v30, v31\n"
"s_waitcnt lgkmcnt(0)\n"
"v_mfma_f32_16x16x32_bf16 v[0:3], a[192:195], v[128:131], v[210:213]\n"
"v_exp_f32 v32, v32\n"
"v_mfma_f32_16x16x32_bf16 v[0:3], a[200:203], v[132:135], v[0:3]\n"
"v_exp_f32 v33, v33\n"
"v_mfma_f32_16x16x32_bf16 v[0:3], a[208:211], v[136:139], v[0:3]\n"
"v_exp_f32 v34, v34\n"
"v_cvt_pk_bf16_f32 v68, v32, v33\n"
"v_mfma_f32_16x16x32_bf16 v[0:3], a[216:219], v[140:143], v[0:3]\n"
"v_exp_f32 v35, v35\n"
"v_mfma_f32_16x16x32_bf16 v[8:11], a[216:219], v[156:159], v[214:217]\n"
"v_exp_f32 v36, v36\n"
"v_cvt_pk_bf16_f32 v69, v34, v35\n"
"v_mfma_f32_16x16x32_bf16 v[8:11], a[208:211], v[152:155], v[8:11]\n"
"v_exp_f32 v37, v37\n"
"v_mfma_f32_16x16x32_bf16 v[8:11], a[200:203], v[148:151], v[8:11]\n"
"v_exp_f32 v38, v38\n"
"v_cvt_pk_bf16_f32 v70, v36, v37\n"
"v_mfma_f32_16x16x32_bf16 v[8:11], a[192:195], v[144:147], v[8:11]\n"
"v_exp_f32 v39, v39\n"
"v_mfma_f32_16x16x32_bf16 v[16:19], a[192:195], v[160:163], v[232:235]\n"
"v_exp_f32 v40, v40\n"
"v_cvt_pk_bf16_f32 v71, v38, v39\n"
"v_mfma_f32_16x16x32_bf16 v[16:19], a[200:203], v[164:167], v[16:19]\n"
"v_exp_f32 v41, v41\n"
"v_mfma_f32_16x16x32_bf16 v[16:19], a[208:211], v[168:171], v[16:19]\n"
"v_exp_f32 v42, v42\n"
"v_cvt_pk_bf16_f32 v76, v40, v41\n"
"v_mfma_f32_16x16x32_bf16 v[16:19], a[216:219], v[172:175], v[16:19]\n"
"v_exp_f32 v43, v43\n"
"v_mfma_f32_16x16x32_bf16 v[24:27], a[216:219], v[188:191], v[236:239]\n"
"v_exp_f32 v44, v44\n"
"v_cvt_pk_bf16_f32 v77, v42, v43\n"
"v_mfma_f32_16x16x32_bf16 v[24:27], a[208:211], v[184:187], v[24:27]\n"
"v_exp_f32 v45, v45\n"
"v_mfma_f32_16x16x32_bf16 v[24:27], a[200:203], v[180:183], v[24:27]\n"
"v_exp_f32 v46, v46\n"
"v_cvt_pk_bf16_f32 v78, v44, v45\n"
"v_mfma_f32_16x16x32_bf16 v[24:27], a[192:195], v[176:179], v[24:27]\n"
"v_exp_f32 v47, v47\n"
"v_mfma_f32_16x16x32_bf16 v[28:31], a[196:199], v[176:179], v[236:239]\n"
"v_exp_f32 v48, v48\n"
"v_cvt_pk_bf16_f32 v79, v46, v47\n"
"v_mfma_f32_16x16x32_bf16 v[28:31], a[204:207], v[180:183], v[28:31]\n"
"v_exp_f32 v49, v49\n"
"ds_read_b128 a[192:195], v192 offset:24576\n"
"v_mfma_f32_16x16x32_bf16 v[28:31], a[212:215], v[184:187], v[28:31]\n"
"v_exp_f32 v50, v50\n"
"v_cvt_pk_bf16_f32 v84, v48, v49\n"
"v_mfma_f32_16x16x32_bf16 v[28:31], a[220:223], v[188:191], v[28:31]\n"
"v_exp_f32 v51, v51\n"
"ds_read_b128 a[200:203], v193 offset:24576\n"
"v_mfma_f32_16x16x32_bf16 v[20:23], a[220:223], v[172:175], v[232:235]\n"
"v_exp_f32 v52, v52\n"
"v_cvt_pk_bf16_f32 v85, v50, v51\n"
"v_mfma_f32_16x16x32_bf16 v[20:23], a[212:215], v[168:171], v[20:23]\n"
"v_exp_f32 v53, v53\n"
"ds_read_b128 a[208:211], v194 offset:24576\n"
"v_mfma_f32_16x16x32_bf16 v[20:23], a[204:207], v[164:167], v[20:23]\n"
"v_exp_f32 v54, v54\n"
"v_cvt_pk_bf16_f32 v86, v52, v53\n"
"v_mfma_f32_16x16x32_bf16 v[20:23], a[196:199], v[160:163], v[20:23]\n"
"v_exp_f32 v55, v55\n"
"ds_read_b128 a[216:219], v195 offset:24576\n"
"v_mfma_f32_16x16x32_bf16 v[12:15], a[196:199], v[144:147], v[214:217]\n"
"v_exp_f32 v56, v56\n"
"v_cvt_pk_bf16_f32 v87, v54, v55\n"
"v_mfma_f32_16x16x32_bf16 v[12:15], a[204:207], v[148:151], v[12:15]\n"
"v_exp_f32 v57, v57\n"
"v_mfma_f32_16x16x32_bf16 v[12:15], a[212:215], v[152:155], v[12:15]\n"
"v_exp_f32 v58, v58\n"
"v_cvt_pk_bf16_f32 v92, v56, v57\n"
"v_mfma_f32_16x16x32_bf16 v[12:15], a[220:223], v[156:159], v[12:15]\n"
"v_exp_f32 v59, v59\n"
"v_mfma_f32_16x16x32_bf16 v[4:7], a[220:223], v[140:143], v[210:213]\n"
"v_exp_f32 v60, v60\n"
"v_cvt_pk_bf16_f32 v93, v58, v59\n"
"v_mfma_f32_16x16x32_bf16 v[4:7], a[212:215], v[136:139], v[4:7]\n"
"v_exp_f32 v61, v61\n"
"v_mfma_f32_16x16x32_bf16 v[4:7], a[204:207], v[132:135], v[4:7]\n"
"v_exp_f32 v62, v62\n"
"v_cvt_pk_bf16_f32 v94, v60, v61\n"
"v_mfma_f32_16x16x32_bf16 v[4:7], a[196:199], v[128:131], v[4:7]\n"
"v_exp_f32 v63, v63\n"
"ds_read_b128 a[196:199], v192 offset:28672\n"
"ds_read_b128 a[204:207], v193 offset:28672\n"
"ds_read_b128 a[212:215], v194 offset:28672\n"
"ds_read_b128 a[220:223], v195 offset:28672\n"
"v_cvt_pk_bf16_f32 v95, v62, v63\n"
"v_xor_b32 v192, 0x8000, v192\n"
"v_xor_b32 v193, 0x8000, v193\n"
"v_xor_b32 v194, 0x8000, v194\n"
"v_xor_b32 v195, 0x8000, v195\n"
"LOOP_A%=:\n"
"s_waitcnt vmcnt(17) lgkmcnt(0)\n"
"v_mfma_f32_16x16x32_bf16 v[32:35], a[192:195], v[128:131], v[210:213]\n"
"s_barrier\n"
"v_mfma_f32_16x16x32_bf16 v[32:35], a[200:203], v[132:135], v[32:35]\n"
"ds_read_b64_tr_b16 a[224:225], v196 offset:0\n"
"ds_read_b64_tr_b16 a[226:227], v196 offset:4096\n"
"v_mfma_f32_16x16x32_bf16 v[32:35], a[208:211], v[136:139], v[32:35]\n"
"v_exp_f32 v0, v0\n"
"v_mfma_f32_16x16x32_bf16 v[32:35], a[216:219], v[140:143], v[32:35]\n"
"ds_read_b64_tr_b16 a[228:229], v196 offset:8192\n"
"ds_read_b64_tr_b16 a[230:231], v196 offset:12288\n"
"v_mfma_f32_16x16x32_bf16 v[40:43], a[216:219], v[156:159], v[214:217]\n"
"v_exp_f32 v1, v1\n"
"v_mfma_f32_16x16x32_bf16 v[40:43], a[208:211], v[152:155], v[40:43]\n"
"ds_read_b64_tr_b16 a[232:233], v197 offset:0\n"
"v_cvt_pk_bf16_f32 v96, v0, v1\n"
"v_mfma_f32_16x16x32_bf16 v[40:43], a[200:203], v[148:151], v[40:43]\n"
"v_exp_f32 v2, v2\n"
"v_mfma_f32_16x16x32_bf16 v[40:43], a[192:195], v[144:147], v[40:43]\n"
"ds_read_b64_tr_b16 a[234:235], v197 offset:4096\n"
"ds_read_b64_tr_b16 a[236:237], v197 offset:8192\n"
"v_mfma_f32_16x16x32_bf16 v[48:51], a[192:195], v[160:163], v[232:235]\n"
"v_exp_f32 v3, v3\n"
"v_mfma_f32_16x16x32_bf16 v[48:51], a[200:203], v[164:167], v[48:51]\n"
"ds_read_b64_tr_b16 a[238:239], v197 offset:12288\n"
"v_cvt_pk_bf16_f32 v97, v2, v3\n"
"v_mfma_f32_16x16x32_bf16 v[48:51], a[208:211], v[168:171], v[48:51]\n"
"v_exp_f32 v4, v4\n"
"v_mfma_f32_16x16x32_bf16 v[48:51], a[216:219], v[172:175], v[48:51]\n"
"ds_read_b64_tr_b16 a[240:241], v198 offset:0\n"
"ds_read_b64_tr_b16 a[242:243], v198 offset:4096\n"
"v_mfma_f32_16x16x32_bf16 v[56:59], a[216:219], v[188:191], v[236:239]\n"
"v_exp_f32 v5, v5\n"
"v_mfma_f32_16x16x32_bf16 v[56:59], a[208:211], v[184:187], v[56:59]\n"
"ds_read_b64_tr_b16 a[244:245], v198 offset:8192\n"
"v_cvt_pk_bf16_f32 v98, v4, v5\n"
"v_mfma_f32_16x16x32_bf16 v[56:59], a[200:203], v[180:183], v[56:59]\n"
"v_exp_f32 v6, v6\n"
"v_mfma_f32_16x16x32_bf16 v[56:59], a[192:195], v[176:179], v[56:59]\n"
"ds_read_b64_tr_b16 a[246:247], v198 offset:12288\n"
"ds_read_b64_tr_b16 a[248:249], v199 offset:0\n"
"v_mfma_f32_16x16x32_bf16 v[60:63], a[196:199], v[176:179], v[236:239]\n"
"v_exp_f32 v7, v7\n"
"v_mfma_f32_16x16x32_bf16 v[60:63], a[204:207], v[180:183], v[60:63]\n"
"ds_read_b64_tr_b16 a[250:251], v199 offset:4096\n"
"v_cvt_pk_bf16_f32 v99, v6, v7\n"
"v_mfma_f32_16x16x32_bf16 v[60:63], a[212:215], v[184:187], v[60:63]\n"
"v_exp_f32 v8, v8\n"
"v_mfma_f32_16x16x32_bf16 v[60:63], a[220:223], v[188:191], v[60:63]\n"
"ds_read_b64_tr_b16 a[252:253], v199 offset:8192\n"
"ds_read_b64_tr_b16 a[254:255], v199 offset:12288\n"
"v_mfma_f32_16x16x32_bf16 v[52:55], a[220:223], v[172:175], v[232:235]\n"
"v_exp_f32 v9, v9\n"
"v_mfma_f32_16x16x32_bf16 v[52:55], a[212:215], v[168:171], v[52:55]\n"
"v_cvt_pk_bf16_f32 v104, v8, v9\n"
"s_xor_b32 m0, s68, 0x4000\n"
"v_mfma_f32_16x16x32_bf16 v[52:55], a[204:207], v[164:167], v[52:55]\n"
"v_exp_f32 v10, v10\n"
"v_mfma_f32_16x16x32_bf16 v[52:55], a[196:199], v[160:163], v[52:55]\n"
"v_exp_f32 v11, v11\n"
"v_mfma_f32_16x16x32_bf16 v[44:47], a[196:199], v[144:147], v[214:217]\n"
"v_cvt_pk_bf16_f32 v105, v10, v11\n"
"s_add_u32 s67, s67, 16384\n"
"v_mfma_f32_16x16x32_bf16 v[44:47], a[204:207], v[148:151], v[44:47]\n"
"v_exp_f32 v12, v12\n"
"v_mfma_f32_16x16x32_bf16 v[44:47], a[212:215], v[152:155], v[44:47]\n"
"v_exp_f32 v13, v13\n"
"v_mfma_f32_16x16x32_bf16 v[44:47], a[220:223], v[156:159], v[44:47]\n"
"v_cvt_pk_bf16_f32 v106, v12, v13\n"
"v_mfma_f32_16x16x32_bf16 v[36:39], a[220:223], v[140:143], v[210:213]\n"
"v_exp_f32 v14, v14\n"
"v_mfma_f32_16x16x32_bf16 v[36:39], a[212:215], v[136:139], v[36:39]\n"
"v_exp_f32 v15, v15\n"
"v_mfma_f32_16x16x32_bf16 v[36:39], a[204:207], v[132:135], v[36:39]\n"
"v_cvt_pk_bf16_f32 v107, v14, v15\n"
"v_mfma_f32_16x16x32_bf16 v[36:39], a[196:199], v[128:131], v[36:39]\n"
"buffer_load_dwordx4 v204, %2, s67 offen offset:0 lds\n"
"v_smfmac_f32_16x16x64_bf16 a[136:139], a[144:147], v[80:87], v229\n"
"s_waitcnt lgkmcnt(0)\n"
"v_smfmac_f32_16x16x64_bf16 a[140:143], a[144:147], v[88:95], v229\n"
"buffer_load_dwordx4 v205, %2, s67 offen offset:1024 lds\n"
"v_mfma_f32_16x16x32_bf16 a[24:27], a[224:227], v[88:91], a[24:27]\n"
"v_mfma_f32_16x16x32_bf16 a[24:27], a[228:231], v[92:95], a[24:27]\n"
"v_exp_f32 v16, v16\n"
"v_mfma_f32_16x16x32_bf16 a[16:19], a[228:231], v[84:87], a[16:19]\n"
"ds_read_b128 a[192:195], v192 offset:0\n"
"ds_read_b128 a[196:199], v192 offset:4096\n"
"v_mfma_f32_16x16x32_bf16 a[16:19], a[224:227], v[80:83], a[16:19]\n"
"v_exp_f32 v17, v17\n"
"v_mfma_f32_16x16x32_bf16 a[8:11], a[224:227], v[72:75], a[8:11]\n"
"ds_read_b128 a[200:203], v193 offset:0\n"
"v_cvt_pk_bf16_f32 v112, v16, v17\n"
"v_mfma_f32_16x16x32_bf16 a[8:11], a[228:231], v[76:79], a[8:11]\n"
"v_exp_f32 v18, v18\n"
"v_mfma_f32_16x16x32_bf16 a[0:3], a[228:231], v[68:71], a[0:3]\n"
"ds_read_b128 a[204:207], v193 offset:4096\n"
"ds_read_b128 a[208:211], v194 offset:0\n"
"v_mfma_f32_16x16x32_bf16 a[0:3], a[224:227], v[64:67], a[0:3]\n"
"v_exp_f32 v19, v19\n"
"v_mfma_f32_16x16x32_bf16 a[4:7], a[232:235], v[64:67], a[4:7]\n"
"ds_read_b128 a[212:215], v194 offset:4096\n"
"v_cvt_pk_bf16_f32 v113, v18, v19\n"
"v_mfma_f32_16x16x32_bf16 a[4:7], a[236:239], v[68:71], a[4:7]\n"
"v_exp_f32 v20, v20\n"
"v_mfma_f32_16x16x32_bf16 a[12:15], a[236:239], v[76:79], a[12:15]\n"
"ds_read_b128 a[216:219], v195 offset:0\n"
"v_mfma_f32_16x16x32_bf16 a[12:15], a[232:235], v[72:75], a[12:15]\n"
"v_exp_f32 v21, v21\n"
"v_mfma_f32_16x16x32_bf16 a[20:23], a[232:235], v[80:83], a[20:23]\n"
"ds_read_b128 a[220:223], v195 offset:4096\n"
"v_cvt_pk_bf16_f32 v114, v20, v21\n"
"v_mfma_f32_16x16x32_bf16 a[20:23], a[236:239], v[84:87], a[20:23]\n"
"v_exp_f32 v22, v22\n"
"v_mfma_f32_16x16x32_bf16 a[28:31], a[236:239], v[92:95], a[28:31]\n"
"v_exp_f32 v23, v23\n"
"v_mfma_f32_16x16x32_bf16 a[28:31], a[232:235], v[88:91], a[28:31]\n"
"v_cvt_pk_bf16_f32 v115, v22, v23\n"
"v_mfma_f32_16x16x32_bf16 a[56:59], a[240:243], v[88:91], a[56:59]\n"
"v_exp_f32 v24, v24\n"
"v_mfma_f32_16x16x32_bf16 a[56:59], a[244:247], v[92:95], a[56:59]\n"
"v_exp_f32 v25, v25\n"
"v_mfma_f32_16x16x32_bf16 a[48:51], a[244:247], v[84:87], a[48:51]\n"
"v_cvt_pk_bf16_f32 v120, v24, v25\n"
"v_mfma_f32_16x16x32_bf16 a[48:51], a[240:243], v[80:83], a[48:51]\n"
"v_exp_f32 v26, v26\n"
"v_mfma_f32_16x16x32_bf16 a[40:43], a[240:243], v[72:75], a[40:43]\n"
"v_exp_f32 v27, v27\n"
"v_mfma_f32_16x16x32_bf16 a[40:43], a[244:247], v[76:79], a[40:43]\n"
"v_cvt_pk_bf16_f32 v121, v26, v27\n"
"v_mfma_f32_16x16x32_bf16 a[32:35], a[244:247], v[68:71], a[32:35]\n"
"v_exp_f32 v28, v28\n"
"v_mfma_f32_16x16x32_bf16 a[32:35], a[240:243], v[64:67], a[32:35]\n"
"v_exp_f32 v29, v29\n"
"v_mfma_f32_16x16x32_bf16 a[36:39], a[248:251], v[64:67], a[36:39]\n"
"v_cvt_pk_bf16_f32 v122, v28, v29\n"
"v_mfma_f32_16x16x32_bf16 a[36:39], a[252:255], v[68:71], a[36:39]\n"
"v_exp_f32 v30, v30\n"
"v_mfma_f32_16x16x32_bf16 a[44:47], a[252:255], v[76:79], a[44:47]\n"
"v_exp_f32 v31, v31\n"
"v_mfma_f32_16x16x32_bf16 a[44:47], a[248:251], v[72:75], a[44:47]\n"
"v_cvt_pk_bf16_f32 v123, v30, v31\n"
"v_mfma_f32_16x16x32_bf16 a[52:55], a[248:251], v[80:83], a[52:55]\n"
"buffer_load_dwordx4 v206, %2, s67 offen offset:2048 lds\n"
"v_mfma_f32_16x16x32_bf16 a[52:55], a[252:255], v[84:87], a[52:55]\n"
"v_mfma_f32_16x16x32_bf16 a[60:63], a[252:255], v[92:95], a[60:63]\n"
"buffer_load_dwordx4 v207, %2, s67 offen offset:3072 lds\n"
"v_mfma_f32_16x16x32_bf16 a[60:63], a[248:251], v[88:91], a[60:63]\n"
"s_waitcnt lgkmcnt(0)\n"
"v_mfma_f32_16x16x32_bf16 v[0:3], a[192:195], v[128:131], v[210:213]\n"
"global_load_dword v228, v[230:231], off\n"
"v_mfma_f32_16x16x32_bf16 v[0:3], a[200:203], v[132:135], v[0:3]\n"
"ds_read_b64_tr_b16 a[224:225], v200 offset:0\n"
"ds_read_b64_tr_b16 a[226:227], v200 offset:4096\n"
"v_mfma_f32_16x16x32_bf16 v[0:3], a[208:211], v[136:139], v[0:3]\n"
"v_exp_f32 v32, v32\n"
"v_mfma_f32_16x16x32_bf16 v[0:3], a[216:219], v[140:143], v[0:3]\n"
"ds_read_b64_tr_b16 a[228:229], v200 offset:8192\n"
"ds_read_b64_tr_b16 a[230:231], v200 offset:12288\n"
"v_mfma_f32_16x16x32_bf16 v[8:11], a[216:219], v[156:159], v[214:217]\n"
"v_exp_f32 v33, v33\n"
"v_mfma_f32_16x16x32_bf16 v[8:11], a[208:211], v[152:155], v[8:11]\n"
"ds_read_b64_tr_b16 a[232:233], v201 offset:0\n"
"v_cvt_pk_bf16_f32 v100, v32, v33\n"
"v_mfma_f32_16x16x32_bf16 v[8:11], a[200:203], v[148:151], v[8:11]\n"
"v_exp_f32 v34, v34\n"
"v_mfma_f32_16x16x32_bf16 v[8:11], a[192:195], v[144:147], v[8:11]\n"
"ds_read_b64_tr_b16 a[234:235], v201 offset:4096\n"
"ds_read_b64_tr_b16 a[236:237], v201 offset:8192\n"
"v_mfma_f32_16x16x32_bf16 v[16:19], a[192:195], v[160:163], v[232:235]\n"
"v_exp_f32 v35, v35\n"
"v_mfma_f32_16x16x32_bf16 v[16:19], a[200:203], v[164:167], v[16:19]\n"
"ds_read_b64_tr_b16 a[238:239], v201 offset:12288\n"
"v_cvt_pk_bf16_f32 v101, v34, v35\n"
"v_mfma_f32_16x16x32_bf16 v[16:19], a[208:211], v[168:171], v[16:19]\n"
"v_exp_f32 v36, v36\n"
"v_mfma_f32_16x16x32_bf16 v[16:19], a[216:219], v[172:175], v[16:19]\n"
"ds_read_b64_tr_b16 a[240:241], v202 offset:0\n"
"ds_read_b64_tr_b16 a[242:243], v202 offset:4096\n"
"v_mfma_f32_16x16x32_bf16 v[24:27], a[216:219], v[188:191], v[236:239]\n"
"v_exp_f32 v37, v37\n"
"v_mfma_f32_16x16x32_bf16 v[24:27], a[208:211], v[184:187], v[24:27]\n"
"ds_read_b64_tr_b16 a[244:245], v202 offset:8192\n"
"v_cvt_pk_bf16_f32 v102, v36, v37\n"
"v_mfma_f32_16x16x32_bf16 v[24:27], a[200:203], v[180:183], v[24:27]\n"
"v_exp_f32 v38, v38\n"
"v_mfma_f32_16x16x32_bf16 v[24:27], a[192:195], v[176:179], v[24:27]\n"
"ds_read_b64_tr_b16 a[246:247], v202 offset:12288\n"
"ds_read_b64_tr_b16 a[248:249], v203 offset:0\n"
"v_mfma_f32_16x16x32_bf16 v[28:31], a[196:199], v[176:179], v[236:239]\n"
"v_exp_f32 v39, v39\n"
"v_mfma_f32_16x16x32_bf16 v[28:31], a[204:207], v[180:183], v[28:31]\n"
"ds_read_b64_tr_b16 a[250:251], v203 offset:4096\n"
"v_cvt_pk_bf16_f32 v103, v38, v39\n"
"v_mfma_f32_16x16x32_bf16 v[28:31], a[212:215], v[184:187], v[28:31]\n"
"v_exp_f32 v40, v40\n"
"v_mfma_f32_16x16x32_bf16 v[28:31], a[220:223], v[188:191], v[28:31]\n"
"ds_read_b64_tr_b16 a[252:253], v203 offset:8192\n"
"ds_read_b64_tr_b16 a[254:255], v203 offset:12288\n"
"v_mfma_f32_16x16x32_bf16 v[20:23], a[220:223], v[172:175], v[232:235]\n"
"v_exp_f32 v41, v41\n"
"v_mfma_f32_16x16x32_bf16 v[20:23], a[212:215], v[168:171], v[20:23]\n"
"v_cvt_pk_bf16_f32 v108, v40, v41\n"
"s_xor_b32 m0, s68, 0x1c000\n"
"v_mfma_f32_16x16x32_bf16 v[20:23], a[204:207], v[164:167], v[20:23]\n"
"v_exp_f32 v42, v42\n"
"v_mfma_f32_16x16x32_bf16 v[20:23], a[196:199], v[160:163], v[20:23]\n"
"v_exp_f32 v43, v43\n"
"v_mfma_f32_16x16x32_bf16 v[12:15], a[196:199], v[144:147], v[214:217]\n"
"v_cvt_pk_bf16_f32 v109, v42, v43\n"
"v_lshl_add_u64 v[230:231], %21, 0, v[230:231]\n"
"v_mfma_f32_16x16x32_bf16 v[12:15], a[204:207], v[148:151], v[12:15]\n"
"v_exp_f32 v44, v44\n"
"v_mfma_f32_16x16x32_bf16 v[12:15], a[212:215], v[152:155], v[12:15]\n"
"v_exp_f32 v45, v45\n"
"v_mfma_f32_16x16x32_bf16 v[12:15], a[220:223], v[156:159], v[12:15]\n"
"v_cvt_pk_bf16_f32 v110, v44, v45\n"
"v_mfma_f32_16x16x32_bf16 v[4:7], a[220:223], v[140:143], v[210:213]\n"
"v_exp_f32 v46, v46\n"
"v_mfma_f32_16x16x32_bf16 v[4:7], a[212:215], v[136:139], v[4:7]\n"
"v_exp_f32 v47, v47\n"
"v_mfma_f32_16x16x32_bf16 v[4:7], a[204:207], v[132:135], v[4:7]\n"
"v_cvt_pk_bf16_f32 v111, v46, v47\n"
"v_mfma_f32_16x16x32_bf16 v[4:7], a[196:199], v[128:131], v[4:7]\n"
"buffer_load_dwordx4 v208, %3, s67 offen offset:0 lds\n"
"v_smfmac_f32_16x16x64_bf16 a[132:135], a[144:147], v[72:79], v229\n"
"s_waitcnt lgkmcnt(0)\n"
"v_smfmac_f32_16x16x64_bf16 a[128:131], a[144:147], v[64:71], v229\n"
"buffer_load_dwordx4 v209, %3, s67 offen offset:1024 lds\n"
"v_mfma_f32_16x16x32_bf16 a[64:67], a[224:227], v[64:67], a[64:67]\n"
"v_mfma_f32_16x16x32_bf16 a[64:67], a[228:231], v[68:71], a[64:67]\n"
"v_exp_f32 v48, v48\n"
"v_mfma_f32_16x16x32_bf16 a[72:75], a[228:231], v[76:79], a[72:75]\n"
"ds_read_b128 a[192:195], v192 offset:8192\n"
"ds_read_b128 a[196:199], v192 offset:12288\n"
"v_mfma_f32_16x16x32_bf16 a[72:75], a[224:227], v[72:75], a[72:75]\n"
"v_exp_f32 v49, v49\n"
"v_mfma_f32_16x16x32_bf16 a[80:83], a[224:227], v[80:83], a[80:83]\n"
"ds_read_b128 a[200:203], v193 offset:8192\n"
"v_cvt_pk_bf16_f32 v116, v48, v49\n"
"v_mfma_f32_16x16x32_bf16 a[80:83], a[228:231], v[84:87], a[80:83]\n"
"v_exp_f32 v50, v50\n"
"v_mfma_f32_16x16x32_bf16 a[88:91], a[228:231], v[92:95], a[88:91]\n"
"ds_read_b128 a[204:207], v193 offset:12288\n"
"ds_read_b128 a[208:211], v194 offset:8192\n"
"v_mfma_f32_16x16x32_bf16 a[88:91], a[224:227], v[88:91], a[88:91]\n"
"v_exp_f32 v51, v51\n"
"v_mfma_f32_16x16x32_bf16 a[92:95], a[232:235], v[88:91], a[92:95]\n"
"ds_read_b128 a[212:215], v194 offset:12288\n"
"v_cvt_pk_bf16_f32 v117, v50, v51\n"
"v_mfma_f32_16x16x32_bf16 a[92:95], a[236:239], v[92:95], a[92:95]\n"
"v_exp_f32 v52, v52\n"
"v_mfma_f32_16x16x32_bf16 a[84:87], a[236:239], v[84:87], a[84:87]\n"
"ds_read_b128 a[216:219], v195 offset:8192\n"
"v_mfma_f32_16x16x32_bf16 a[84:87], a[232:235], v[80:83], a[84:87]\n"
"v_exp_f32 v53, v53\n"
"v_mfma_f32_16x16x32_bf16 a[76:79], a[232:235], v[72:75], a[76:79]\n"
"ds_read_b128 a[220:223], v195 offset:12288\n"
"v_cvt_pk_bf16_f32 v118, v52, v53\n"
"v_mfma_f32_16x16x32_bf16 a[76:79], a[236:239], v[76:79], a[76:79]\n"
"v_exp_f32 v54, v54\n"
"v_mfma_f32_16x16x32_bf16 a[68:71], a[236:239], v[68:71], a[68:71]\n"
"v_exp_f32 v55, v55\n"
"v_mfma_f32_16x16x32_bf16 a[68:71], a[232:235], v[64:67], a[68:71]\n"
"v_cvt_pk_bf16_f32 v119, v54, v55\n"
"v_mfma_f32_16x16x32_bf16 a[96:99], a[240:243], v[64:67], a[96:99]\n"
"v_exp_f32 v56, v56\n"
"v_mfma_f32_16x16x32_bf16 a[96:99], a[244:247], v[68:71], a[96:99]\n"
"v_exp_f32 v57, v57\n"
"v_mfma_f32_16x16x32_bf16 a[104:107], a[244:247], v[76:79], a[104:107]\n"
"v_cvt_pk_bf16_f32 v124, v56, v57\n"
"v_mfma_f32_16x16x32_bf16 a[104:107], a[240:243], v[72:75], a[104:107]\n"
"v_exp_f32 v58, v58\n"
"v_mfma_f32_16x16x32_bf16 a[112:115], a[240:243], v[80:83], a[112:115]\n"
"v_exp_f32 v59, v59\n"
"v_mfma_f32_16x16x32_bf16 a[112:115], a[244:247], v[84:87], a[112:115]\n"
"v_cvt_pk_bf16_f32 v125, v58, v59\n"
"v_mfma_f32_16x16x32_bf16 a[120:123], a[244:247], v[92:95], a[120:123]\n"
"v_exp_f32 v60, v60\n"
"v_mfma_f32_16x16x32_bf16 a[120:123], a[240:243], v[88:91], a[120:123]\n"
"v_exp_f32 v61, v61\n"
"v_mfma_f32_16x16x32_bf16 a[124:127], a[248:251], v[88:91], a[124:127]\n"
"v_cvt_pk_bf16_f32 v126, v60, v61\n"
"v_mfma_f32_16x16x32_bf16 a[124:127], a[252:255], v[92:95], a[124:127]\n"
"v_exp_f32 v62, v62\n"
"v_mfma_f32_16x16x32_bf16 a[116:119], a[252:255], v[84:87], a[116:119]\n"
"v_exp_f32 v63, v63\n"
"v_mfma_f32_16x16x32_bf16 a[116:119], a[248:251], v[80:83], a[116:119]\n"
"v_cvt_pk_bf16_f32 v127, v62, v63\n"
"v_mfma_f32_16x16x32_bf16 a[108:111], a[248:251], v[72:75], a[108:111]\n"
"buffer_load_dwordx4 v208, %3, s67 offen offset:2048 lds\n"
"v_mfma_f32_16x16x32_bf16 a[108:111], a[252:255], v[76:79], a[108:111]\n"
"v_mfma_f32_16x16x32_bf16 a[100:103], a[252:255], v[68:71], a[100:103]\n"
"buffer_load_dwordx4 v209, %3, s67 offen offset:3072 lds\n"
"v_mfma_f32_16x16x32_bf16 a[100:103], a[248:251], v[64:67], a[100:103]\n"
"s_waitcnt vmcnt(17) lgkmcnt(0)\n"
"v_mfma_f32_16x16x32_bf16 v[32:35], a[192:195], v[128:131], v[210:213]\n"
"s_barrier\n"
"v_mfma_f32_16x16x32_bf16 v[32:35], a[200:203], v[132:135], v[32:35]\n"
"ds_read_b64_tr_b16 a[224:225], v196 offset:16384\n"
"ds_read_b64_tr_b16 a[226:227], v196 offset:20480\n"
"v_mfma_f32_16x16x32_bf16 v[32:35], a[208:211], v[136:139], v[32:35]\n"
"v_exp_f32 v0, v0\n"
"v_mfma_f32_16x16x32_bf16 v[32:35], a[216:219], v[140:143], v[32:35]\n"
"ds_read_b64_tr_b16 a[228:229], v196 offset:24576\n"
"ds_read_b64_tr_b16 a[230:231], v196 offset:28672\n"
"v_mfma_f32_16x16x32_bf16 v[40:43], a[216:219], v[156:159], v[214:217]\n"
"v_exp_f32 v1, v1\n"
"v_mfma_f32_16x16x32_bf16 v[40:43], a[208:211], v[152:155], v[40:43]\n"
"ds_read_b64_tr_b16 a[232:233], v197 offset:16384\n"
"v_cvt_pk_bf16_f32 v64, v0, v1\n"
"v_mfma_f32_16x16x32_bf16 v[40:43], a[200:203], v[148:151], v[40:43]\n"
"v_exp_f32 v2, v2\n"
"v_mfma_f32_16x16x32_bf16 v[40:43], a[192:195], v[144:147], v[40:43]\n"
"ds_read_b64_tr_b16 a[234:235], v197 offset:20480\n"
"ds_read_b64_tr_b16 a[236:237], v197 offset:24576\n"
"v_mfma_f32_16x16x32_bf16 v[48:51], a[192:195], v[160:163], v[232:235]\n"
"v_exp_f32 v3, v3\n"
"v_mfma_f32_16x16x32_bf16 v[48:51], a[200:203], v[164:167], v[48:51]\n"
"ds_read_b64_tr_b16 a[238:239], v197 offset:28672\n"
"v_cvt_pk_bf16_f32 v65, v2, v3\n"
"v_mfma_f32_16x16x32_bf16 v[48:51], a[208:211], v[168:171], v[48:51]\n"
"v_exp_f32 v4, v4\n"
"v_mfma_f32_16x16x32_bf16 v[48:51], a[216:219], v[172:175], v[48:51]\n"
"ds_read_b64_tr_b16 a[240:241], v198 offset:16384\n"
"ds_read_b64_tr_b16 a[242:243], v198 offset:20480\n"
"v_mfma_f32_16x16x32_bf16 v[56:59], a[216:219], v[188:191], v[236:239]\n"
"v_exp_f32 v5, v5\n"
"v_mfma_f32_16x16x32_bf16 v[56:59], a[208:211], v[184:187], v[56:59]\n"
"ds_read_b64_tr_b16 a[244:245], v198 offset:24576\n"
"v_cvt_pk_bf16_f32 v66, v4, v5\n"
"v_mfma_f32_16x16x32_bf16 v[56:59], a[200:203], v[180:183], v[56:59]\n"
"v_exp_f32 v6, v6\n"
"v_mfma_f32_16x16x32_bf16 v[56:59], a[192:195], v[176:179], v[56:59]\n"
"ds_read_b64_tr_b16 a[246:247], v198 offset:28672\n"
"ds_read_b64_tr_b16 a[248:249], v199 offset:16384\n"
"v_mfma_f32_16x16x32_bf16 v[60:63], a[196:199], v[176:179], v[236:239]\n"
"v_exp_f32 v7, v7\n"
"v_mfma_f32_16x16x32_bf16 v[60:63], a[204:207], v[180:183], v[60:63]\n"
"ds_read_b64_tr_b16 a[250:251], v199 offset:20480\n"
"v_cvt_pk_bf16_f32 v67, v6, v7\n"
"v_mfma_f32_16x16x32_bf16 v[60:63], a[212:215], v[184:187], v[60:63]\n"
"v_exp_f32 v8, v8\n"
"v_mfma_f32_16x16x32_bf16 v[60:63], a[220:223], v[188:191], v[60:63]\n"
"ds_read_b64_tr_b16 a[252:253], v199 offset:24576\n"
"ds_read_b64_tr_b16 a[254:255], v199 offset:28672\n"
"v_mfma_f32_16x16x32_bf16 v[52:55], a[220:223], v[172:175], v[232:235]\n"
"v_exp_f32 v9, v9\n"
"v_mfma_f32_16x16x32_bf16 v[52:55], a[212:215], v[168:171], v[52:55]\n"
"v_cvt_pk_bf16_f32 v72, v8, v9\n"
"s_xor_b32 m0, s68, 0x8000\n"
"v_mfma_f32_16x16x32_bf16 v[52:55], a[204:207], v[164:167], v[52:55]\n"
"v_exp_f32 v10, v10\n"
"v_mfma_f32_16x16x32_bf16 v[52:55], a[196:199], v[160:163], v[52:55]\n"
"v_exp_f32 v11, v11\n"
"v_mfma_f32_16x16x32_bf16 v[44:47], a[196:199], v[144:147], v[214:217]\n"
"v_cvt_pk_bf16_f32 v73, v10, v11\n"
"s_add_u32 s67, s67, 16384\n"
"v_mfma_f32_16x16x32_bf16 v[44:47], a[204:207], v[148:151], v[44:47]\n"
"v_exp_f32 v12, v12\n"
"v_mfma_f32_16x16x32_bf16 v[44:47], a[212:215], v[152:155], v[44:47]\n"
"v_exp_f32 v13, v13\n"
"v_mfma_f32_16x16x32_bf16 v[44:47], a[220:223], v[156:159], v[44:47]\n"
"v_cvt_pk_bf16_f32 v74, v12, v13\n"
"v_mfma_f32_16x16x32_bf16 v[36:39], a[220:223], v[140:143], v[210:213]\n"
"v_exp_f32 v14, v14\n"
"v_mfma_f32_16x16x32_bf16 v[36:39], a[212:215], v[136:139], v[36:39]\n"
"v_exp_f32 v15, v15\n"
"v_mfma_f32_16x16x32_bf16 v[36:39], a[204:207], v[132:135], v[36:39]\n"
"v_cvt_pk_bf16_f32 v75, v14, v15\n"
"v_mfma_f32_16x16x32_bf16 v[36:39], a[196:199], v[128:131], v[36:39]\n"
"buffer_load_dwordx4 v204, %2, s67 offen offset:0 lds\n"
"v_smfmac_f32_16x16x64_bf16 a[136:139], a[144:147], v[112:119], v229\n"
"s_waitcnt lgkmcnt(0)\n"
"v_smfmac_f32_16x16x64_bf16 a[140:143], a[144:147], v[120:127], v229\n"
"buffer_load_dwordx4 v205, %2, s67 offen offset:1024 lds\n"
"v_mfma_f32_16x16x32_bf16 a[24:27], a[224:227], v[120:123], a[24:27]\n"
"v_mfma_f32_16x16x32_bf16 a[24:27], a[228:231], v[124:127], a[24:27]\n"
"v_exp_f32 v16, v16\n"
"v_mfma_f32_16x16x32_bf16 a[16:19], a[228:231], v[116:119], a[16:19]\n"
"ds_read_b128 a[192:195], v192 offset:16384\n"
"ds_read_b128 a[196:199], v192 offset:20480\n"
"v_mfma_f32_16x16x32_bf16 a[16:19], a[224:227], v[112:115], a[16:19]\n"
"v_exp_f32 v17, v17\n"
"v_mfma_f32_16x16x32_bf16 a[8:11], a[224:227], v[104:107], a[8:11]\n"
"ds_read_b128 a[200:203], v193 offset:16384\n"
"v_cvt_pk_bf16_f32 v80, v16, v17\n"
"v_mfma_f32_16x16x32_bf16 a[8:11], a[228:231], v[108:111], a[8:11]\n"
"v_exp_f32 v18, v18\n"
"v_mfma_f32_16x16x32_bf16 a[0:3], a[228:231], v[100:103], a[0:3]\n"
"ds_read_b128 a[204:207], v193 offset:20480\n"
"ds_read_b128 a[208:211], v194 offset:16384\n"
"v_mfma_f32_16x16x32_bf16 a[0:3], a[224:227], v[96:99], a[0:3]\n"
"v_exp_f32 v19, v19\n"
"v_mfma_f32_16x16x32_bf16 a[4:7], a[232:235], v[96:99], a[4:7]\n"
"ds_read_b128 a[212:215], v194 offset:20480\n"
"v_cvt_pk_bf16_f32 v81, v18, v19\n"
"v_mfma_f32_16x16x32_bf16 a[4:7], a[236:239], v[100:103], a[4:7]\n"
"v_exp_f32 v20, v20\n"
"v_mfma_f32_16x16x32_bf16 a[12:15], a[236:239], v[108:111], a[12:15]\n"
"ds_read_b128 a[216:219], v195 offset:16384\n"
"v_mfma_f32_16x16x32_bf16 a[12:15], a[232:235], v[104:107], a[12:15]\n"
"v_exp_f32 v21, v21\n"
"v_mfma_f32_16x16x32_bf16 a[20:23], a[232:235], v[112:115], a[20:23]\n"
"ds_read_b128 a[220:223], v195 offset:20480\n"
"v_cvt_pk_bf16_f32 v82, v20, v21\n"
"v_mfma_f32_16x16x32_bf16 a[20:23], a[236:239], v[116:119], a[20:23]\n"
"v_exp_f32 v22, v22\n"
"v_mfma_f32_16x16x32_bf16 a[28:31], a[236:239], v[124:127], a[28:31]\n"
"v_exp_f32 v23, v23\n"
"v_mfma_f32_16x16x32_bf16 a[28:31], a[232:235], v[120:123], a[28:31]\n"
"v_cvt_pk_bf16_f32 v83, v22, v23\n"
"v_mfma_f32_16x16x32_bf16 a[56:59], a[240:243], v[120:123], a[56:59]\n"
"v_exp_f32 v24, v24\n"
"v_mfma_f32_16x16x32_bf16 a[56:59], a[244:247], v[124:127], a[56:59]\n"
"v_exp_f32 v25, v25\n"
"v_mfma_f32_16x16x32_bf16 a[48:51], a[244:247], v[116:119], a[48:51]\n"
"v_cvt_pk_bf16_f32 v88, v24, v25\n"
"v_mfma_f32_16x16x32_bf16 a[48:51], a[240:243], v[112:115], a[48:51]\n"
"v_exp_f32 v26, v26\n"
"v_mfma_f32_16x16x32_bf16 a[40:43], a[240:243], v[104:107], a[40:43]\n"
"v_exp_f32 v27, v27\n"
"v_mfma_f32_16x16x32_bf16 a[40:43], a[244:247], v[108:111], a[40:43]\n"
"v_cvt_pk_bf16_f32 v89, v26, v27\n"
"v_mfma_f32_16x16x32_bf16 a[32:35], a[244:247], v[100:103], a[32:35]\n"
"v_exp_f32 v28, v28\n"
"v_mfma_f32_16x16x32_bf16 a[32:35], a[240:243], v[96:99], a[32:35]\n"
"v_exp_f32 v29, v29\n"
"v_mfma_f32_16x16x32_bf16 a[36:39], a[248:251], v[96:99], a[36:39]\n"
"v_cvt_pk_bf16_f32 v90, v28, v29\n"
"v_mfma_f32_16x16x32_bf16 a[36:39], a[252:255], v[100:103], a[36:39]\n"
"v_exp_f32 v30, v30\n"
"v_mfma_f32_16x16x32_bf16 a[44:47], a[252:255], v[108:111], a[44:47]\n"
"v_exp_f32 v31, v31\n"
"v_mfma_f32_16x16x32_bf16 a[44:47], a[248:251], v[104:107], a[44:47]\n"
"v_cvt_pk_bf16_f32 v91, v30, v31\n"
"v_mfma_f32_16x16x32_bf16 a[52:55], a[248:251], v[112:115], a[52:55]\n"
"buffer_load_dwordx4 v206, %2, s67 offen offset:2048 lds\n"
"v_mfma_f32_16x16x32_bf16 a[52:55], a[252:255], v[116:119], a[52:55]\n"
"v_mfma_f32_16x16x32_bf16 a[60:63], a[252:255], v[124:127], a[60:63]\n"
"buffer_load_dwordx4 v207, %2, s67 offen offset:3072 lds\n"
"v_mfma_f32_16x16x32_bf16 a[60:63], a[248:251], v[120:123], a[60:63]\n"
"s_waitcnt lgkmcnt(0)\n"
"v_mfma_f32_16x16x32_bf16 v[0:3], a[192:195], v[128:131], v[210:213]\n"
"s_sub_u32 s66, s66, 1\n"
"v_mfma_f32_16x16x32_bf16 v[0:3], a[200:203], v[132:135], v[0:3]\n"
"ds_read_b64_tr_b16 a[224:225], v200 offset:16384\n"
"ds_read_b64_tr_b16 a[226:227], v200 offset:20480\n"
"v_mfma_f32_16x16x32_bf16 v[0:3], a[208:211], v[136:139], v[0:3]\n"
"v_exp_f32 v32, v32\n"
"v_mfma_f32_16x16x32_bf16 v[0:3], a[216:219], v[140:143], v[0:3]\n"
"ds_read_b64_tr_b16 a[228:229], v200 offset:24576\n"
"ds_read_b64_tr_b16 a[230:231], v200 offset:28672\n"
"v_mfma_f32_16x16x32_bf16 v[8:11], a[216:219], v[156:159], v[214:217]\n"
"v_exp_f32 v33, v33\n"
"v_mfma_f32_16x16x32_bf16 v[8:11], a[208:211], v[152:155], v[8:11]\n"
"ds_read_b64_tr_b16 a[232:233], v201 offset:16384\n"
"v_cvt_pk_bf16_f32 v68, v32, v33\n"
"v_mfma_f32_16x16x32_bf16 v[8:11], a[200:203], v[148:151], v[8:11]\n"
"v_exp_f32 v34, v34\n"
"v_mfma_f32_16x16x32_bf16 v[8:11], a[192:195], v[144:147], v[8:11]\n"
"ds_read_b64_tr_b16 a[234:235], v201 offset:20480\n"
"ds_read_b64_tr_b16 a[236:237], v201 offset:24576\n"
"v_mfma_f32_16x16x32_bf16 v[16:19], a[192:195], v[160:163], v[232:235]\n"
"v_exp_f32 v35, v35\n"
"v_mfma_f32_16x16x32_bf16 v[16:19], a[200:203], v[164:167], v[16:19]\n"
"ds_read_b64_tr_b16 a[238:239], v201 offset:28672\n"
"v_cvt_pk_bf16_f32 v69, v34, v35\n"
"v_mfma_f32_16x16x32_bf16 v[16:19], a[208:211], v[168:171], v[16:19]\n"
"v_exp_f32 v36, v36\n"
"v_mfma_f32_16x16x32_bf16 v[16:19], a[216:219], v[172:175], v[16:19]\n"
"ds_read_b64_tr_b16 a[240:241], v202 offset:16384\n"
"ds_read_b64_tr_b16 a[242:243], v202 offset:20480\n"
"v_mfma_f32_16x16x32_bf16 v[24:27], a[216:219], v[188:191], v[236:239]\n"
"v_exp_f32 v37, v37\n"
"v_mfma_f32_16x16x32_bf16 v[24:27], a[208:211], v[184:187], v[24:27]\n"
"ds_read_b64_tr_b16 a[244:245], v202 offset:24576\n"
"v_cvt_pk_bf16_f32 v70, v36, v37\n"
"v_mfma_f32_16x16x32_bf16 v[24:27], a[200:203], v[180:183], v[24:27]\n"
"v_exp_f32 v38, v38\n"
"v_mfma_f32_16x16x32_bf16 v[24:27], a[192:195], v[176:179], v[24:27]\n"
"ds_read_b64_tr_b16 a[246:247], v202 offset:28672\n"
"ds_read_b64_tr_b16 a[248:249], v203 offset:16384\n"
"v_mfma_f32_16x16x32_bf16 v[28:31], a[196:199], v[176:179], v[236:239]\n"
"v_exp_f32 v39, v39\n"
"v_mfma_f32_16x16x32_bf16 v[28:31], a[204:207], v[180:183], v[28:31]\n"
"ds_read_b64_tr_b16 a[250:251], v203 offset:20480\n"
"v_cvt_pk_bf16_f32 v71, v38, v39\n"
"v_mfma_f32_16x16x32_bf16 v[28:31], a[212:215], v[184:187], v[28:31]\n"
"v_exp_f32 v40, v40\n"
"v_mfma_f32_16x16x32_bf16 v[28:31], a[220:223], v[188:191], v[28:31]\n"
"ds_read_b64_tr_b16 a[252:253], v203 offset:24576\n"
"ds_read_b64_tr_b16 a[254:255], v203 offset:28672\n"
"v_mfma_f32_16x16x32_bf16 v[20:23], a[220:223], v[172:175], v[232:235]\n"
"v_exp_f32 v41, v41\n"
"v_mfma_f32_16x16x32_bf16 v[20:23], a[212:215], v[168:171], v[20:23]\n"
"v_cvt_pk_bf16_f32 v76, v40, v41\n"
"s_xor_b32 m0, s68, 0x10000\n"
"v_mfma_f32_16x16x32_bf16 v[20:23], a[204:207], v[164:167], v[20:23]\n"
"v_exp_f32 v42, v42\n"
"v_mfma_f32_16x16x32_bf16 v[20:23], a[196:199], v[160:163], v[20:23]\n"
"v_exp_f32 v43, v43\n"
"v_mfma_f32_16x16x32_bf16 v[12:15], a[196:199], v[144:147], v[214:217]\n"
"v_cvt_pk_bf16_f32 v77, v42, v43\n"
"v_xor_b32 v196, 0x8000, v196\n"
"v_mfma_f32_16x16x32_bf16 v[12:15], a[204:207], v[148:151], v[12:15]\n"
"v_exp_f32 v44, v44\n"
"v_mfma_f32_16x16x32_bf16 v[12:15], a[212:215], v[152:155], v[12:15]\n"
"v_exp_f32 v45, v45\n"
"v_mfma_f32_16x16x32_bf16 v[12:15], a[220:223], v[156:159], v[12:15]\n"
"v_cvt_pk_bf16_f32 v78, v44, v45\n"
"v_xor_b32 v197, 0x8000, v197\n"
"v_mfma_f32_16x16x32_bf16 v[4:7], a[220:223], v[140:143], v[210:213]\n"
"v_exp_f32 v46, v46\n"
"v_mfma_f32_16x16x32_bf16 v[4:7], a[212:215], v[136:139], v[4:7]\n"
"v_exp_f32 v47, v47\n"
"v_mfma_f32_16x16x32_bf16 v[4:7], a[204:207], v[132:135], v[4:7]\n"
"v_cvt_pk_bf16_f32 v79, v46, v47\n"
"v_xor_b32 v198, 0x8000, v198\n"
"v_mfma_f32_16x16x32_bf16 v[4:7], a[196:199], v[128:131], v[4:7]\n"
"buffer_load_dwordx4 v208, %3, s67 offen offset:0 lds\n"
"v_smfmac_f32_16x16x64_bf16 a[132:135], a[144:147], v[104:111], v229\n"
"v_xor_b32 v199, 0x8000, v199\n"
"s_waitcnt lgkmcnt(0)\n"
"v_smfmac_f32_16x16x64_bf16 a[128:131], a[144:147], v[96:103], v229\n"
"buffer_load_dwordx4 v209, %3, s67 offen offset:1024 lds\n"
"v_mfma_f32_16x16x32_bf16 a[64:67], a[224:227], v[96:99], a[64:67]\n"
"v_xor_b32 v200, 0x8000, v200\n"
"v_xor_b32 v201, 0x8000, v201\n"
"v_mfma_f32_16x16x32_bf16 a[64:67], a[228:231], v[100:103], a[64:67]\n"
"v_exp_f32 v48, v48\n"
"v_mfma_f32_16x16x32_bf16 a[72:75], a[228:231], v[108:111], a[72:75]\n"
"ds_read_b128 a[192:195], v192 offset:24576\n"
"ds_read_b128 a[196:199], v192 offset:28672\n"
"v_mfma_f32_16x16x32_bf16 a[72:75], a[224:227], v[104:107], a[72:75]\n"
"v_exp_f32 v49, v49\n"
"v_mfma_f32_16x16x32_bf16 a[80:83], a[224:227], v[112:115], a[80:83]\n"
"ds_read_b128 a[200:203], v193 offset:24576\n"
"v_cvt_pk_bf16_f32 v84, v48, v49\n"
"v_mfma_f32_16x16x32_bf16 a[80:83], a[228:231], v[116:119], a[80:83]\n"
"v_exp_f32 v50, v50\n"
"v_mfma_f32_16x16x32_bf16 a[88:91], a[228:231], v[124:127], a[88:91]\n"
"ds_read_b128 a[204:207], v193 offset:28672\n"
"ds_read_b128 a[208:211], v194 offset:24576\n"
"v_mfma_f32_16x16x32_bf16 a[88:91], a[224:227], v[120:123], a[88:91]\n"
"v_exp_f32 v51, v51\n"
"v_mfma_f32_16x16x32_bf16 a[92:95], a[232:235], v[120:123], a[92:95]\n"
"ds_read_b128 a[212:215], v194 offset:28672\n"
"v_cvt_pk_bf16_f32 v85, v50, v51\n"
"v_mfma_f32_16x16x32_bf16 a[92:95], a[236:239], v[124:127], a[92:95]\n"
"v_exp_f32 v52, v52\n"
"v_mfma_f32_16x16x32_bf16 a[84:87], a[236:239], v[116:119], a[84:87]\n"
"ds_read_b128 a[216:219], v195 offset:24576\n"
"v_xor_b32 v202, 0x8000, v202\n"
"v_mfma_f32_16x16x32_bf16 a[84:87], a[232:235], v[112:115], a[84:87]\n"
"v_exp_f32 v53, v53\n"
"v_mfma_f32_16x16x32_bf16 a[76:79], a[232:235], v[104:107], a[76:79]\n"
"ds_read_b128 a[220:223], v195 offset:28672\n"
"v_cvt_pk_bf16_f32 v86, v52, v53\n"
"v_mfma_f32_16x16x32_bf16 a[76:79], a[236:239], v[108:111], a[76:79]\n"
"v_exp_f32 v54, v54\n"
"v_mfma_f32_16x16x32_bf16 a[68:71], a[236:239], v[100:103], a[68:71]\n"
"v_exp_f32 v55, v55\n"
"v_mfma_f32_16x16x32_bf16 a[68:71], a[232:235], v[96:99], a[68:71]\n"
"v_cvt_pk_bf16_f32 v87, v54, v55\n"
"v_xor_b32 v203, 0x8000, v203\n"
"v_mfma_f32_16x16x32_bf16 a[96:99], a[240:243], v[96:99], a[96:99]\n"
"v_exp_f32 v56, v56\n"
"v_mfma_f32_16x16x32_bf16 a[96:99], a[244:247], v[100:103], a[96:99]\n"
"v_exp_f32 v57, v57\n"
"v_mfma_f32_16x16x32_bf16 a[104:107], a[244:247], v[108:111], a[104:107]\n"
"v_cvt_pk_bf16_f32 v92, v56, v57\n"
"v_xor_b32 v192, 0x8000, v192\n"
"v_mfma_f32_16x16x32_bf16 a[104:107], a[240:243], v[104:107], a[104:107]\n"
"v_exp_f32 v58, v58\n"
"v_mfma_f32_16x16x32_bf16 a[112:115], a[240:243], v[112:115], a[112:115]\n"
"v_exp_f32 v59, v59\n"
"v_mfma_f32_16x16x32_bf16 a[112:115], a[244:247], v[116:119], a[112:115]\n"
"v_cvt_pk_bf16_f32 v93, v58, v59\n"
"v_xor_b32 v193, 0x8000, v193\n"
"v_mfma_f32_16x16x32_bf16 a[120:123], a[244:247], v[124:127], a[120:123]\n"
"v_exp_f32 v60, v60\n"
"v_mfma_f32_16x16x32_bf16 a[120:123], a[240:243], v[120:123], a[120:123]\n"
"v_exp_f32 v61, v61\n"
"v_mfma_f32_16x16x32_bf16 a[124:127], a[248:251], v[120:123], a[124:127]\n"
"v_cvt_pk_bf16_f32 v94, v60, v61\n"
"v_xor_b32 v194, 0x8000, v194\n"
"v_mfma_f32_16x16x32_bf16 a[124:127], a[252:255], v[124:127], a[124:127]\n"
"v_exp_f32 v62, v62\n"
"v_mfma_f32_16x16x32_bf16 a[116:119], a[252:255], v[116:119], a[116:119]\n"
"v_exp_f32 v63, v63\n"
"v_mfma_f32_16x16x32_bf16 a[116:119], a[248:251], v[112:115], a[116:119]\n"
"v_cvt_pk_bf16_f32 v95, v62, v63\n"
"v_xor_b32 v195, 0x8000, v195\n"
"v_mfma_f32_16x16x32_bf16 a[108:111], a[248:251], v[104:107], a[108:111]\n"
"buffer_load_dwordx4 v208, %3, s67 offen offset:2048 lds\n"
"v_mfma_f32_16x16x32_bf16 a[108:111], a[252:255], v[108:111], a[108:111]\n"
"s_xor_b32 s68, s68, 0x8000\n"
"s_cmp_lg_u32 s66, 0\n"
"v_mfma_f32_16x16x32_bf16 a[100:103], a[252:255], v[100:103], a[100:103]\n"
"buffer_load_dwordx4 v209, %3, s67 offen offset:3072 lds\n"
"v_mfma_f32_16x16x32_bf16 a[100:103], a[248:251], v[96:99], a[100:103]\n"
"s_cbranch_scc1 LOOP_A%=\n"
"s_waitcnt vmcnt(17) lgkmcnt(0)\n"
"v_mfma_f32_16x16x32_bf16 v[32:35], a[192:195], v[128:131], v[210:213]\n"
"s_barrier\n"
"v_mfma_f32_16x16x32_bf16 v[32:35], a[200:203], v[132:135], v[32:35]\n"
"ds_read_b64_tr_b16 a[224:225], v196 offset:0\n"
"ds_read_b64_tr_b16 a[226:227], v196 offset:4096\n"
"v_mfma_f32_16x16x32_bf16 v[32:35], a[208:211], v[136:139], v[32:35]\n"
"v_exp_f32 v0, v0\n"
"v_mfma_f32_16x16x32_bf16 v[32:35], a[216:219], v[140:143], v[32:35]\n"
"ds_read_b64_tr_b16 a[228:229], v196 offset:8192\n"
"ds_read_b64_tr_b16 a[230:231], v196 offset:12288\n"
"v_mfma_f32_16x16x32_bf16 v[40:43], a[216:219], v[156:159], v[214:217]\n"
"v_exp_f32 v1, v1\n"
"v_mfma_f32_16x16x32_bf16 v[40:43], a[208:211], v[152:155], v[40:43]\n"
"ds_read_b64_tr_b16 a[232:233], v197 offset:0\n"
"v_cvt_pk_bf16_f32 v96, v0, v1\n"
"v_mfma_f32_16x16x32_bf16 v[40:43], a[200:203], v[148:151], v[40:43]\n"
"v_exp_f32 v2, v2\n"
"v_mfma_f32_16x16x32_bf16 v[40:43], a[192:195], v[144:147], v[40:43]\n"
"ds_read_b64_tr_b16 a[234:235], v197 offset:4096\n"
"ds_read_b64_tr_b16 a[236:237], v197 offset:8192\n"
"v_mfma_f32_16x16x32_bf16 v[48:51], a[192:195], v[160:163], v[232:235]\n"
"v_exp_f32 v3, v3\n"
"v_mfma_f32_16x16x32_bf16 v[48:51], a[200:203], v[164:167], v[48:51]\n"
"ds_read_b64_tr_b16 a[238:239], v197 offset:12288\n"
"v_cvt_pk_bf16_f32 v97, v2, v3\n"
"v_mfma_f32_16x16x32_bf16 v[48:51], a[208:211], v[168:171], v[48:51]\n"
"v_exp_f32 v4, v4\n"
"v_mfma_f32_16x16x32_bf16 v[48:51], a[216:219], v[172:175], v[48:51]\n"
"ds_read_b64_tr_b16 a[240:241], v198 offset:0\n"
"ds_read_b64_tr_b16 a[242:243], v198 offset:4096\n"
"v_mfma_f32_16x16x32_bf16 v[56:59], a[216:219], v[188:191], v[236:239]\n"
"v_exp_f32 v5, v5\n"
"v_mfma_f32_16x16x32_bf16 v[56:59], a[208:211], v[184:187], v[56:59]\n"
"ds_read_b64_tr_b16 a[244:245], v198 offset:8192\n"
"v_cvt_pk_bf16_f32 v98, v4, v5\n"
"v_mfma_f32_16x16x32_bf16 v[56:59], a[200:203], v[180:183], v[56:59]\n"
"v_exp_f32 v6, v6\n"
"v_mfma_f32_16x16x32_bf16 v[56:59], a[192:195], v[176:179], v[56:59]\n"
"ds_read_b64_tr_b16 a[246:247], v198 offset:12288\n"
"ds_read_b64_tr_b16 a[248:249], v199 offset:0\n"
"v_mfma_f32_16x16x32_bf16 v[60:63], a[196:199], v[176:179], v[236:239]\n"
"v_exp_f32 v7, v7\n"
"v_mfma_f32_16x16x32_bf16 v[60:63], a[204:207], v[180:183], v[60:63]\n"
"ds_read_b64_tr_b16 a[250:251], v199 offset:4096\n"
"v_cvt_pk_bf16_f32 v99, v6, v7\n"
"v_mfma_f32_16x16x32_bf16 v[60:63], a[212:215], v[184:187], v[60:63]\n"
"v_exp_f32 v8, v8\n"
"v_mfma_f32_16x16x32_bf16 v[60:63], a[220:223], v[188:191], v[60:63]\n"
"ds_read_b64_tr_b16 a[252:253], v199 offset:8192\n"
"ds_read_b64_tr_b16 a[254:255], v199 offset:12288\n"
"v_mfma_f32_16x16x32_bf16 v[52:55], a[220:223], v[172:175], v[232:235]\n"
"v_exp_f32 v9, v9\n"
"v_mfma_f32_16x16x32_bf16 v[52:55], a[212:215], v[168:171], v[52:55]\n"
"v_cvt_pk_bf16_f32 v104, v8, v9\n"
"s_lshl_b32 s64, %12, 18\n"
"v_mfma_f32_16x16x32_bf16 v[52:55], a[204:207], v[164:167], v[52:55]\n"
"v_exp_f32 v10, v10\n"
"v_mfma_f32_16x16x32_bf16 v[52:55], a[196:199], v[160:163], v[52:55]\n"
"v_exp_f32 v11, v11\n"
"v_mfma_f32_16x16x32_bf16 v[44:47], a[196:199], v[144:147], v[214:217]\n"
"v_cvt_pk_bf16_f32 v105, v10, v11\n"
"s_lshl_b32 s72, %11, 3\n"
"v_mfma_f32_16x16x32_bf16 v[44:47], a[204:207], v[148:151], v[44:47]\n"
"v_exp_f32 v12, v12\n"
"v_mfma_f32_16x16x32_bf16 v[44:47], a[212:215], v[152:155], v[44:47]\n"
"v_exp_f32 v13, v13\n"
"v_mfma_f32_16x16x32_bf16 v[44:47], a[220:223], v[156:159], v[44:47]\n"
"v_cvt_pk_bf16_f32 v106, v12, v13\n"
"s_add_u32 s64, s64, s72\n"
"v_mfma_f32_16x16x32_bf16 v[36:39], a[220:223], v[140:143], v[210:213]\n"
"v_exp_f32 v14, v14\n"
"v_mfma_f32_16x16x32_bf16 v[36:39], a[212:215], v[136:139], v[36:39]\n"
"v_exp_f32 v15, v15\n"
"v_mfma_f32_16x16x32_bf16 v[36:39], a[204:207], v[132:135], v[36:39]\n"
"v_cvt_pk_bf16_f32 v107, v14, v15\n"
"s_add_u32 m0, %11, 65536\n"
"v_mfma_f32_16x16x32_bf16 v[36:39], a[196:199], v[128:131], v[36:39]\n"
"buffer_load_dwordx4 v220, %6, s64 offen offset:0 lds\n"
"v_smfmac_f32_16x16x64_bf16 a[136:139], a[144:147], v[80:87], v229\n"
"buffer_load_dwordx4 v221, %6, s64 offen offset:1024 lds\n"
"s_waitcnt lgkmcnt(0)\n"
"v_smfmac_f32_16x16x64_bf16 a[140:143], a[144:147], v[88:95], v229\n"
"buffer_load_dwordx4 v222, %6, s64 offen offset:2048 lds\n"
"v_mfma_f32_16x16x32_bf16 a[24:27], a[224:227], v[88:91], a[24:27]\n"
"buffer_load_dwordx4 v223, %6, s64 offen offset:3072 lds\n"
"v_mfma_f32_16x16x32_bf16 a[24:27], a[228:231], v[92:95], a[24:27]\n"
"v_exp_f32 v16, v16\n"
"v_mfma_f32_16x16x32_bf16 a[16:19], a[228:231], v[84:87], a[16:19]\n"
"ds_read_b64_tr_b16 a[192:193], v200 offset:0\n"
"ds_read_b64_tr_b16 a[194:195], v200 offset:4096\n"
"v_mfma_f32_16x16x32_bf16 a[16:19], a[224:227], v[80:83], a[16:19]\n"
"v_exp_f32 v17, v17\n"
"v_mfma_f32_16x16x32_bf16 a[8:11], a[224:227], v[72:75], a[8:11]\n"
"ds_read_b64_tr_b16 a[196:197], v200 offset:8192\n"
"v_cvt_pk_bf16_f32 v112, v16, v17\n"
"s_lshr_b32 s72, %11, 2\n"
"v_mfma_f32_16x16x32_bf16 a[8:11], a[228:231], v[76:79], a[8:11]\n"
"v_exp_f32 v18, v18\n"
"v_mfma_f32_16x16x32_bf16 a[0:3], a[228:231], v[68:71], a[0:3]\n"
"ds_read_b64_tr_b16 a[198:199], v200 offset:12288\n"
"ds_read_b64_tr_b16 a[200:201], v201 offset:0\n"
"v_mfma_f32_16x16x32_bf16 a[0:3], a[224:227], v[64:67], a[0:3]\n"
"v_exp_f32 v19, v19\n"
"v_mfma_f32_16x16x32_bf16 a[4:7], a[232:235], v[64:67], a[4:7]\n"
"ds_read_b64_tr_b16 a[202:203], v201 offset:4096\n"
"v_cvt_pk_bf16_f32 v113, v18, v19\n"
"s_add_u32 s72, s72, 148480\n"
"v_mfma_f32_16x16x32_bf16 a[4:7], a[236:239], v[68:71], a[4:7]\n"
"v_exp_f32 v20, v20\n"
"v_mfma_f32_16x16x32_bf16 a[12:15], a[236:239], v[76:79], a[12:15]\n"
"ds_read_b64_tr_b16 a[204:205], v201 offset:8192\n"
"ds_read_b64_tr_b16 a[206:207], v201 offset:12288\n"
"v_mfma_f32_16x16x32_bf16 a[12:15], a[232:235], v[72:75], a[12:15]\n"
"v_exp_f32 v21, v21\n"
"v_mfma_f32_16x16x32_bf16 a[20:23], a[232:235], v[80:83], a[20:23]\n"
"ds_read_b64_tr_b16 a[208:209], v202 offset:0\n"
"v_cvt_pk_bf16_f32 v114, v20, v21\n"
"v_add_u32 v232, s72, %19\n"
"v_mfma_f32_16x16x32_bf16 a[20:23], a[236:239], v[84:87], a[20:23]\n"
"v_exp_f32 v22, v22\n"
"v_mfma_f32_16x16x32_bf16 a[28:31], a[236:239], v[92:95], a[28:31]\n"
"v_exp_f32 v23, v23\n"
"v_mfma_f32_16x16x32_bf16 a[28:31], a[232:235], v[88:91], a[28:31]\n"
"ds_read_b64_tr_b16 a[210:211], v202 offset:4096\n"
"ds_read_b64_tr_b16 a[212:213], v202 offset:8192\n"
"v_cvt_pk_bf16_f32 v115, v22, v23\n"
"v_mfma_f32_16x16x32_bf16 a[56:59], a[240:243], v[88:91], a[56:59]\n"
"v_exp_f32 v24, v24\n"
"v_mfma_f32_16x16x32_bf16 a[56:59], a[244:247], v[92:95], a[56:59]\n"
"v_exp_f32 v25, v25\n"
"v_mfma_f32_16x16x32_bf16 a[48:51], a[244:247], v[84:87], a[48:51]\n"
"ds_read_b64_tr_b16 a[214:215], v202 offset:12288\n"
"ds_read_b64_tr_b16 a[216:217], v203 offset:0\n"
"v_cvt_pk_bf16_f32 v120, v24, v25\n"
"v_mfma_f32_16x16x32_bf16 a[48:51], a[240:243], v[80:83], a[48:51]\n"
"v_exp_f32 v26, v26\n"
"v_mfma_f32_16x16x32_bf16 a[40:43], a[240:243], v[72:75], a[40:43]\n"
"v_exp_f32 v27, v27\n"
"v_mfma_f32_16x16x32_bf16 a[40:43], a[244:247], v[76:79], a[40:43]\n"
"ds_read_b64_tr_b16 a[218:219], v203 offset:4096\n"
"ds_read_b64_tr_b16 a[220:221], v203 offset:8192\n"
"v_cvt_pk_bf16_f32 v121, v26, v27\n"
"v_mfma_f32_16x16x32_bf16 a[32:35], a[244:247], v[68:71], a[32:35]\n"
"v_exp_f32 v28, v28\n"
"v_mfma_f32_16x16x32_bf16 a[32:35], a[240:243], v[64:67], a[32:35]\n"
"v_exp_f32 v29, v29\n"
"v_mfma_f32_16x16x32_bf16 a[36:39], a[248:251], v[64:67], a[36:39]\n"
"ds_read_b64_tr_b16 a[222:223], v203 offset:12288\n"
"v_cvt_pk_bf16_f32 v122, v28, v29\n"
"ds_read_b128 v[236:239], v232\n"
"v_mfma_f32_16x16x32_bf16 a[36:39], a[252:255], v[68:71], a[36:39]\n"
"v_exp_f32 v30, v30\n"
"v_mfma_f32_16x16x32_bf16 a[44:47], a[252:255], v[76:79], a[44:47]\n"
"v_exp_f32 v31, v31\n"
"v_mfma_f32_16x16x32_bf16 a[44:47], a[248:251], v[72:75], a[44:47]\n"
"v_cvt_pk_bf16_f32 v123, v30, v31\n"
"s_add_u32 s64, s64, 131072\n"
"s_add_u32 m0, %11, 81920\n"
"v_mfma_f32_16x16x32_bf16 a[52:55], a[248:251], v[80:83], a[52:55]\n"
"buffer_load_dwordx4 v220, %6, s64 offen offset:0 lds\n"
"v_mfma_f32_16x16x32_bf16 a[52:55], a[252:255], v[84:87], a[52:55]\n"
"buffer_load_dwordx4 v221, %6, s64 offen offset:1024 lds\n"
"v_mfma_f32_16x16x32_bf16 a[60:63], a[252:255], v[92:95], a[60:63]\n"
"buffer_load_dwordx4 v222, %6, s64 offen offset:2048 lds\n"
"v_mfma_f32_16x16x32_bf16 a[60:63], a[248:251], v[88:91], a[60:63]\n"
"buffer_load_dwordx4 v223, %6, s64 offen offset:3072 lds\n"
"s_waitcnt vmcnt(16) lgkmcnt(0)\n"
"v_smfmac_f32_16x16x64_bf16 a[132:135], a[144:147], v[72:79], v229\n"
"s_barrier\n"
"v_smfmac_f32_16x16x64_bf16 a[128:131], a[144:147], v[64:71], v229\n"
"v_exp_f32 v32, v32\n"
"v_mfma_f32_16x16x32_bf16 a[64:67], a[192:195], v[64:67], a[64:67]\n"
"ds_read_b64_tr_b16 a[226:227], v196 offset:20480\n"
"ds_read_b64_tr_b16 a[224:225], v196 offset:16384\n"
"v_exp_f32 v33, v33\n"
"v_mfma_f32_16x16x32_bf16 a[64:67], a[196:199], v[68:71], a[64:67]\n"
"s_mov_b32 s72, 0\n"
"s_add_u32 m0, %11, 0\n"
"v_exp_f32 v34, v34\n"
"v_cvt_pk_bf16_f32 v100, v32, v33\n"
"v_mfma_f32_16x16x32_bf16 a[72:75], a[196:199], v[76:79], a[72:75]\n"
"ds_read_b64_tr_b16 a[228:229], v196 offset:24576\n"
"buffer_load_dwordx4 v236, %8, s72 offen offset:0 lds\n"
"v_exp_f32 v35, v35\n"
"v_mfma_f32_16x16x32_bf16 a[72:75], a[192:195], v[72:75], a[72:75]\n"
"v_exp_f32 v36, v36\n"
"v_cvt_pk_bf16_f32 v101, v34, v35\n"
"v_mfma_f32_16x16x32_bf16 a[80:83], a[192:195], v[80:83], a[80:83]\n"
"ds_read_b64_tr_b16 a[230:231], v196 offset:28672\n"
"v_exp_f32 v37, v37\n"
"v_mfma_f32_16x16x32_bf16 a[80:83], a[196:199], v[84:87], a[80:83]\n"
"v_exp_f32 v38, v38\n"
"v_cvt_pk_bf16_f32 v102, v36, v37\n"
"v_mfma_f32_16x16x32_bf16 a[88:91], a[196:199], v[92:95], a[88:91]\n"
"ds_read_b64_tr_b16 a[234:235], v197 offset:20480\n"
"ds_read_b64_tr_b16 a[232:233], v197 offset:16384\n"
"buffer_load_dwordx4 v237, %8, s72 offen offset:1024 lds\n"
"v_exp_f32 v39, v39\n"
"v_mfma_f32_16x16x32_bf16 a[88:91], a[192:195], v[88:91], a[88:91]\n"
"v_exp_f32 v40, v40\n"
"v_cvt_pk_bf16_f32 v103, v38, v39\n"
"v_mfma_f32_16x16x32_bf16 a[92:95], a[200:203], v[88:91], a[92:95]\n"
"ds_read_b64_tr_b16 a[236:237], v197 offset:24576\n"
"v_exp_f32 v41, v41\n"
"v_mfma_f32_16x16x32_bf16 a[92:95], a[204:207], v[92:95], a[92:95]\n"
"v_exp_f32 v42, v42\n"
"v_cvt_pk_bf16_f32 v108, v40, v41\n"
"v_mfma_f32_16x16x32_bf16 a[84:87], a[204:207], v[84:87], a[84:87]\n"
"ds_read_b64_tr_b16 a[238:239], v197 offset:28672\n"
"buffer_load_dwordx4 v238, %8, s72 offen offset:2048 lds\n"
"v_exp_f32 v43, v43\n"
"v_mfma_f32_16x16x32_bf16 a[84:87], a[200:203], v[80:83], a[84:87]\n"
"v_exp_f32 v44, v44\n"
"v_cvt_pk_bf16_f32 v109, v42, v43\n"
"v_mfma_f32_16x16x32_bf16 a[76:79], a[200:203], v[72:75], a[76:79]\n"
"ds_read_b64_tr_b16 a[242:243], v198 offset:20480\n"
"ds_read_b64_tr_b16 a[240:241], v198 offset:16384\n"
"v_exp_f32 v45, v45\n"
"v_mfma_f32_16x16x32_bf16 a[76:79], a[204:207], v[76:79], a[76:79]\n"
"v_exp_f32 v46, v46\n"
"v_cvt_pk_bf16_f32 v110, v44, v45\n"
"v_mfma_f32_16x16x32_bf16 a[68:71], a[204:207], v[68:71], a[68:71]\n"
"ds_read_b64_tr_b16 a[244:245], v198 offset:24576\n"
"buffer_load_dwordx4 v239, %8, s72 offen offset:3072 lds\n"
"v_exp_f32 v47, v47\n"
"v_mfma_f32_16x16x32_bf16 a[68:71], a[200:203], v[64:67], a[68:71]\n"
"v_exp_f32 v48, v48\n"
"v_cvt_pk_bf16_f32 v111, v46, v47\n"
"v_mfma_f32_16x16x32_bf16 a[96:99], a[208:211], v[64:67], a[96:99]\n"
"ds_read_b64_tr_b16 a[246:247], v198 offset:28672\n"
"v_exp_f32 v49, v49\n"
"v_mfma_f32_16x16x32_bf16 a[96:99], a[212:215], v[68:71], a[96:99]\n"
"s_mov_b32 s72, 458752\n"
"s_add_u32 m0, %11, 16384\n"
"v_exp_f32 v50, v50\n"
"v_cvt_pk_bf16_f32 v116, v48, v49\n"
"v_mfma_f32_16x16x32_bf16 a[104:107], a[212:215], v[76:79], a[104:107]\n"
"ds_read_b64_tr_b16 a[250:251], v199 offset:20480\n"
"ds_read_b64_tr_b16 a[248:249], v199 offset:16384\n"
"buffer_load_dwordx4 v236, %8, s72 offen offset:0 lds\n"
"v_exp_f32 v51, v51\n"
"v_mfma_f32_16x16x32_bf16 a[104:107], a[208:211], v[72:75], a[104:107]\n"
"v_exp_f32 v52, v52\n"
"v_cvt_pk_bf16_f32 v117, v50, v51\n"
"v_mfma_f32_16x16x32_bf16 a[112:115], a[208:211], v[80:83], a[112:115]\n"
"ds_read_b64_tr_b16 a[252:253], v199 offset:24576\n"
"v_exp_f32 v53, v53\n"
"v_mfma_f32_16x16x32_bf16 a[112:115], a[212:215], v[84:87], a[112:115]\n"
"v_exp_f32 v54, v54\n"
"v_cvt_pk_bf16_f32 v118, v52, v53\n"
"v_mfma_f32_16x16x32_bf16 a[120:123], a[212:215], v[92:95], a[120:123]\n"
"ds_read_b64_tr_b16 a[254:255], v199 offset:28672\n"
"buffer_load_dwordx4 v237, %8, s72 offen offset:1024 lds\n"
"v_exp_f32 v55, v55\n"
"v_mfma_f32_16x16x32_bf16 a[120:123], a[208:211], v[88:91], a[120:123]\n"
"v_exp_f32 v56, v56\n"
"v_cvt_pk_bf16_f32 v119, v54, v55\n"
"v_mfma_f32_16x16x32_bf16 a[124:127], a[216:219], v[88:91], a[124:127]\n"
"v_exp_f32 v57, v57\n"
"v_mfma_f32_16x16x32_bf16 a[124:127], a[220:223], v[92:95], a[124:127]\n"
"v_exp_f32 v58, v58\n"
"v_cvt_pk_bf16_f32 v124, v56, v57\n"
"v_mfma_f32_16x16x32_bf16 a[116:119], a[220:223], v[84:87], a[116:119]\n"
"buffer_load_dwordx4 v238, %8, s72 offen offset:2048 lds\n"
"v_exp_f32 v59, v59\n"
"v_mfma_f32_16x16x32_bf16 a[116:119], a[216:219], v[80:83], a[116:119]\n"
"v_exp_f32 v60, v60\n"
"v_cvt_pk_bf16_f32 v125, v58, v59\n"
"v_mfma_f32_16x16x32_bf16 a[108:111], a[216:219], v[72:75], a[108:111]\n"
"v_exp_f32 v61, v61\n"
"v_mfma_f32_16x16x32_bf16 a[108:111], a[220:223], v[76:79], a[108:111]\n"
"v_exp_f32 v62, v62\n"
"v_cvt_pk_bf16_f32 v126, v60, v61\n"
"v_mfma_f32_16x16x32_bf16 a[100:103], a[220:223], v[68:71], a[100:103]\n"
"buffer_load_dwordx4 v239, %8, s72 offen offset:3072 lds\n"
"v_exp_f32 v63, v63\n"
"v_mfma_f32_16x16x32_bf16 a[100:103], a[216:219], v[64:67], a[100:103]\n"
"v_cvt_pk_bf16_f32 v127, v62, v63\n"
"v_smfmac_f32_16x16x64_bf16 a[128:131], a[144:147], v[96:103], v229\n"
"s_mov_b32 s72, 917504\n"
"s_add_u32 m0, %11, 32768\n"
"ds_read_b64_tr_b16 a[192:193], v200 offset:16384\n"
"v_smfmac_f32_16x16x64_bf16 a[132:135], a[144:147], v[104:111], v229\n"
"buffer_load_dwordx4 v236, %8, s72 offen offset:0 lds\n"
"ds_read_b64_tr_b16 a[194:195], v200 offset:20480\n"
"v_smfmac_f32_16x16x64_bf16 a[136:139], a[144:147], v[112:119], v229\n"
"ds_read_b64_tr_b16 a[196:197], v200 offset:24576\n"
"v_smfmac_f32_16x16x64_bf16 a[140:143], a[144:147], v[120:127], v229\n"
"ds_read_b64_tr_b16 a[198:199], v200 offset:28672\n"
"s_waitcnt lgkmcnt(4)\n"
"v_mfma_f32_16x16x32_bf16 a[24:27], a[224:227], v[120:123], a[24:27]\n"
"ds_read_b64_tr_b16 a[200:201], v201 offset:16384\n"
"v_mfma_f32_16x16x32_bf16 a[24:27], a[228:231], v[124:127], a[24:27]\n"
"buffer_load_dwordx4 v237, %8, s72 offen offset:1024 lds\n"
"ds_read_b64_tr_b16 a[202:203], v201 offset:20480\n"
"v_mfma_f32_16x16x32_bf16 a[16:19], a[228:231], v[116:119], a[16:19]\n"
"ds_read_b64_tr_b16 a[204:205], v201 offset:24576\n"
"v_mfma_f32_16x16x32_bf16 a[16:19], a[224:227], v[112:115], a[16:19]\n"
"ds_read_b64_tr_b16 a[206:207], v201 offset:28672\n"
"v_mfma_f32_16x16x32_bf16 a[8:11], a[224:227], v[104:107], a[8:11]\n"
"ds_read_b64_tr_b16 a[208:209], v202 offset:16384\n"
"v_mfma_f32_16x16x32_bf16 a[8:11], a[228:231], v[108:111], a[8:11]\n"
"buffer_load_dwordx4 v238, %8, s72 offen offset:2048 lds\n"
"ds_read_b64_tr_b16 a[210:211], v202 offset:20480\n"
"v_mfma_f32_16x16x32_bf16 a[0:3], a[228:231], v[100:103], a[0:3]\n"
"ds_read_b64_tr_b16 a[212:213], v202 offset:24576\n"
"v_accvgpr_read_b32 v232, a128\n"
"v_mfma_f32_16x16x32_bf16 a[0:3], a[224:227], v[96:99], a[0:3]\n"
"ds_read_b64_tr_b16 a[214:215], v202 offset:28672\n"
"v_accvgpr_read_b32 v233, a132\n"
"v_mfma_f32_16x16x32_bf16 a[4:7], a[232:235], v[96:99], a[4:7]\n"
"ds_read_b64_tr_b16 a[216:217], v203 offset:16384\n"
"v_accvgpr_read_b32 v234, a136\n"
"v_mfma_f32_16x16x32_bf16 a[4:7], a[236:239], v[100:103], a[4:7]\n"
"buffer_load_dwordx4 v239, %8, s72 offen offset:3072 lds\n"
"ds_read_b64_tr_b16 a[218:219], v203 offset:20480\n"
"v_accvgpr_read_b32 v235, a140\n"
"v_mfma_f32_16x16x32_bf16 a[12:15], a[236:239], v[108:111], a[12:15]\n"
"ds_read_b64_tr_b16 a[220:221], v203 offset:24576\n"
"v_accvgpr_read_b32 v224, a129\n"
"v_accvgpr_read_b32 v225, a133\n"
"v_mfma_f32_16x16x32_bf16 a[12:15], a[232:235], v[104:107], a[12:15]\n"
"ds_read_b64_tr_b16 a[222:223], v203 offset:28672\n"
"v_accvgpr_read_b32 v226, a137\n"
"v_accvgpr_read_b32 v227, a141\n"
"v_mfma_f32_16x16x32_bf16 a[20:23], a[232:235], v[112:115], a[20:23]\n"
"s_mov_b32 s72, 1376256\n"
"s_add_u32 m0, %11, 49152\n"
"v_mov_b32 v210, 0\n"
"v_mov_b32 v211, 0\n"
"v_mfma_f32_16x16x32_bf16 a[20:23], a[236:239], v[116:119], a[20:23]\n"
"buffer_load_dwordx4 v236, %8, s72 offen offset:0 lds\n"
"v_mov_b32 v212, 0\n"
"v_mov_b32 v213, 0\n"
"v_mfma_f32_16x16x32_bf16 a[28:31], a[236:239], v[124:127], a[28:31]\n"
"v_mov_b32 v214, 0\n"
"v_mov_b32 v215, 0\n"
"v_mfma_f32_16x16x32_bf16 a[28:31], a[232:235], v[120:123], a[28:31]\n"
"v_mov_b32 v216, 0\n"
"v_mov_b32 v217, 0\n"
"v_mfma_f32_16x16x32_bf16 a[56:59], a[240:243], v[120:123], a[56:59]\n"
"v_add_f32 v232, v232, v224\n"
"v_add_f32 v233, v233, v225\n"
"v_mfma_f32_16x16x32_bf16 a[56:59], a[244:247], v[124:127], a[56:59]\n"
"buffer_load_dwordx4 v237, %8, s72 offen offset:1024 lds\n"
"v_add_f32 v234, v234, v226\n"
"v_add_f32 v235, v235, v227\n"
"v_mfma_f32_16x16x32_bf16 a[48:51], a[244:247], v[116:119], a[48:51]\n"
"v_rcp_f32 v224, v232\n"
"v_mfma_f32_16x16x32_bf16 a[48:51], a[240:243], v[112:115], a[48:51]\n"
"v_rcp_f32 v225, v233\n"
"v_mfma_f32_16x16x32_bf16 a[40:43], a[240:243], v[104:107], a[40:43]\n"
"v_rcp_f32 v226, v234\n"
"v_mfma_f32_16x16x32_bf16 a[40:43], a[244:247], v[108:111], a[40:43]\n"
"buffer_load_dwordx4 v238, %8, s72 offen offset:2048 lds\n"
"v_rcp_f32 v227, v235\n"
"v_mfma_f32_16x16x32_bf16 a[32:35], a[244:247], v[100:103], a[32:35]\n"
"v_add_f32 v232, v232, v233\n"
"v_add_f32 v234, v234, v235\n"
"v_mfma_f32_16x16x32_bf16 a[32:35], a[240:243], v[96:99], a[32:35]\n"
"v_add_f32 v232, v232, v234\n"
"v_mfma_f32_16x16x32_bf16 a[36:39], a[248:251], v[96:99], a[36:39]\n"
"v_accvgpr_read_b32 v64, a0\n"
"v_accvgpr_read_b32 v65, a1\n"
"v_mfma_f32_16x16x32_bf16 a[36:39], a[252:255], v[100:103], a[36:39]\n"
"buffer_load_dwordx4 v239, %8, s72 offen offset:3072 lds\n"
"v_accvgpr_read_b32 v66, a2\n"
"v_accvgpr_read_b32 v67, a3\n"
"v_mfma_f32_16x16x32_bf16 a[44:47], a[252:255], v[108:111], a[44:47]\n"
"v_accvgpr_read_b32 v68, a4\n"
"v_accvgpr_read_b32 v69, a5\n"
"v_mfma_f32_16x16x32_bf16 a[44:47], a[248:251], v[104:107], a[44:47]\n"
"v_accvgpr_read_b32 v70, a6\n"
"v_accvgpr_read_b32 v71, a7\n"
"v_mfma_f32_16x16x32_bf16 a[52:55], a[248:251], v[112:115], a[52:55]\n"
"v_mul_f32 v64, v64, v224\n"
"v_mul_f32 v65, v65, v224\n"
"v_mfma_f32_16x16x32_bf16 a[52:55], a[252:255], v[116:119], a[52:55]\n"
"v_mul_f32 v66, v66, v224\n"
"v_mul_f32 v67, v67, v224\n"
"v_mfma_f32_16x16x32_bf16 a[60:63], a[252:255], v[124:127], a[60:63]\n"
"v_mul_f32 v68, v68, v224\n"
"v_mul_f32 v69, v69, v224\n"
"v_mfma_f32_16x16x32_bf16 a[60:63], a[248:251], v[120:123], a[60:63]\n"
"v_mul_f32 v70, v70, v224\n"
"v_mul_f32 v71, v71, v224\n"
"s_waitcnt lgkmcnt(0)\n"
"v_mfma_f32_16x16x32_bf16 a[64:67], a[192:195], v[96:99], a[64:67]\n"
"v_cvt_pk_bf16_f32 v0, v64, v65\n"
"v_cvt_pk_bf16_f32 v1, v66, v67\n"
"v_mfma_f32_16x16x32_bf16 a[64:67], a[196:199], v[100:103], a[64:67]\n"
"v_cvt_pk_bf16_f32 v2, v68, v69\n"
"v_cvt_pk_bf16_f32 v3, v70, v71\n"
"v_mfma_f32_16x16x32_bf16 a[72:75], a[196:199], v[108:111], a[72:75]\n"
"buffer_store_dwordx4 v[0:3], %19, %4, 0 offen offset:0 sc1\n"
"v_mfma_f32_16x16x32_bf16 a[72:75], a[192:195], v[104:107], a[72:75]\n"
"v_accvgpr_read_b32 v72, a8\n"
"v_accvgpr_read_b32 v73, a9\n"
"v_mfma_f32_16x16x32_bf16 a[80:83], a[192:195], v[112:115], a[80:83]\n"
"v_accvgpr_read_b32 v74, a10\n"
"v_accvgpr_read_b32 v75, a11\n"
"v_mfma_f32_16x16x32_bf16 a[80:83], a[196:199], v[116:119], a[80:83]\n"
"v_accvgpr_read_b32 v76, a12\n"
"v_accvgpr_read_b32 v77, a13\n"
"v_mfma_f32_16x16x32_bf16 a[88:91], a[196:199], v[124:127], a[88:91]\n"
"v_accvgpr_read_b32 v78, a14\n"
"v_accvgpr_read_b32 v79, a15\n"
"v_mfma_f32_16x16x32_bf16 a[88:91], a[192:195], v[120:123], a[88:91]\n"
"v_mul_f32 v72, v72, v225\n"
"v_mul_f32 v73, v73, v225\n"
"v_mfma_f32_16x16x32_bf16 a[92:95], a[200:203], v[120:123], a[92:95]\n"
"v_mul_f32 v74, v74, v225\n"
"v_mul_f32 v75, v75, v225\n"
"v_mfma_f32_16x16x32_bf16 a[92:95], a[204:207], v[124:127], a[92:95]\n"
"v_mul_f32 v76, v76, v225\n"
"v_mul_f32 v77, v77, v225\n"
"v_mfma_f32_16x16x32_bf16 a[84:87], a[204:207], v[116:119], a[84:87]\n"
"v_mul_f32 v78, v78, v225\n"
"v_mul_f32 v79, v79, v225\n"
"v_mfma_f32_16x16x32_bf16 a[84:87], a[200:203], v[112:115], a[84:87]\n"
"v_cvt_pk_bf16_f32 v4, v72, v73\n"
"v_cvt_pk_bf16_f32 v5, v74, v75\n"
"v_mfma_f32_16x16x32_bf16 a[76:79], a[200:203], v[104:107], a[76:79]\n"
"v_cvt_pk_bf16_f32 v6, v76, v77\n"
"v_cvt_pk_bf16_f32 v7, v78, v79\n"
"v_mfma_f32_16x16x32_bf16 a[76:79], a[204:207], v[108:111], a[76:79]\n"
"buffer_store_dwordx4 v[4:7], %19, %4, 0 offen offset:1024 sc1\n"
"v_mfma_f32_16x16x32_bf16 a[68:71], a[204:207], v[100:103], a[68:71]\n"
"v_accvgpr_read_b32 v80, a16\n"
"v_accvgpr_read_b32 v81, a17\n"
"v_mfma_f32_16x16x32_bf16 a[68:71], a[200:203], v[96:99], a[68:71]\n"
"v_accvgpr_read_b32 v82, a18\n"
"v_accvgpr_read_b32 v83, a19\n"
"v_mfma_f32_16x16x32_bf16 a[96:99], a[208:211], v[96:99], a[96:99]\n"
"v_accvgpr_read_b32 v84, a20\n"
"v_accvgpr_read_b32 v85, a21\n"
"v_mfma_f32_16x16x32_bf16 a[96:99], a[212:215], v[100:103], a[96:99]\n"
"v_accvgpr_read_b32 v86, a22\n"
"v_accvgpr_read_b32 v87, a23\n"
"v_mfma_f32_16x16x32_bf16 a[104:107], a[212:215], v[108:111], a[104:107]\n"
"v_mul_f32 v80, v80, v226\n"
"v_mul_f32 v81, v81, v226\n"
"v_mfma_f32_16x16x32_bf16 a[104:107], a[208:211], v[104:107], a[104:107]\n"
"v_mul_f32 v82, v82, v226\n"
"v_mul_f32 v83, v83, v226\n"
"v_mfma_f32_16x16x32_bf16 a[112:115], a[208:211], v[112:115], a[112:115]\n"
"v_mul_f32 v84, v84, v226\n"
"v_mul_f32 v85, v85, v226\n"
"v_mfma_f32_16x16x32_bf16 a[112:115], a[212:215], v[116:119], a[112:115]\n"
"v_mul_f32 v86, v86, v226\n"
"v_mul_f32 v87, v87, v226\n"
"v_mfma_f32_16x16x32_bf16 a[120:123], a[212:215], v[124:127], a[120:123]\n"
"v_cvt_pk_bf16_f32 v8, v80, v81\n"
"v_cvt_pk_bf16_f32 v9, v82, v83\n"
"v_mfma_f32_16x16x32_bf16 a[120:123], a[208:211], v[120:123], a[120:123]\n"
"v_cvt_pk_bf16_f32 v10, v84, v85\n"
"v_cvt_pk_bf16_f32 v11, v86, v87\n"
"v_mfma_f32_16x16x32_bf16 a[124:127], a[216:219], v[120:123], a[124:127]\n"
"buffer_store_dwordx4 v[8:11], v219, %4, 0 offen offset:0 sc1\n"
"v_mfma_f32_16x16x32_bf16 a[124:127], a[220:223], v[124:127], a[124:127]\n"
"v_accvgpr_read_b32 v88, a24\n"
"v_accvgpr_read_b32 v89, a25\n"
"v_mfma_f32_16x16x32_bf16 a[116:119], a[220:223], v[116:119], a[116:119]\n"
"v_accvgpr_read_b32 v90, a26\n"
"v_accvgpr_read_b32 v91, a27\n"
"v_mfma_f32_16x16x32_bf16 a[116:119], a[216:219], v[112:115], a[116:119]\n"
"v_accvgpr_read_b32 v92, a28\n"
"v_accvgpr_read_b32 v93, a29\n"
"v_mfma_f32_16x16x32_bf16 a[108:111], a[216:219], v[104:107], a[108:111]\n"
"v_accvgpr_read_b32 v94, a30\n"
"v_accvgpr_read_b32 v95, a31\n"
"v_mfma_f32_16x16x32_bf16 a[108:111], a[220:223], v[108:111], a[108:111]\n"
"v_mul_f32 v88, v88, v227\n"
"v_mul_f32 v89, v89, v227\n"
"v_mfma_f32_16x16x32_bf16 a[100:103], a[220:223], v[100:103], a[100:103]\n"
"v_mul_f32 v90, v90, v227\n"
"v_mul_f32 v91, v91, v227\n"
"v_mfma_f32_16x16x32_bf16 a[100:103], a[216:219], v[96:99], a[100:103]\n"
"v_mul_f32 v92, v92, v227\n"
"v_mul_f32 v93, v93, v227\n"
"v_mul_f32 v94, v94, v227\n"
"v_mul_f32 v95, v95, v227\n"
"v_cvt_pk_bf16_f32 v12, v88, v89\n"
"v_cvt_pk_bf16_f32 v13, v90, v91\n"
"v_cvt_pk_bf16_f32 v14, v92, v93\n"
"v_cvt_pk_bf16_f32 v15, v94, v95\n"
"buffer_store_dwordx4 v[12:15], v219, %4, 0 offen offset:1024 sc1\n"
"v_mfma_f32_16x16x32_bf16 v[214:217], v[210:213], v[0:3], v[214:217]\n"
"v_mfma_f32_16x16x32_bf16 v[214:217], v[210:213], v[4:7], v[214:217]\n"
"v_mfma_f32_16x16x32_bf16 v[214:217], v[210:213], v[8:11], v[214:217]\n"
"v_accvgpr_read_b32 v64, a32\n"
"v_accvgpr_read_b32 v65, a33\n"
"v_accvgpr_read_b32 v66, a34\n"
"v_accvgpr_read_b32 v67, a35\n"
"v_accvgpr_read_b32 v68, a36\n"
"v_accvgpr_read_b32 v69, a37\n"
"v_accvgpr_read_b32 v70, a38\n"
"v_accvgpr_read_b32 v71, a39\n"
"v_pk_mul_f32 v[64:65], v[64:65], v[224:225] op_sel:[0,0] op_sel_hi:[1,0]\n"
"v_pk_mul_f32 v[66:67], v[66:67], v[224:225] op_sel:[0,0] op_sel_hi:[1,0]\n"
"v_pk_mul_f32 v[68:69], v[68:69], v[224:225] op_sel:[0,0] op_sel_hi:[1,0]\n"
"v_pk_mul_f32 v[70:71], v[70:71], v[224:225] op_sel:[0,0] op_sel_hi:[1,0]\n"
"v_cvt_pk_bf16_f32 v16, v64, v65\n"
"v_cvt_pk_bf16_f32 v17, v66, v67\n"
"v_cvt_pk_bf16_f32 v18, v68, v69\n"
"v_cvt_pk_bf16_f32 v19, v70, v71\n"
"buffer_store_dwordx4 v[16:19], %19, %4, 0 offen offset:2048 sc1\n"
"v_mfma_f32_16x16x32_bf16 v[214:217], v[210:213], v[12:15], v[214:217]\n"
"v_accvgpr_read_b32 v72, a40\n"
"v_accvgpr_read_b32 v73, a41\n"
"v_accvgpr_read_b32 v74, a42\n"
"v_accvgpr_read_b32 v75, a43\n"
"v_accvgpr_read_b32 v76, a44\n"
"v_accvgpr_read_b32 v77, a45\n"
"v_accvgpr_read_b32 v78, a46\n"
"v_accvgpr_read_b32 v79, a47\n"
"v_pk_mul_f32 v[72:73], v[72:73], v[224:225] op_sel:[0,1] op_sel_hi:[1,1]\n"
"v_pk_mul_f32 v[74:75], v[74:75], v[224:225] op_sel:[0,1] op_sel_hi:[1,1]\n"
"v_pk_mul_f32 v[76:77], v[76:77], v[224:225] op_sel:[0,1] op_sel_hi:[1,1]\n"
"v_pk_mul_f32 v[78:79], v[78:79], v[224:225] op_sel:[0,1] op_sel_hi:[1,1]\n"
"v_cvt_pk_bf16_f32 v20, v72, v73\n"
"v_cvt_pk_bf16_f32 v21, v74, v75\n"
"v_cvt_pk_bf16_f32 v22, v76, v77\n"
"v_cvt_pk_bf16_f32 v23, v78, v79\n"
"buffer_store_dwordx4 v[20:23], %19, %4, 0 offen offset:3072 sc1\n"
"v_mfma_f32_16x16x32_bf16 v[214:217], v[210:213], v[16:19], v[214:217]\n"
"v_accvgpr_read_b32 v80, a48\n"
"v_accvgpr_read_b32 v81, a49\n"
"v_accvgpr_read_b32 v82, a50\n"
"v_accvgpr_read_b32 v83, a51\n"
"v_accvgpr_read_b32 v84, a52\n"
"v_accvgpr_read_b32 v85, a53\n"
"v_accvgpr_read_b32 v86, a54\n"
"v_accvgpr_read_b32 v87, a55\n"
"v_pk_mul_f32 v[80:81], v[80:81], v[226:227] op_sel:[0,0] op_sel_hi:[1,0]\n"
"v_pk_mul_f32 v[82:83], v[82:83], v[226:227] op_sel:[0,0] op_sel_hi:[1,0]\n"
"v_pk_mul_f32 v[84:85], v[84:85], v[226:227] op_sel:[0,0] op_sel_hi:[1,0]\n"
"v_pk_mul_f32 v[86:87], v[86:87], v[226:227] op_sel:[0,0] op_sel_hi:[1,0]\n"
"v_cvt_pk_bf16_f32 v24, v80, v81\n"
"v_cvt_pk_bf16_f32 v25, v82, v83\n"
"v_cvt_pk_bf16_f32 v26, v84, v85\n"
"v_cvt_pk_bf16_f32 v27, v86, v87\n"
"buffer_store_dwordx4 v[24:27], v219, %4, 0 offen offset:2048 sc1\n"
"v_mfma_f32_16x16x32_bf16 v[214:217], v[210:213], v[20:23], v[214:217]\n"
"v_accvgpr_read_b32 v88, a56\n"
"v_accvgpr_read_b32 v89, a57\n"
"v_accvgpr_read_b32 v90, a58\n"
"v_accvgpr_read_b32 v91, a59\n"
"v_accvgpr_read_b32 v92, a60\n"
"v_accvgpr_read_b32 v93, a61\n"
"v_accvgpr_read_b32 v94, a62\n"
"v_accvgpr_read_b32 v95, a63\n"
"v_pk_mul_f32 v[88:89], v[88:89], v[226:227] op_sel:[0,1] op_sel_hi:[1,1]\n"
"v_pk_mul_f32 v[90:91], v[90:91], v[226:227] op_sel:[0,1] op_sel_hi:[1,1]\n"
"v_pk_mul_f32 v[92:93], v[92:93], v[226:227] op_sel:[0,1] op_sel_hi:[1,1]\n"
"v_pk_mul_f32 v[94:95], v[94:95], v[226:227] op_sel:[0,1] op_sel_hi:[1,1]\n"
"v_cvt_pk_bf16_f32 v28, v88, v89\n"
"v_cvt_pk_bf16_f32 v29, v90, v91\n"
"v_cvt_pk_bf16_f32 v30, v92, v93\n"
"v_cvt_pk_bf16_f32 v31, v94, v95\n"
"buffer_store_dwordx4 v[28:31], v219, %4, 0 offen offset:3072 sc1\n"
"v_mfma_f32_16x16x32_bf16 v[214:217], v[210:213], v[24:27], v[214:217]\n"
"v_accvgpr_read_b32 v64, a64\n"
"v_accvgpr_read_b32 v65, a65\n"
"v_accvgpr_read_b32 v66, a66\n"
"v_accvgpr_read_b32 v67, a67\n"
"v_accvgpr_read_b32 v68, a68\n"
"v_accvgpr_read_b32 v69, a69\n"
"v_accvgpr_read_b32 v70, a70\n"
"v_accvgpr_read_b32 v71, a71\n"
"v_pk_mul_f32 v[64:65], v[64:65], v[224:225] op_sel:[0,0] op_sel_hi:[1,0]\n"
"v_pk_mul_f32 v[66:67], v[66:67], v[224:225] op_sel:[0,0] op_sel_hi:[1,0]\n"
"v_pk_mul_f32 v[68:69], v[68:69], v[224:225] op_sel:[0,0] op_sel_hi:[1,0]\n"
"v_pk_mul_f32 v[70:71], v[70:71], v[224:225] op_sel:[0,0] op_sel_hi:[1,0]\n"
"v_cvt_pk_bf16_f32 v32, v64, v65\n"
"v_cvt_pk_bf16_f32 v33, v66, v67\n"
"v_cvt_pk_bf16_f32 v34, v68, v69\n"
"v_cvt_pk_bf16_f32 v35, v70, v71\n"
"v_mfma_f32_16x16x32_bf16 v[214:217], v[210:213], v[28:31], v[214:217]\n"
"v_accvgpr_read_b32 v72, a72\n"
"v_accvgpr_read_b32 v73, a73\n"
"v_accvgpr_read_b32 v74, a74\n"
"v_accvgpr_read_b32 v75, a75\n"
"v_accvgpr_read_b32 v76, a76\n"
"v_accvgpr_read_b32 v77, a77\n"
"v_accvgpr_read_b32 v78, a78\n"
"v_accvgpr_read_b32 v79, a79\n"
"v_pk_mul_f32 v[72:73], v[72:73], v[224:225] op_sel:[0,1] op_sel_hi:[1,1]\n"
"v_pk_mul_f32 v[74:75], v[74:75], v[224:225] op_sel:[0,1] op_sel_hi:[1,1]\n"
"v_pk_mul_f32 v[76:77], v[76:77], v[224:225] op_sel:[0,1] op_sel_hi:[1,1]\n"
"v_pk_mul_f32 v[78:79], v[78:79], v[224:225] op_sel:[0,1] op_sel_hi:[1,1]\n"
"v_cvt_pk_bf16_f32 v36, v72, v73\n"
"v_cvt_pk_bf16_f32 v37, v74, v75\n"
"v_cvt_pk_bf16_f32 v38, v76, v77\n"
"v_cvt_pk_bf16_f32 v39, v78, v79\n"
"v_mfma_f32_16x16x32_bf16 v[214:217], v[210:213], v[32:35], v[214:217]\n"
"v_accvgpr_read_b32 v80, a80\n"
"v_accvgpr_read_b32 v81, a81\n"
"v_accvgpr_read_b32 v82, a82\n"
"v_accvgpr_read_b32 v83, a83\n"
"v_accvgpr_read_b32 v84, a84\n"
"v_accvgpr_read_b32 v85, a85\n"
"v_accvgpr_read_b32 v86, a86\n"
"v_accvgpr_read_b32 v87, a87\n"
"v_pk_mul_f32 v[80:81], v[80:81], v[226:227] op_sel:[0,0] op_sel_hi:[1,0]\n"
"v_pk_mul_f32 v[82:83], v[82:83], v[226:227] op_sel:[0,0] op_sel_hi:[1,0]\n"
"v_pk_mul_f32 v[84:85], v[84:85], v[226:227] op_sel:[0,0] op_sel_hi:[1,0]\n"
"v_pk_mul_f32 v[86:87], v[86:87], v[226:227] op_sel:[0,0] op_sel_hi:[1,0]\n"
"v_cvt_pk_bf16_f32 v40, v80, v81\n"
"v_cvt_pk_bf16_f32 v41, v82, v83\n"
"v_cvt_pk_bf16_f32 v42, v84, v85\n"
"v_cvt_pk_bf16_f32 v43, v86, v87\n"
"v_mfma_f32_16x16x32_bf16 v[214:217], v[210:213], v[36:39], v[214:217]\n"
"v_accvgpr_read_b32 v88, a88\n"
"v_accvgpr_read_b32 v89, a89\n"
"v_accvgpr_read_b32 v90, a90\n"
"v_accvgpr_read_b32 v91, a91\n"
"v_accvgpr_read_b32 v92, a92\n"
"v_accvgpr_read_b32 v93, a93\n"
"v_accvgpr_read_b32 v94, a94\n"
"v_accvgpr_read_b32 v95, a95\n"
"v_pk_mul_f32 v[88:89], v[88:89], v[226:227] op_sel:[0,1] op_sel_hi:[1,1]\n"
"v_pk_mul_f32 v[90:91], v[90:91], v[226:227] op_sel:[0,1] op_sel_hi:[1,1]\n"
"v_pk_mul_f32 v[92:93], v[92:93], v[226:227] op_sel:[0,1] op_sel_hi:[1,1]\n"
"v_pk_mul_f32 v[94:95], v[94:95], v[226:227] op_sel:[0,1] op_sel_hi:[1,1]\n"
"v_cvt_pk_bf16_f32 v44, v88, v89\n"
"v_cvt_pk_bf16_f32 v45, v90, v91\n"
"v_cvt_pk_bf16_f32 v46, v92, v93\n"
"v_cvt_pk_bf16_f32 v47, v94, v95\n"
"v_mfma_f32_16x16x32_bf16 v[214:217], v[210:213], v[40:43], v[214:217]\n"
"v_accvgpr_read_b32 v64, a96\n"
"v_accvgpr_read_b32 v65, a97\n"
"v_accvgpr_read_b32 v66, a98\n"
"v_accvgpr_read_b32 v67, a99\n"
"v_accvgpr_read_b32 v68, a100\n"
"v_accvgpr_read_b32 v69, a101\n"
"v_accvgpr_read_b32 v70, a102\n"
"v_accvgpr_read_b32 v71, a103\n"
"v_pk_mul_f32 v[64:65], v[64:65], v[224:225] op_sel:[0,0] op_sel_hi:[1,0]\n"
"v_pk_mul_f32 v[66:67], v[66:67], v[224:225] op_sel:[0,0] op_sel_hi:[1,0]\n"
"v_pk_mul_f32 v[68:69], v[68:69], v[224:225] op_sel:[0,0] op_sel_hi:[1,0]\n"
"v_pk_mul_f32 v[70:71], v[70:71], v[224:225] op_sel:[0,0] op_sel_hi:[1,0]\n"
"v_cvt_pk_bf16_f32 v48, v64, v65\n"
"v_cvt_pk_bf16_f32 v49, v66, v67\n"
"v_cvt_pk_bf16_f32 v50, v68, v69\n"
"v_cvt_pk_bf16_f32 v51, v70, v71\n"
"v_mfma_f32_16x16x32_bf16 v[214:217], v[210:213], v[44:47], v[214:217]\n"
"v_accvgpr_read_b32 v72, a104\n"
"v_accvgpr_read_b32 v73, a105\n"
"v_accvgpr_read_b32 v74, a106\n"
"v_accvgpr_read_b32 v75, a107\n"
"v_accvgpr_read_b32 v76, a108\n"
"v_accvgpr_read_b32 v77, a109\n"
"v_accvgpr_read_b32 v78, a110\n"
"v_accvgpr_read_b32 v79, a111\n"
"v_pk_mul_f32 v[72:73], v[72:73], v[224:225] op_sel:[0,1] op_sel_hi:[1,1]\n"
"v_pk_mul_f32 v[74:75], v[74:75], v[224:225] op_sel:[0,1] op_sel_hi:[1,1]\n"
"v_pk_mul_f32 v[76:77], v[76:77], v[224:225] op_sel:[0,1] op_sel_hi:[1,1]\n"
"v_pk_mul_f32 v[78:79], v[78:79], v[224:225] op_sel:[0,1] op_sel_hi:[1,1]\n"
"v_cvt_pk_bf16_f32 v52, v72, v73\n"
"v_cvt_pk_bf16_f32 v53, v74, v75\n"
"v_cvt_pk_bf16_f32 v54, v76, v77\n"
"v_cvt_pk_bf16_f32 v55, v78, v79\n"
"v_mfma_f32_16x16x32_bf16 v[214:217], v[210:213], v[48:51], v[214:217]\n"
"v_accvgpr_read_b32 v80, a112\n"
"v_accvgpr_read_b32 v81, a113\n"
"v_accvgpr_read_b32 v82, a114\n"
"v_accvgpr_read_b32 v83, a115\n"
"v_accvgpr_read_b32 v84, a116\n"
"v_accvgpr_read_b32 v85, a117\n"
"v_accvgpr_read_b32 v86, a118\n"
"v_accvgpr_read_b32 v87, a119\n"
"v_pk_mul_f32 v[80:81], v[80:81], v[226:227] op_sel:[0,0] op_sel_hi:[1,0]\n"
"v_pk_mul_f32 v[82:83], v[82:83], v[226:227] op_sel:[0,0] op_sel_hi:[1,0]\n"
"v_pk_mul_f32 v[84:85], v[84:85], v[226:227] op_sel:[0,0] op_sel_hi:[1,0]\n"
"v_pk_mul_f32 v[86:87], v[86:87], v[226:227] op_sel:[0,0] op_sel_hi:[1,0]\n"
"v_cvt_pk_bf16_f32 v56, v80, v81\n"
"v_cvt_pk_bf16_f32 v57, v82, v83\n"
"v_cvt_pk_bf16_f32 v58, v84, v85\n"
"v_cvt_pk_bf16_f32 v59, v86, v87\n"
"v_mfma_f32_16x16x32_bf16 v[214:217], v[210:213], v[52:55], v[214:217]\n"
"v_accvgpr_read_b32 v88, a120\n"
"v_accvgpr_read_b32 v89, a121\n"
"v_accvgpr_read_b32 v90, a122\n"
"v_accvgpr_read_b32 v91, a123\n"
"v_accvgpr_read_b32 v92, a124\n"
"v_accvgpr_read_b32 v93, a125\n"
"v_accvgpr_read_b32 v94, a126\n"
"v_accvgpr_read_b32 v95, a127\n"
"v_pk_mul_f32 v[88:89], v[88:89], v[226:227] op_sel:[0,1] op_sel_hi:[1,1]\n"
"v_pk_mul_f32 v[90:91], v[90:91], v[226:227] op_sel:[0,1] op_sel_hi:[1,1]\n"
"v_pk_mul_f32 v[92:93], v[92:93], v[226:227] op_sel:[0,1] op_sel_hi:[1,1]\n"
"v_pk_mul_f32 v[94:95], v[94:95], v[226:227] op_sel:[0,1] op_sel_hi:[1,1]\n"
"v_cvt_pk_bf16_f32 v60, v88, v89\n"
"v_cvt_pk_bf16_f32 v61, v90, v91\n"
"v_cvt_pk_bf16_f32 v62, v92, v93\n"
"v_cvt_pk_bf16_f32 v63, v94, v95\n"
"v_mfma_f32_16x16x32_bf16 v[214:217], v[210:213], v[56:59], v[214:217]\n"
"s_nop 0\n"
"v_mfma_f32_16x16x32_bf16 v[214:217], v[210:213], v[60:63], v[214:217]\n"
"v_mov_b32 v234, 0x7b800000\n"
"s_nop 7\n"
"v_add_f32 v232, v232, v214\n"
"v_cmp_nlt_f32 vcc, v232, v234\n"
"s_nop 3\n"
"s_cmp_lg_u64 vcc, 0\n"
"s_cselect_b32 s72, 1, 0\n"
"s_lshr_b32 s73, %11, 10\n"
"s_add_u32 s73, s73, 131072\n"
"v_mov_b32 v232, s72\n"
"v_mov_b32 v233, s73\n"
"v_mov_b32 v234, 131072\n"
"ds_write_b32 v233, v232\n"
"v_xor_b32 v196, 0, %14\n"
"v_xor_b32 v197, 32, %14\n"
"v_xor_b32 v198, 64, %14\n"
"v_xor_b32 v199, 96, %14\n"
"v_xor_b32 v200, 128, %14\n"
"v_xor_b32 v201, 160, %14\n"
"v_xor_b32 v202, 192, %14\n"
"v_xor_b32 v203, 224, %14\n"
"s_waitcnt vmcnt(28) lgkmcnt(0)\n"
"s_barrier\n"
"ds_read_b128 v[236:239], v234\n"
"ds_read_b64_tr_b16 a[224:225], v196 offset:0\n"
"ds_read_b64_tr_b16 a[226:227], v196 offset:4096\n"
"ds_read_b64_tr_b16 a[228:229], v196 offset:8192\n"
"ds_read_b64_tr_b16 a[230:231], v196 offset:12288\n"
"ds_read_b64_tr_b16 a[232:233], v197 offset:0\n"
"ds_read_b64_tr_b16 a[234:235], v197 offset:4096\n"
"ds_read_b64_tr_b16 a[236:237], v197 offset:8192\n"
"ds_read_b64_tr_b16 a[238:239], v197 offset:12288\n"
"s_waitcnt lgkmcnt(8)\n"
"v_or_b32 v236, v236, v237\n"
"v_or3_b32 v236, v236, v238, v239\n"
"s_nop 1\n"
"v_readfirstlane_b32 s72, v236\n"
"s_nop 3\n"
"s_andn2_b32 s72, s72, s69\n"
"s_cmp_lg_u32 s72, 0\n"
"s_cbranch_scc1 FALLBACK%=\n"
"v_lshrrev_b32 v193, 4, %19\n"
"v_add_u32 v193, -2, v193\n"
"v_cmp_lt_u32 vcc, 6, v193\n"
"s_lshl_b32 s67, %11, 3\n"
"s_nop 0\n"
"v_cndmask_b32 v194, 0, 1, vcc\n"
"v_mad_i32_i24 v193, v194, -7, v193\n"
"v_cmp_le_u32 vcc, %12, v193\n"
"s_mov_b32 s74, 2\n"
"s_mov_b32 s75, 2\n"
"v_addc_co_u32 v193, vcc, 0, v193, vcc\n"
"v_lshl_add_u32 v192, v193, 1, v194\n"
"s_nop 0\n"
"v_readlane_b32 s68, v192, 2\n"
"v_readlane_b32 s76, v192, 2\n"
"s_waitcnt lgkmcnt(4)\n"
"v_mfma_f32_16x16x32_bf16 v[64:67], a[224:227], v[0:3], 0\n"
"ds_read_b64_tr_b16 a[240:241], v198 offset:0\n"
"v_mfma_f32_16x16x32_bf16 v[64:67], a[228:231], v[16:19], v[64:67]\n"
"ds_read_b64_tr_b16 a[242:243], v198 offset:4096\n"
"v_mfma_f32_16x16x32_bf16 v[72:75], a[228:231], v[20:23], 0\n"
"v_mfma_f32_16x16x32_bf16 v[72:75], a[224:227], v[4:7], v[72:75]\n"
"s_lshl_b32 s64, s68, 17\n"
"v_mfma_f32_16x16x32_bf16 v[80:83], a[224:227], v[8:11], 0\n"
"ds_read_b64_tr_b16 a[244:245], v198 offset:8192\n"
"s_add_u32 s64, s64, s67\n"
"v_mfma_f32_16x16x32_bf16 v[80:83], a[228:231], v[24:27], v[80:83]\n"
"ds_read_b64_tr_b16 a[246:247], v198 offset:12288\n"
"s_add_u32 m0, %11, 98304\n"
"v_mfma_f32_16x16x32_bf16 v[88:91], a[228:231], v[28:31], 0\n"
"buffer_load_dwordx4 v220, %6, s64 offen offset:0 lds\n"
"v_mfma_f32_16x16x32_bf16 v[88:91], a[224:227], v[12:15], v[88:91]\n"
"s_waitcnt vmcnt(1)\n"
"s_barrier\n"
"s_cmp_lg_u32 %11, 0\n"
"s_cbranch_scc1 AO%=\n"
"v_lshlrev_b32 v234, 2, %12\n"
"v_mov_b32 v235, 1\n"
"s_mov_b64 exec, 1\n"
"global_store_dword v234, v235, %10 offset:0 sc1\n"
"s_mov_b64 exec, -1\n"
"AO%=:\n"
"s_waitcnt lgkmcnt(4)\n"
"v_mfma_f32_16x16x32_bf16 v[92:95], a[232:235], v[12:15], 0\n"
"ds_read_b64_tr_b16 a[248:249], v199 offset:0\n"
"s_add_u32 s74, s74, 1\n"
"v_mfma_f32_16x16x32_bf16 v[92:95], a[236:239], v[28:31], v[92:95]\n"
"ds_read_b64_tr_b16 a[250:251], v199 offset:4096\n"
"v_readlane_b32 s68, v192, s74\n"
"v_mfma_f32_16x16x32_bf16 v[84:87], a[236:239], v[24:27], 0\n"
"buffer_load_dwordx4 v221, %6, s64 offen offset:1024 lds\n"
"v_mfma_f32_16x16x32_bf16 v[84:87], a[232:235], v[8:11], v[84:87]\n"
"buffer_store_dwordx4 v[32:35], %19, %4, s70 offen offset:0 sc1\n"
"v_mfma_f32_16x16x32_bf16 v[76:79], a[232:235], v[4:7], 0\n"
"ds_read_b64_tr_b16 a[252:253], v199 offset:8192\n"
"v_mfma_f32_16x16x32_bf16 v[76:79], a[236:239], v[20:23], v[76:79]\n"
"ds_read_b64_tr_b16 a[254:255], v199 offset:12288\n"
"v_mfma_f32_16x16x32_bf16 v[68:71], a[236:239], v[16:19], 0\n"
"buffer_load_dwordx4 v222, %6, s64 offen offset:2048 lds\n"
"v_mfma_f32_16x16x32_bf16 v[68:71], a[232:235], v[0:3], v[68:71]\n"
"buffer_store_dwordx4 v[36:39], %19, %4, s70 offen offset:1024 sc1\n"
"s_waitcnt lgkmcnt(4)\n"
"v_mfma_f32_16x16x32_bf16 v[96:99], a[240:243], v[0:3], 0\n"
"ds_read_b64_tr_b16 a[224:225], v200 offset:0\n"
"v_mfma_f32_16x16x32_bf16 v[96:99], a[244:247], v[16:19], v[96:99]\n"
"ds_read_b64_tr_b16 a[226:227], v200 offset:4096\n"
"v_mfma_f32_16x16x32_bf16 v[104:107], a[244:247], v[20:23], 0\n"
"buffer_load_dwordx4 v223, %6, s64 offen offset:3072 lds\n"
"v_mfma_f32_16x16x32_bf16 v[104:107], a[240:243], v[4:7], v[104:107]\n"
"s_lshl_b32 s64, s68, 17\n"
"buffer_store_dwordx4 v[40:43], v219, %4, s70 offen offset:0 sc1\n"
"v_mfma_f32_16x16x32_bf16 v[112:115], a[240:243], v[8:11], 0\n"
"ds_read_b64_tr_b16 a[228:229], v200 offset:8192\n"
"s_add_u32 s64, s64, s67\n"
"v_mfma_f32_16x16x32_bf16 v[112:115], a[244:247], v[24:27], v[112:115]\n"
"ds_read_b64_tr_b16 a[230:231], v200 offset:12288\n"
"s_add_u32 m0, %11, 114688\n"
"v_mfma_f32_16x16x32_bf16 v[120:123], a[244:247], v[28:31], 0\n"
"buffer_load_dwordx4 v220, %6, s64 offen offset:0 lds\n"
"v_mfma_f32_16x16x32_bf16 v[120:123], a[240:243], v[12:15], v[120:123]\n"
"buffer_store_dwordx4 v[44:47], v219, %4, s70 offen offset:1024 sc1\n"
"s_waitcnt lgkmcnt(4)\n"
"v_mfma_f32_16x16x32_bf16 v[124:127], a[248:251], v[12:15], 0\n"
"ds_read_b64_tr_b16 a[232:233], v201 offset:0\n"
"s_add_u32 s74, s74, 1\n"
"v_mfma_f32_16x16x32_bf16 v[124:127], a[252:255], v[28:31], v[124:127]\n"
"ds_read_b64_tr_b16 a[234:235], v201 offset:4096\n"
"v_readlane_b32 s68, v192, s74\n"
"v_mfma_f32_16x16x32_bf16 v[116:119], a[252:255], v[24:27], 0\n"
"buffer_load_dwordx4 v221, %6, s64 offen offset:1024 lds\n"
"v_mfma_f32_16x16x32_bf16 v[116:119], a[248:251], v[8:11], v[116:119]\n"
"buffer_store_dwordx4 v[48:51], %19, %4, s70 offen offset:2048 sc1\n"
"v_mfma_f32_16x16x32_bf16 v[108:111], a[248:251], v[4:7], 0\n"
"ds_read_b64_tr_b16 a[236:237], v201 offset:8192\n"
"v_mfma_f32_16x16x32_bf16 v[108:111], a[252:255], v[20:23], v[108:111]\n"
"ds_read_b64_tr_b16 a[238:239], v201 offset:12288\n"
"v_mfma_f32_16x16x32_bf16 v[100:103], a[252:255], v[16:19], 0\n"
"buffer_load_dwordx4 v222, %6, s64 offen offset:2048 lds\n"
"v_mfma_f32_16x16x32_bf16 v[100:103], a[248:251], v[0:3], v[100:103]\n"
"buffer_store_dwordx4 v[52:55], %19, %4, s70 offen offset:3072 sc1\n"
"s_waitcnt lgkmcnt(4)\n"
"v_mfma_f32_16x16x32_bf16 v[128:131], a[224:227], v[0:3], 0\n"
"ds_read_b64_tr_b16 a[240:241], v202 offset:0\n"
"v_mfma_f32_16x16x32_bf16 v[128:131], a[228:231], v[16:19], v[128:131]\n"
"ds_read_b64_tr_b16 a[242:243], v202 offset:4096\n"
"v_mfma_f32_16x16x32_bf16 v[136:139], a[228:231], v[20:23], 0\n"
"buffer_load_dwordx4 v223, %6, s64 offen offset:3072 lds\n"
"v_mfma_f32_16x16x32_bf16 v[136:139], a[224:227], v[4:7], v[136:139]\n"
"buffer_store_dwordx4 v[56:59], v219, %4, s70 offen offset:2048 sc1\n"
"v_mfma_f32_16x16x32_bf16 v[144:147], a[224:227], v[8:11], 0\n"
"ds_read_b64_tr_b16 a[244:245], v202 offset:8192\n"
"v_mfma_f32_16x16x32_bf16 v[144:147], a[228:231], v[24:27], v[144:147]\n"
"ds_read_b64_tr_b16 a[246:247], v202 offset:12288\n"
"v_mfma_f32_16x16x32_bf16 v[152:155], a[228:231], v[28:31], 0\n"
"v_mfma_f32_16x16x32_bf16 v[152:155], a[224:227], v[12:15], v[152:155]\n"
"buffer_store_dwordx4 v[60:63], v219, %4, s70 offen offset:3072 sc1\n"
"s_waitcnt lgkmcnt(4)\n"
"v_mfma_f32_16x16x32_bf16 v[156:159], a[232:235], v[12:15], 0\n"
"ds_read_b64_tr_b16 a[248:249], v203 offset:0\n"
"v_mfma_f32_16x16x32_bf16 v[156:159], a[236:239], v[28:31], v[156:159]\n"
"ds_read_b64_tr_b16 a[250:251], v203 offset:4096\n"
"v_mfma_f32_16x16x32_bf16 v[148:151], a[236:239], v[24:27], 0\n"
"v_mfma_f32_16x16x32_bf16 v[148:151], a[232:235], v[8:11], v[148:151]\n"
"v_mfma_f32_16x16x32_bf16 v[140:143], a[232:235], v[4:7], 0\n"
"ds_read_b64_tr_b16 a[252:253], v203 offset:8192\n"
"v_mfma_f32_16x16x32_bf16 v[140:143], a[236:239], v[20:23], v[140:143]\n"
"ds_read_b64_tr_b16 a[254:255], v203 offset:12288\n"
"v_mfma_f32_16x16x32_bf16 v[132:135], a[236:239], v[16:19], 0\n"
"v_mfma_f32_16x16x32_bf16 v[132:135], a[232:235], v[0:3], v[132:135]\n"
"s_waitcnt lgkmcnt(4)\n"
"v_mfma_f32_16x16x32_bf16 v[160:163], a[240:243], v[0:3], 0\n"
"s_waitcnt vmcnt(40)\n"
"s_barrier\n"
"ds_read_b64_tr_b16 a[224:225], v196 offset:16384\n"
"v_mfma_f32_16x16x32_bf16 v[160:163], a[244:247], v[16:19], v[160:163]\n"
"ds_read_b64_tr_b16 a[226:227], v196 offset:20480\n"
"v_mfma_f32_16x16x32_bf16 v[168:171], a[244:247], v[20:23], 0\n"
"v_mfma_f32_16x16x32_bf16 v[168:171], a[240:243], v[4:7], v[168:171]\n"
"v_mfma_f32_16x16x32_bf16 v[176:179], a[240:243], v[8:11], 0\n"
"ds_read_b64_tr_b16 a[228:229], v196 offset:24576\n"
"v_mfma_f32_16x16x32_bf16 v[176:179], a[244:247], v[24:27], v[176:179]\n"
"ds_read_b64_tr_b16 a[230:231], v196 offset:28672\n"
"v_mfma_f32_16x16x32_bf16 v[184:187], a[244:247], v[28:31], 0\n"
"v_mfma_f32_16x16x32_bf16 v[184:187], a[240:243], v[12:15], v[184:187]\n"
"s_waitcnt lgkmcnt(4)\n"
"v_mfma_f32_16x16x32_bf16 v[188:191], a[248:251], v[12:15], 0\n"
"ds_read_b64_tr_b16 a[232:233], v197 offset:16384\n"
"v_mfma_f32_16x16x32_bf16 v[188:191], a[252:255], v[28:31], v[188:191]\n"
"ds_read_b64_tr_b16 a[234:235], v197 offset:20480\n"
"v_mfma_f32_16x16x32_bf16 v[180:183], a[252:255], v[24:27], 0\n"
"v_mfma_f32_16x16x32_bf16 v[180:183], a[248:251], v[8:11], v[180:183]\n"
"v_mfma_f32_16x16x32_bf16 v[172:175], a[248:251], v[4:7], 0\n"
"ds_read_b64_tr_b16 a[236:237], v197 offset:24576\n"
"v_mfma_f32_16x16x32_bf16 v[172:175], a[252:255], v[20:23], v[172:175]\n"
"ds_read_b64_tr_b16 a[238:239], v197 offset:28672\n"
"v_mfma_f32_16x16x32_bf16 v[164:167], a[252:255], v[16:19], 0\n"
"v_mfma_f32_16x16x32_bf16 v[164:167], a[248:251], v[0:3], v[164:167]\n"
"s_waitcnt lgkmcnt(4)\n"
"v_mfma_f32_16x16x32_bf16 v[64:67], a[224:227], v[32:35], v[64:67]\n"
"ds_read_b64_tr_b16 a[240:241], v198 offset:16384\n"
"v_mfma_f32_16x16x32_bf16 v[64:67], a[228:231], v[48:51], v[64:67]\n"
"ds_read_b64_tr_b16 a[242:243], v198 offset:20480\n"
"v_mfma_f32_16x16x32_bf16 v[72:75], a[228:231], v[52:55], v[72:75]\n"
"v_mfma_f32_16x16x32_bf16 v[72:75], a[224:227], v[36:39], v[72:75]\n"
"v_mfma_f32_16x16x32_bf16 v[80:83], a[224:227], v[40:43], v[80:83]\n"
"ds_read_b64_tr_b16 a[244:245], v198 offset:24576\n"
"v_mfma_f32_16x16x32_bf16 v[80:83], a[228:231], v[56:59], v[80:83]\n"
"ds_read_b64_tr_b16 a[246:247], v198 offset:28672\n"
"v_mfma_f32_16x16x32_bf16 v[88:91], a[228:231], v[60:63], v[88:91]\n"
"v_mfma_f32_16x16x32_bf16 v[88:91], a[224:227], v[44:47], v[88:91]\n"
"s_waitcnt lgkmcnt(4)\n"
"v_mfma_f32_16x16x32_bf16 v[92:95], a[232:235], v[44:47], v[92:95]\n"
"ds_read_b64_tr_b16 a[248:249], v199 offset:16384\n"
"v_mfma_f32_16x16x32_bf16 v[92:95], a[236:239], v[60:63], v[92:95]\n"
"ds_read_b64_tr_b16 a[250:251], v199 offset:20480\n"
"v_mfma_f32_16x16x32_bf16 v[84:87], a[236:239], v[56:59], v[84:87]\n"
"v_mfma_f32_16x16x32_bf16 v[84:87], a[232:235], v[40:43], v[84:87]\n"
"v_mfma_f32_16x16x32_bf16 v[76:79], a[232:235], v[36:39], v[76:79]\n"
"ds_read_b64_tr_b16 a[252:253], v199 offset:24576\n"
"v_mfma_f32_16x16x32_bf16 v[76:79], a[236:239], v[52:55], v[76:79]\n"
"ds_read_b64_tr_b16 a[254:255], v199 offset:28672\n"
"v_mfma_f32_16x16x32_bf16 v[68:71], a[236:239], v[48:51], v[68:71]\n"
"v_mfma_f32_16x16x32_bf16 v[68:71], a[232:235], v[32:35], v[68:71]\n"
"s_waitcnt lgkmcnt(4)\n"
"v_mfma_f32_16x16x32_bf16 v[96:99], a[240:243], v[32:35], v[96:99]\n"
"ds_read_b64_tr_b16 a[224:225], v200 offset:16384\n"
"v_mfma_f32_16x16x32_bf16 v[96:99], a[244:247], v[48:51], v[96:99]\n"
"ds_read_b64_tr_b16 a[226:227], v200 offset:20480\n"
"v_mfma_f32_16x16x32_bf16 v[104:107], a[244:247], v[52:55], v[104:107]\n"
"v_mfma_f32_16x16x32_bf16 v[104:107], a[240:243], v[36:39], v[104:107]\n"
"v_mfma_f32_16x16x32_bf16 v[112:115], a[240:243], v[40:43], v[112:115]\n"
"ds_read_b64_tr_b16 a[228:229], v200 offset:24576\n"
"v_mfma_f32_16x16x32_bf16 v[112:115], a[244:247], v[56:59], v[112:115]\n"
"ds_read_b64_tr_b16 a[230:231], v200 offset:28672\n"
"v_mfma_f32_16x16x32_bf16 v[120:123], a[244:247], v[60:63], v[120:123]\n"
"v_mfma_f32_16x16x32_bf16 v[120:123], a[240:243], v[44:47], v[120:123]\n"
"s_waitcnt lgkmcnt(4)\n"
"v_mfma_f32_16x16x32_bf16 v[124:127], a[248:251], v[44:47], v[124:127]\n"
"ds_read_b64_tr_b16 a[232:233], v201 offset:16384\n"
"v_mfma_f32_16x16x32_bf16 v[124:127], a[252:255], v[60:63], v[124:127]\n"
"ds_read_b64_tr_b16 a[234:235], v201 offset:20480\n"
"v_mfma_f32_16x16x32_bf16 v[116:119], a[252:255], v[56:59], v[116:119]\n"
"v_mfma_f32_16x16x32_bf16 v[116:119], a[248:251], v[40:43], v[116:119]\n"
"s_mov_b32 s71, 0x40000\n"
"v_bfe_u32 v233, %19, 2, 5\n"
"PO%=:\n"
"global_load_dword v232, v233, %10 offset:0 sc1\n"
"s_waitcnt vmcnt(0)\n"
"v_cmp_eq_u32 vcc, 0, v232\n"
"s_sub_u32 s71, s71, 1\n"
"s_cbranch_scc1 POX%=\n"
"s_cmp_eq_u64 vcc, 0\n"
"s_cbranch_scc1 POX%=\n"
"s_sleep 2\n"
"s_branch PO%=\n"
"POX%=:\n"
"s_lshl_b32 s65, s76, 12\n"
"s_add_u32 s75, s75, 1\n"
"v_readlane_b32 s76, v192, s75\n"
"buffer_load_dwordx4 a[64:67], v218, %5, s65 offen offset:0 sc1\n"
"buffer_load_dwordx4 a[68:71], v218, %5, s65 offen offset:1024 sc1\n"
"buffer_load_dwordx4 a[72:75], v219, %5, s65 offen offset:0 sc1\n"
"buffer_load_dwordx4 a[76:79], v219, %5, s65 offen offset:1024 sc1\n"
"buffer_load_dwordx4 a[80:83], v218, %5, s65 offen offset:2048 sc1\n"
"buffer_load_dwordx4 a[84:87], v218, %5, s65 offen offset:3072 sc1\n"
"buffer_load_dwordx4 a[88:91], v219, %5, s65 offen offset:2048 sc1\n"
"buffer_load_dwordx4 a[92:95], v219, %5, s65 offen offset:3072 sc1\n"
"v_mfma_f32_16x16x32_bf16 v[108:111], a[248:251], v[36:39], v[108:111]\n"
"ds_read_b64_tr_b16 a[236:237], v201 offset:24576\n"
"s_lshl_b32 s65, s76, 12\n"
"s_add_u32 s75, s75, 1\n"
"v_readlane_b32 s76, v192, s75\n"
"buffer_load_dwordx4 a[96:99], v218, %5, s65 offen offset:0 sc1\n"
"v_mfma_f32_16x16x32_bf16 v[108:111], a[252:255], v[52:55], v[108:111]\n"
"ds_read_b64_tr_b16 a[238:239], v201 offset:28672\n"
"v_mfma_f32_16x16x32_bf16 v[100:103], a[252:255], v[48:51], v[100:103]\n"
"buffer_load_dwordx4 a[100:103], v218, %5, s65 offen offset:1024 sc1\n"
"v_mfma_f32_16x16x32_bf16 v[100:103], a[248:251], v[32:35], v[100:103]\n"
"s_waitcnt lgkmcnt(4)\n"
"v_mfma_f32_16x16x32_bf16 v[128:131], a[224:227], v[32:35], v[128:131]\n"
"ds_read_b64_tr_b16 a[240:241], v202 offset:16384\n"
"buffer_load_dwordx4 a[104:107], v219, %5, s65 offen offset:0 sc1\n"
"v_mfma_f32_16x16x32_bf16 v[128:131], a[228:231], v[48:51], v[128:131]\n"
"ds_read_b64_tr_b16 a[242:243], v202 offset:20480\n"
"v_mfma_f32_16x16x32_bf16 v[136:139], a[228:231], v[52:55], v[136:139]\n"
"buffer_load_dwordx4 a[108:111], v219, %5, s65 offen offset:1024 sc1\n"
"v_mfma_f32_16x16x32_bf16 v[136:139], a[224:227], v[36:39], v[136:139]\n"
"v_mfma_f32_16x16x32_bf16 v[144:147], a[224:227], v[40:43], v[144:147]\n"
"ds_read_b64_tr_b16 a[244:245], v202 offset:24576\n"
"buffer_load_dwordx4 a[112:115], v218, %5, s65 offen offset:2048 sc1\n"
"v_mfma_f32_16x16x32_bf16 v[144:147], a[228:231], v[56:59], v[144:147]\n"
"ds_read_b64_tr_b16 a[246:247], v202 offset:28672\n"
"v_mfma_f32_16x16x32_bf16 v[152:155], a[228:231], v[60:63], v[152:155]\n"
"buffer_load_dwordx4 a[116:119], v218, %5, s65 offen offset:3072 sc1\n"
"v_mfma_f32_16x16x32_bf16 v[152:155], a[224:227], v[44:47], v[152:155]\n"
"s_waitcnt lgkmcnt(4)\n"
"v_mfma_f32_16x16x32_bf16 v[156:159], a[232:235], v[44:47], v[156:159]\n"
"ds_read_b64_tr_b16 a[248:249], v203 offset:16384\n"
"buffer_load_dwordx4 a[120:123], v219, %5, s65 offen offset:2048 sc1\n"
"v_mfma_f32_16x16x32_bf16 v[156:159], a[236:239], v[60:63], v[156:159]\n"
"ds_read_b64_tr_b16 a[250:251], v203 offset:20480\n"
"v_mfma_f32_16x16x32_bf16 v[148:151], a[236:239], v[56:59], v[148:151]\n"
"buffer_load_dwordx4 a[124:127], v219, %5, s65 offen offset:3072 sc1\n"
"v_mfma_f32_16x16x32_bf16 v[148:151], a[232:235], v[40:43], v[148:151]\n"
"v_mfma_f32_16x16x32_bf16 v[140:143], a[232:235], v[36:39], v[140:143]\n"
"ds_read_b64_tr_b16 a[252:253], v203 offset:24576\n"
"s_lshl_b32 s65, s76, 12\n"
"s_add_u32 s75, s75, 1\n"
"v_readlane_b32 s76, v192, s75\n"
"buffer_load_dwordx4 a[0:3], v218, %5, s65 offen offset:0 sc1\n"
"v_mfma_f32_16x16x32_bf16 v[140:143], a[236:239], v[52:55], v[140:143]\n"
"ds_read_b64_tr_b16 a[254:255], v203 offset:28672\n"
"v_mfma_f32_16x16x32_bf16 v[132:135], a[236:239], v[48:51], v[132:135]\n"
"buffer_load_dwordx4 a[4:7], v218, %5, s65 offen offset:1024 sc1\n"
"v_mfma_f32_16x16x32_bf16 v[132:135], a[232:235], v[32:35], v[132:135]\n"
"s_waitcnt lgkmcnt(4)\n"
"v_mfma_f32_16x16x32_bf16 v[160:163], a[240:243], v[32:35], v[160:163]\n"
"s_waitcnt vmcnt(19)\n"
"s_barrier\n"
"s_cmp_lg_u32 %11, 0\n"
"s_cbranch_scc1 AP%=\n"
"v_lshlrev_b32 v234, 2, %12\n"
"v_mov_b32 v235, 1\n"
"s_mov_b64 exec, 1\n"
"global_store_dword v234, v235, %10 offset:32 sc1\n"
"s_mov_b64 exec, -1\n"
"AP%=:\n"
"ds_read_b64_tr_b16 a[224:225], v196 offset:32768\n"
"buffer_load_dwordx4 a[8:11], v219, %5, s65 offen offset:0 sc1\n"
"v_mfma_f32_16x16x32_bf16 v[160:163], a[244:247], v[48:51], v[160:163]\n"
"ds_read_b64_tr_b16 a[226:227], v196 offset:36864\n"
"v_mfma_f32_16x16x32_bf16 v[168:171], a[244:247], v[52:55], v[168:171]\n"
"buffer_load_dwordx4 a[12:15], v219, %5, s65 offen offset:1024 sc1\n"
"v_mfma_f32_16x16x32_bf16 v[168:171], a[240:243], v[36:39], v[168:171]\n"
"v_mfma_f32_16x16x32_bf16 v[176:179], a[240:243], v[40:43], v[176:179]\n"
"ds_read_b64_tr_b16 a[228:229], v196 offset:40960\n"
"buffer_load_dwordx4 a[16:19], v218, %5, s65 offen offset:2048 sc1\n"
"v_mfma_f32_16x16x32_bf16 v[176:179], a[244:247], v[56:59], v[176:179]\n"
"ds_read_b64_tr_b16 a[230:231], v196 offset:45056\n"
"v_mfma_f32_16x16x32_bf16 v[184:187], a[244:247], v[60:63], v[184:187]\n"
"buffer_load_dwordx4 a[20:23], v218, %5, s65 offen offset:3072 sc1\n"
"v_mfma_f32_16x16x32_bf16 v[184:187], a[240:243], v[44:47], v[184:187]\n"
"s_waitcnt lgkmcnt(4)\n"
"v_mfma_f32_16x16x32_bf16 v[188:191], a[248:251], v[44:47], v[188:191]\n"
"ds_read_b64_tr_b16 a[232:233], v197 offset:32768\n"
"buffer_load_dwordx4 a[24:27], v219, %5, s65 offen offset:2048 sc1\n"
"v_mfma_f32_16x16x32_bf16 v[188:191], a[252:255], v[60:63], v[188:191]\n"
"ds_read_b64_tr_b16 a[234:235], v197 offset:36864\n"
"v_mfma_f32_16x16x32_bf16 v[180:183], a[252:255], v[56:59], v[180:183]\n"
"buffer_load_dwordx4 a[28:31], v219, %5, s65 offen offset:3072 sc1\n"
"v_mfma_f32_16x16x32_bf16 v[180:183], a[248:251], v[40:43], v[180:183]\n"
"v_mfma_f32_16x16x32_bf16 v[172:175], a[248:251], v[36:39], v[172:175]\n"
"ds_read_b64_tr_b16 a[236:237], v197 offset:40960\n"
"v_mfma_f32_16x16x32_bf16 v[172:175], a[252:255], v[52:55], v[172:175]\n"
"ds_read_b64_tr_b16 a[238:239], v197 offset:45056\n"
"v_mfma_f32_16x16x32_bf16 v[164:167], a[252:255], v[48:51], v[164:167]\n"
"v_mfma_f32_16x16x32_bf16 v[164:167], a[248:251], v[32:35], v[164:167]\n"
"s_mov_b32 s66, 2\n"
"LOOP_C%=:\n"
"s_waitcnt vmcnt(16) lgkmcnt(4)\n"
"v_mfma_f32_16x16x32_bf16 v[64:67], a[224:227], a[64:67], v[64:67]\n"
"ds_read_b64_tr_b16 a[240:241], v198 offset:32768\n"
"v_mfma_f32_16x16x32_bf16 v[64:67], a[228:231], a[80:83], v[64:67]\n"
"ds_read_b64_tr_b16 a[242:243], v198 offset:36864\n"
"v_mfma_f32_16x16x32_bf16 v[72:75], a[228:231], a[84:87], v[72:75]\n"
"v_mfma_f32_16x16x32_bf16 v[72:75], a[224:227], a[68:71], v[72:75]\n"
"s_lshl_b32 s64, s68, 17\n"
"v_mfma_f32_16x16x32_bf16 v[80:83], a[224:227], a[72:75], v[80:83]\n"
"ds_read_b64_tr_b16 a[244:245], v198 offset:40960\n"
"s_add_u32 s64, s64, s67\n"
"v_mfma_f32_16x16x32_bf16 v[80:83], a[228:231], a[88:91], v[80:83]\n"
"ds_read_b64_tr_b16 a[246:247], v198 offset:45056\n"
"s_add_u32 m0, %11, 65536\n"
"v_mfma_f32_16x16x32_bf16 v[88:91], a[228:231], a[92:95], v[88:91]\n"
"buffer_load_dwordx4 v220, %6, s64 offen offset:0 lds\n"
"v_mfma_f32_16x16x32_bf16 v[88:91], a[224:227], a[76:79], v[88:91]\n"
"s_waitcnt lgkmcnt(4)\n"
"v_mfma_f32_16x16x32_bf16 v[92:95], a[232:235], a[76:79], v[92:95]\n"
"ds_read_b64_tr_b16 a[248:249], v199 offset:32768\n"
"s_add_u32 s74, s74, 1\n"
"v_mfma_f32_16x16x32_bf16 v[92:95], a[236:239], a[92:95], v[92:95]\n"
"ds_read_b64_tr_b16 a[250:251], v199 offset:36864\n"
"v_readlane_b32 s68, v192, s74\n"
"v_mfma_f32_16x16x32_bf16 v[84:87], a[236:239], a[88:91], v[84:87]\n"
"buffer_load_dwordx4 v221, %6, s64 offen offset:1024 lds\n"
"v_mfma_f32_16x16x32_bf16 v[84:87], a[232:235], a[72:75], v[84:87]\n"
"v_mfma_f32_16x16x32_bf16 v[76:79], a[232:235], a[68:71], v[76:79]\n"
"ds_read_b64_tr_b16 a[252:253], v199 offset:40960\n"
"v_mfma_f32_16x16x32_bf16 v[76:79], a[236:239], a[84:87], v[76:79]\n"
"ds_read_b64_tr_b16 a[254:255], v199 offset:45056\n"
"v_mfma_f32_16x16x32_bf16 v[68:71], a[236:239], a[80:83], v[68:71]\n"
"buffer_load_dwordx4 v222, %6, s64 offen offset:2048 lds\n"
"v_mfma_f32_16x16x32_bf16 v[68:71], a[232:235], a[64:67], v[68:71]\n"
"s_waitcnt lgkmcnt(4)\n"
"v_mfma_f32_16x16x32_bf16 v[96:99], a[240:243], a[64:67], v[96:99]\n"
"ds_read_b64_tr_b16 a[224:225], v200 offset:32768\n"
"v_mfma_f32_16x16x32_bf16 v[96:99], a[244:247], a[80:83], v[96:99]\n"
"ds_read_b64_tr_b16 a[226:227], v200 offset:36864\n"
"v_mfma_f32_16x16x32_bf16 v[104:107], a[244:247], a[84:87], v[104:107]\n"
"buffer_load_dwordx4 v223, %6, s64 offen offset:3072 lds\n"
"v_mfma_f32_16x16x32_bf16 v[104:107], a[240:243], a[68:71], v[104:107]\n"
"v_mfma_f32_16x16x32_bf16 v[112:115], a[240:243], a[72:75], v[112:115]\n"
"ds_read_b64_tr_b16 a[228:229], v200 offset:40960\n"
"v_mfma_f32_16x16x32_bf16 v[112:115], a[244:247], a[88:91], v[112:115]\n"
"ds_read_b64_tr_b16 a[230:231], v200 offset:45056\n"
"v_mfma_f32_16x16x32_bf16 v[120:123], a[244:247], a[92:95], v[120:123]\n"
"v_mfma_f32_16x16x32_bf16 v[120:123], a[240:243], a[76:79], v[120:123]\n"
"s_waitcnt lgkmcnt(4)\n"
"v_mfma_f32_16x16x32_bf16 v[124:127], a[248:251], a[76:79], v[124:127]\n"
"ds_read_b64_tr_b16 a[232:233], v201 offset:32768\n"
"v_mfma_f32_16x16x32_bf16 v[124:127], a[252:255], a[92:95], v[124:127]\n"
"ds_read_b64_tr_b16 a[234:235], v201 offset:36864\n"
"s_lshl_b32 s65, s76, 12\n"
"v_mfma_f32_16x16x32_bf16 v[116:119], a[252:255], a[88:91], v[116:119]\n"
"buffer_load_dwordx4 a[32:35], v218, %5, s65 offen offset:0 sc1\n"
"v_mfma_f32_16x16x32_bf16 v[116:119], a[248:251], a[72:75], v[116:119]\n"
"v_mfma_f32_16x16x32_bf16 v[108:111], a[248:251], a[68:71], v[108:111]\n"
"ds_read_b64_tr_b16 a[236:237], v201 offset:40960\n"
"s_add_u32 s75, s75, 1\n"
"v_mfma_f32_16x16x32_bf16 v[108:111], a[252:255], a[84:87], v[108:111]\n"
"ds_read_b64_tr_b16 a[238:239], v201 offset:45056\n"
"v_readlane_b32 s76, v192, s75\n"
"v_mfma_f32_16x16x32_bf16 v[100:103], a[252:255], a[80:83], v[100:103]\n"
"buffer_load_dwordx4 a[36:39], v218, %5, s65 offen offset:1024 sc1\n"
"v_mfma_f32_16x16x32_bf16 v[100:103], a[248:251], a[64:67], v[100:103]\n"
"s_waitcnt lgkmcnt(4)\n"
"v_mfma_f32_16x16x32_bf16 v[128:131], a[224:227], a[64:67], v[128:131]\n"
"ds_read_b64_tr_b16 a[240:241], v202 offset:32768\n"
"v_mfma_f32_16x16x32_bf16 v[128:131], a[228:231], a[80:83], v[128:131]\n"
"ds_read_b64_tr_b16 a[242:243], v202 offset:36864\n"
"v_mfma_f32_16x16x32_bf16 v[136:139], a[228:231], a[84:87], v[136:139]\n"
"buffer_load_dwordx4 a[40:43], v219, %5, s65 offen offset:0 sc1\n"
"v_mfma_f32_16x16x32_bf16 v[136:139], a[224:227], a[68:71], v[136:139]\n"
"v_mfma_f32_16x16x32_bf16 v[144:147], a[224:227], a[72:75], v[144:147]\n"
"ds_read_b64_tr_b16 a[244:245], v202 offset:40960\n"
"v_mfma_f32_16x16x32_bf16 v[144:147], a[228:231], a[88:91], v[144:147]\n"
"ds_read_b64_tr_b16 a[246:247], v202 offset:45056\n"
"v_mfma_f32_16x16x32_bf16 v[152:155], a[228:231], a[92:95], v[152:155]\n"
"buffer_load_dwordx4 a[44:47], v219, %5, s65 offen offset:1024 sc1\n"
"v_mfma_f32_16x16x32_bf16 v[152:155], a[224:227], a[76:79], v[152:155]\n"
"s_waitcnt lgkmcnt(4)\n"
"v_mfma_f32_16x16x32_bf16 v[156:159], a[232:235], a[76:79], v[156:159]\n"
"ds_read_b64_tr_b16 a[248:249], v203 offset:32768\n"
"v_mfma_f32_16x16x32_bf16 v[156:159], a[236:239], a[92:95], v[156:159]\n"
"ds_read_b64_tr_b16 a[250:251], v203 offset:36864\n"
"v_mfma_f32_16x16x32_bf16 v[148:151], a[236:239], a[88:91], v[148:151]\n"
"v_mfma_f32_16x16x32_bf16 v[148:151], a[232:235], a[72:75], v[148:151]\n"
"v_mfma_f32_16x16x32_bf16 v[140:143], a[232:235], a[68:71], v[140:143]\n"
"ds_read_b64_tr_b16 a[252:253], v203 offset:40960\n"
"v_mfma_f32_16x16x32_bf16 v[140:143], a[236:239], a[84:87], v[140:143]\n"
"ds_read_b64_tr_b16 a[254:255], v203 offset:45056\n"
"v_mfma_f32_16x16x32_bf16 v[132:135], a[236:239], a[80:83], v[132:135]\n"
"buffer_load_dwordx4 a[48:51], v218, %5, s65 offen offset:2048 sc1\n"
"v_mfma_f32_16x16x32_bf16 v[132:135], a[232:235], a[64:67], v[132:135]\n"
"s_waitcnt lgkmcnt(4)\n"
"v_mfma_f32_16x16x32_bf16 v[160:163], a[240:243], a[64:67], v[160:163]\n"
"s_waitcnt vmcnt(17)\n"
"s_barrier\n"
"ds_read_b64_tr_b16 a[224:225], v196 offset:49152\n"
"v_mfma_f32_16x16x32_bf16 v[160:163], a[244:247], a[80:83], v[160:163]\n"
"ds_read_b64_tr_b16 a[226:227], v196 offset:53248\n"
"v_mfma_f32_16x16x32_bf16 v[168:171], a[244:247], a[84:87], v[168:171]\n"
"buffer_load_dwordx4 a[52:55], v218, %5, s65 offen offset:3072 sc1\n"
"v_mfma_f32_16x16x32_bf16 v[168:171], a[240:243], a[68:71], v[168:171]\n"
"v_mfma_f32_16x16x32_bf16 v[176:179], a[240:243], a[72:75], v[176:179]\n"
"ds_read_b64_tr_b16 a[228:229], v196 offset:57344\n"
"v_mfma_f32_16x16x32_bf16 v[176:179], a[244:247], a[88:91], v[176:179]\n"
"ds_read_b64_tr_b16 a[230:231], v196 offset:61440\n"
"v_mfma_f32_16x16x32_bf16 v[184:187], a[244:247], a[92:95], v[184:187]\n"
"buffer_load_dwordx4 a[56:59], v219, %5, s65 offen offset:2048 sc1\n"
"v_mfma_f32_16x16x32_bf16 v[184:187], a[240:243], a[76:79], v[184:187]\n"
"s_waitcnt lgkmcnt(4)\n"
"v_mfma_f32_16x16x32_bf16 v[188:191], a[248:251], a[76:79], v[188:191]\n"
"ds_read_b64_tr_b16 a[232:233], v197 offset:49152\n"
"v_mfma_f32_16x16x32_bf16 v[188:191], a[252:255], a[92:95], v[188:191]\n"
"ds_read_b64_tr_b16 a[234:235], v197 offset:53248\n"
"v_mfma_f32_16x16x32_bf16 v[180:183], a[252:255], a[88:91], v[180:183]\n"
"buffer_load_dwordx4 a[60:63], v219, %5, s65 offen offset:3072 sc1\n"
"v_mfma_f32_16x16x32_bf16 v[180:183], a[248:251], a[72:75], v[180:183]\n"
"v_mfma_f32_16x16x32_bf16 v[172:175], a[248:251], a[68:71], v[172:175]\n"
"ds_read_b64_tr_b16 a[236:237], v197 offset:57344\n"
"v_mfma_f32_16x16x32_bf16 v[172:175], a[252:255], a[84:87], v[172:175]\n"
"ds_read_b64_tr_b16 a[238:239], v197 offset:61440\n"
"v_mfma_f32_16x16x32_bf16 v[164:167], a[252:255], a[80:83], v[164:167]\n"
"v_mfma_f32_16x16x32_bf16 v[164:167], a[248:251], a[64:67], v[164:167]\n"
"s_waitcnt vmcnt(20) lgkmcnt(4)\n"
"v_mfma_f32_16x16x32_bf16 v[64:67], a[224:227], a[96:99], v[64:67]\n"
"ds_read_b64_tr_b16 a[240:241], v198 offset:49152\n"
"v_mfma_f32_16x16x32_bf16 v[64:67], a[228:231], a[112:115], v[64:67]\n"
"ds_read_b64_tr_b16 a[242:243], v198 offset:53248\n"
"v_mfma_f32_16x16x32_bf16 v[72:75], a[228:231], a[116:119], v[72:75]\n"
"v_mfma_f32_16x16x32_bf16 v[72:75], a[224:227], a[100:103], v[72:75]\n"
"s_lshl_b32 s64, s68, 17\n"
"v_mfma_f32_16x16x32_bf16 v[80:83], a[224:227], a[104:107], v[80:83]\n"
"ds_read_b64_tr_b16 a[244:245], v198 offset:57344\n"
"s_add_u32 s64, s64, s67\n"
"v_mfma_f32_16x16x32_bf16 v[80:83], a[228:231], a[120:123], v[80:83]\n"
"ds_read_b64_tr_b16 a[246:247], v198 offset:61440\n"
"s_add_u32 m0, %11, 81920\n"
"v_mfma_f32_16x16x32_bf16 v[88:91], a[228:231], a[124:127], v[88:91]\n"
"buffer_load_dwordx4 v220, %6, s64 offen offset:0 lds\n"
"v_mfma_f32_16x16x32_bf16 v[88:91], a[224:227], a[108:111], v[88:91]\n"
"s_waitcnt lgkmcnt(4)\n"
"v_mfma_f32_16x16x32_bf16 v[92:95], a[232:235], a[108:111], v[92:95]\n"
"ds_read_b64_tr_b16 a[248:249], v199 offset:49152\n"
"s_add_u32 s74, s74, 1\n"
"v_mfma_f32_16x16x32_bf16 v[92:95], a[236:239], a[124:127], v[92:95]\n"
"ds_read_b64_tr_b16 a[250:251], v199 offset:53248\n"
"v_readlane_b32 s68, v192, s74\n"
"v_mfma_f32_16x16x32_bf16 v[84:87], a[236:239], a[120:123], v[84:87]\n"
"buffer_load_dwordx4 v221, %6, s64 offen offset:1024 lds\n"
"v_mfma_f32_16x16x32_bf16 v[84:87], a[232:235], a[104:107], v[84:87]\n"
"v_mfma_f32_16x16x32_bf16 v[76:79], a[232:235], a[100:103], v[76:79]\n"
"ds_read_b64_tr_b16 a[252:253], v199 offset:57344\n"
"v_mfma_f32_16x16x32_bf16 v[76:79], a[236:239], a[116:119], v[76:79]\n"
"ds_read_b64_tr_b16 a[254:255], v199 offset:61440\n"
"v_mfma_f32_16x16x32_bf16 v[68:71], a[236:239], a[112:115], v[68:71]\n"
"buffer_load_dwordx4 v222, %6, s64 offen offset:2048 lds\n"
"v_mfma_f32_16x16x32_bf16 v[68:71], a[232:235], a[96:99], v[68:71]\n"
"s_waitcnt lgkmcnt(4)\n"
"v_mfma_f32_16x16x32_bf16 v[96:99], a[240:243], a[96:99], v[96:99]\n"
"ds_read_b64_tr_b16 a[224:225], v200 offset:49152\n"
"v_mfma_f32_16x16x32_bf16 v[96:99], a[244:247], a[112:115], v[96:99]\n"
"ds_read_b64_tr_b16 a[226:227], v200 offset:53248\n"
"v_mfma_f32_16x16x32_bf16 v[104:107], a[244:247], a[116:119], v[104:107]\n"
"buffer_load_dwordx4 v223, %6, s64 offen offset:3072 lds\n"
"v_mfma_f32_16x16x32_bf16 v[104:107], a[240:243], a[100:103], v[104:107]\n"
"v_mfma_f32_16x16x32_bf16 v[112:115], a[240:243], a[104:107], v[112:115]\n"
"ds_read_b64_tr_b16 a[228:229], v200 offset:57344\n"
"v_mfma_f32_16x16x32_bf16 v[112:115], a[244:247], a[120:123], v[112:115]\n"
"ds_read_b64_tr_b16 a[230:231], v200 offset:61440\n"
"v_mfma_f32_16x16x32_bf16 v[120:123], a[244:247], a[124:127], v[120:123]\n"
"v_mfma_f32_16x16x32_bf16 v[120:123], a[240:243], a[108:111], v[120:123]\n"
"s_waitcnt lgkmcnt(4)\n"
"v_mfma_f32_16x16x32_bf16 v[124:127], a[248:251], a[108:111], v[124:127]\n"
"ds_read_b64_tr_b16 a[232:233], v201 offset:49152\n"
"v_mfma_f32_16x16x32_bf16 v[124:127], a[252:255], a[124:127], v[124:127]\n"
"ds_read_b64_tr_b16 a[234:235], v201 offset:53248\n"
"s_lshl_b32 s65, s76, 12\n"
"v_mfma_f32_16x16x32_bf16 v[116:119], a[252:255], a[120:123], v[116:119]\n"
"buffer_load_dwordx4 a[64:67], v218, %5, s65 offen offset:0 sc1\n"
"v_mfma_f32_16x16x32_bf16 v[116:119], a[248:251], a[104:107], v[116:119]\n"
"v_mfma_f32_16x16x32_bf16 v[108:111], a[248:251], a[100:103], v[108:111]\n"
"ds_read_b64_tr_b16 a[236:237], v201 offset:57344\n"
"s_add_u32 s75, s75, 1\n"
"v_mfma_f32_16x16x32_bf16 v[108:111], a[252:255], a[116:119], v[108:111]\n"
"ds_read_b64_tr_b16 a[238:239], v201 offset:61440\n"
"v_readlane_b32 s76, v192, s75\n"
"v_mfma_f32_16x16x32_bf16 v[100:103], a[252:255], a[112:115], v[100:103]\n"
"buffer_load_dwordx4 a[68:71], v218, %5, s65 offen offset:1024 sc1\n"
"v_mfma_f32_16x16x32_bf16 v[100:103], a[248:251], a[96:99], v[100:103]\n"
"s_waitcnt lgkmcnt(4)\n"
"v_mfma_f32_16x16x32_bf16 v[128:131], a[224:227], a[96:99], v[128:131]\n"
"ds_read_b64_tr_b16 a[240:241], v202 offset:49152\n"
"v_mfma_f32_16x16x32_bf16 v[128:131], a[228:231], a[112:115], v[128:131]\n"
"ds_read_b64_tr_b16 a[242:243], v202 offset:53248\n"
"v_mfma_f32_16x16x32_bf16 v[136:139], a[228:231], a[116:119], v[136:139]\n"
"buffer_load_dwordx4 a[72:75], v219, %5, s65 offen offset:0 sc1\n"
"v_mfma_f32_16x16x32_bf16 v[136:139], a[224:227], a[100:103], v[136:139]\n"
"v_mfma_f32_16x16x32_bf16 v[144:147], a[224:227], a[104:107], v[144:147]\n"
"ds_read_b64_tr_b16 a[244:245], v202 offset:57344\n"
"v_mfma_f32_16x16x32_bf16 v[144:147], a[228:231], a[120:123], v[144:147]\n"
"ds_read_b64_tr_b16 a[246:247], v202 offset:61440\n"
"v_mfma_f32_16x16x32_bf16 v[152:155], a[228:231], a[124:127], v[152:155]\n"
"buffer_load_dwordx4 a[76:79], v219, %5, s65 offen offset:1024 sc1\n"
"v_mfma_f32_16x16x32_bf16 v[152:155], a[224:227], a[108:111], v[152:155]\n"
"s_waitcnt lgkmcnt(4)\n"
"v_mfma_f32_16x16x32_bf16 v[156:159], a[232:235], a[108:111], v[156:159]\n"
"ds_read_b64_tr_b16 a[248:249], v203 offset:49152\n"
"v_mfma_f32_16x16x32_bf16 v[156:159], a[236:239], a[124:127], v[156:159]\n"
"ds_read_b64_tr_b16 a[250:251], v203 offset:53248\n"
"v_mfma_f32_16x16x32_bf16 v[148:151], a[236:239], a[120:123], v[148:151]\n"
"v_mfma_f32_16x16x32_bf16 v[148:151], a[232:235], a[104:107], v[148:151]\n"
"v_mfma_f32_16x16x32_bf16 v[140:143], a[232:235], a[100:103], v[140:143]\n"
"ds_read_b64_tr_b16 a[252:253], v203 offset:57344\n"
"v_mfma_f32_16x16x32_bf16 v[140:143], a[236:239], a[116:119], v[140:143]\n"
"ds_read_b64_tr_b16 a[254:255], v203 offset:61440\n"
"v_mfma_f32_16x16x32_bf16 v[132:135], a[236:239], a[112:115], v[132:135]\n"
"buffer_load_dwordx4 a[80:83], v218, %5, s65 offen offset:2048 sc1\n"
"v_mfma_f32_16x16x32_bf16 v[132:135], a[232:235], a[96:99], v[132:135]\n"
"s_waitcnt lgkmcnt(4)\n"
"v_mfma_f32_16x16x32_bf16 v[160:163], a[240:243], a[96:99], v[160:163]\n"
"s_waitcnt vmcnt(17)\n"
"s_barrier\n"
"ds_read_b64_tr_b16 a[224:225], v196 offset:0\n"
"v_mfma_f32_16x16x32_bf16 v[160:163], a[244:247], a[112:115], v[160:163]\n"
"ds_read_b64_tr_b16 a[226:227], v196 offset:4096\n"
"v_mfma_f32_16x16x32_bf16 v[168:171], a[244:247], a[116:119], v[168:171]\n"
"buffer_load_dwordx4 a[84:87], v218, %5, s65 offen offset:3072 sc1\n"
"v_mfma_f32_16x16x32_bf16 v[168:171], a[240:243], a[100:103], v[168:171]\n"
"v_mfma_f32_16x16x32_bf16 v[176:179], a[240:243], a[104:107], v[176:179]\n"
"ds_read_b64_tr_b16 a[228:229], v196 offset:8192\n"
"v_mfma_f32_16x16x32_bf16 v[176:179], a[244:247], a[120:123], v[176:179]\n"
"ds_read_b64_tr_b16 a[230:231], v196 offset:12288\n"
"v_mfma_f32_16x16x32_bf16 v[184:187], a[244:247], a[124:127], v[184:187]\n"
"buffer_load_dwordx4 a[88:91], v219, %5, s65 offen offset:2048 sc1\n"
"v_mfma_f32_16x16x32_bf16 v[184:187], a[240:243], a[108:111], v[184:187]\n"
"s_waitcnt lgkmcnt(4)\n"
"v_mfma_f32_16x16x32_bf16 v[188:191], a[248:251], a[108:111], v[188:191]\n"
"ds_read_b64_tr_b16 a[232:233], v197 offset:0\n"
"v_mfma_f32_16x16x32_bf16 v[188:191], a[252:255], a[124:127], v[188:191]\n"
"ds_read_b64_tr_b16 a[234:235], v197 offset:4096\n"
"v_mfma_f32_16x16x32_bf16 v[180:183], a[252:255], a[120:123], v[180:183]\n"
"buffer_load_dwordx4 a[92:95], v219, %5, s65 offen offset:3072 sc1\n"
"v_mfma_f32_16x16x32_bf16 v[180:183], a[248:251], a[104:107], v[180:183]\n"
"v_mfma_f32_16x16x32_bf16 v[172:175], a[248:251], a[100:103], v[172:175]\n"
"ds_read_b64_tr_b16 a[236:237], v197 offset:8192\n"
"v_mfma_f32_16x16x32_bf16 v[172:175], a[252:255], a[116:119], v[172:175]\n"
"ds_read_b64_tr_b16 a[238:239], v197 offset:12288\n"
"v_mfma_f32_16x16x32_bf16 v[164:167], a[252:255], a[112:115], v[164:167]\n"
"v_mfma_f32_16x16x32_bf16 v[164:167], a[248:251], a[96:99], v[164:167]\n"
"s_waitcnt vmcnt(24) lgkmcnt(4)\n"
"v_mfma_f32_16x16x32_bf16 v[64:67], a[224:227], a[0:3], v[64:67]\n"
"ds_read_b64_tr_b16 a[240:241], v198 offset:0\n"
"v_mfma_f32_16x16x32_bf16 v[64:67], a[228:231], a[16:19], v[64:67]\n"
"ds_read_b64_tr_b16 a[242:243], v198 offset:4096\n"
"v_mfma_f32_16x16x32_bf16 v[72:75], a[228:231], a[20:23], v[72:75]\n"
"global_load_dword v232, v233, %10 offset:32 sc1\n"
"v_mfma_f32_16x16x32_bf16 v[72:75], a[224:227], a[4:7], v[72:75]\n"
"s_lshl_b32 s64, s68, 17\n"
"v_mfma_f32_16x16x32_bf16 v[80:83], a[224:227], a[8:11], v[80:83]\n"
"ds_read_b64_tr_b16 a[244:245], v198 offset:8192\n"
"s_add_u32 s64, s64, s67\n"
"v_mfma_f32_16x16x32_bf16 v[80:83], a[228:231], a[24:27], v[80:83]\n"
"ds_read_b64_tr_b16 a[246:247], v198 offset:12288\n"
"s_add_u32 m0, %11, 98304\n"
"v_mfma_f32_16x16x32_bf16 v[88:91], a[228:231], a[28:31], v[88:91]\n"
"buffer_load_dwordx4 v220, %6, s64 offen offset:0 lds\n"
"v_mfma_f32_16x16x32_bf16 v[88:91], a[224:227], a[12:15], v[88:91]\n"
"s_waitcnt lgkmcnt(4)\n"
"v_mfma_f32_16x16x32_bf16 v[92:95], a[232:235], a[12:15], v[92:95]\n"
"ds_read_b64_tr_b16 a[248:249], v199 offset:0\n"
"s_add_u32 s74, s74, 1\n"
"v_mfma_f32_16x16x32_bf16 v[92:95], a[236:239], a[28:31], v[92:95]\n"
"ds_read_b64_tr_b16 a[250:251], v199 offset:4096\n"
"v_readlane_b32 s68, v192, s74\n"
"v_mfma_f32_16x16x32_bf16 v[84:87], a[236:239], a[24:27], v[84:87]\n"
"buffer_load_dwordx4 v221, %6, s64 offen offset:1024 lds\n"
"v_mfma_f32_16x16x32_bf16 v[84:87], a[232:235], a[8:11], v[84:87]\n"
"v_mfma_f32_16x16x32_bf16 v[76:79], a[232:235], a[4:7], v[76:79]\n"
"ds_read_b64_tr_b16 a[252:253], v199 offset:8192\n"
"v_mfma_f32_16x16x32_bf16 v[76:79], a[236:239], a[20:23], v[76:79]\n"
"ds_read_b64_tr_b16 a[254:255], v199 offset:12288\n"
"v_mfma_f32_16x16x32_bf16 v[68:71], a[236:239], a[16:19], v[68:71]\n"
"buffer_load_dwordx4 v222, %6, s64 offen offset:2048 lds\n"
"v_mfma_f32_16x16x32_bf16 v[68:71], a[232:235], a[0:3], v[68:71]\n"
"s_waitcnt lgkmcnt(4)\n"
"v_mfma_f32_16x16x32_bf16 v[96:99], a[240:243], a[0:3], v[96:99]\n"
"ds_read_b64_tr_b16 a[224:225], v200 offset:0\n"
"v_mfma_f32_16x16x32_bf16 v[96:99], a[244:247], a[16:19], v[96:99]\n"
"ds_read_b64_tr_b16 a[226:227], v200 offset:4096\n"
"v_mfma_f32_16x16x32_bf16 v[104:107], a[244:247], a[20:23], v[104:107]\n"
"buffer_load_dwordx4 v223, %6, s64 offen offset:3072 lds\n"
"v_mfma_f32_16x16x32_bf16 v[104:107], a[240:243], a[4:7], v[104:107]\n"
"v_mfma_f32_16x16x32_bf16 v[112:115], a[240:243], a[8:11], v[112:115]\n"
"ds_read_b64_tr_b16 a[228:229], v200 offset:8192\n"
"v_mfma_f32_16x16x32_bf16 v[112:115], a[244:247], a[24:27], v[112:115]\n"
"ds_read_b64_tr_b16 a[230:231], v200 offset:12288\n"
"v_mfma_f32_16x16x32_bf16 v[120:123], a[244:247], a[28:31], v[120:123]\n"
"v_mfma_f32_16x16x32_bf16 v[120:123], a[240:243], a[12:15], v[120:123]\n"
"s_waitcnt lgkmcnt(4)\n"
"v_mfma_f32_16x16x32_bf16 v[124:127], a[248:251], a[12:15], v[124:127]\n"
"ds_read_b64_tr_b16 a[232:233], v201 offset:0\n"
"v_mfma_f32_16x16x32_bf16 v[124:127], a[252:255], a[28:31], v[124:127]\n"
"ds_read_b64_tr_b16 a[234:235], v201 offset:4096\n"
"s_lshl_b32 s65, s76, 12\n"
"v_mfma_f32_16x16x32_bf16 v[116:119], a[252:255], a[24:27], v[116:119]\n"
"buffer_load_dwordx4 a[96:99], v218, %5, s65 offen offset:0 sc1\n"
"v_mfma_f32_16x16x32_bf16 v[116:119], a[248:251], a[8:11], v[116:119]\n"
"v_mfma_f32_16x16x32_bf16 v[108:111], a[248:251], a[4:7], v[108:111]\n"
"ds_read_b64_tr_b16 a[236:237], v201 offset:8192\n"
"s_add_u32 s75, s75, 1\n"
"v_mfma_f32_16x16x32_bf16 v[108:111], a[252:255], a[20:23], v[108:111]\n"
"ds_read_b64_tr_b16 a[238:239], v201 offset:12288\n"
"v_readlane_b32 s76, v192, s75\n"
"v_mfma_f32_16x16x32_bf16 v[100:103], a[252:255], a[16:19], v[100:103]\n"
"buffer_load_dwordx4 a[100:103], v218, %5, s65 offen offset:1024 sc1\n"
"v_mfma_f32_16x16x32_bf16 v[100:103], a[248:251], a[0:3], v[100:103]\n"
"s_waitcnt lgkmcnt(4)\n"
"v_mfma_f32_16x16x32_bf16 v[128:131], a[224:227], a[0:3], v[128:131]\n"
"ds_read_b64_tr_b16 a[240:241], v202 offset:0\n"
"v_mfma_f32_16x16x32_bf16 v[128:131], a[228:231], a[16:19], v[128:131]\n"
"ds_read_b64_tr_b16 a[242:243], v202 offset:4096\n"
"v_mfma_f32_16x16x32_bf16 v[136:139], a[228:231], a[20:23], v[136:139]\n"
"buffer_load_dwordx4 a[104:107], v219, %5, s65 offen offset:0 sc1\n"
"v_mfma_f32_16x16x32_bf16 v[136:139], a[224:227], a[4:7], v[136:139]\n"
"v_mfma_f32_16x16x32_bf16 v[144:147], a[224:227], a[8:11], v[144:147]\n"
"ds_read_b64_tr_b16 a[244:245], v202 offset:8192\n"
"v_mfma_f32_16x16x32_bf16 v[144:147], a[228:231], a[24:27], v[144:147]\n"
"ds_read_b64_tr_b16 a[246:247], v202 offset:12288\n"
"v_mfma_f32_16x16x32_bf16 v[152:155], a[228:231], a[28:31], v[152:155]\n"
"buffer_load_dwordx4 a[108:111], v219, %5, s65 offen offset:1024 sc1\n"
"v_mfma_f32_16x16x32_bf16 v[152:155], a[224:227], a[12:15], v[152:155]\n"
"s_waitcnt lgkmcnt(4)\n"
"v_mfma_f32_16x16x32_bf16 v[156:159], a[232:235], a[12:15], v[156:159]\n"
"ds_read_b64_tr_b16 a[248:249], v203 offset:0\n"
"v_mfma_f32_16x16x32_bf16 v[156:159], a[236:239], a[28:31], v[156:159]\n"
"ds_read_b64_tr_b16 a[250:251], v203 offset:4096\n"
"v_mfma_f32_16x16x32_bf16 v[148:151], a[236:239], a[24:27], v[148:151]\n"
"v_mfma_f32_16x16x32_bf16 v[148:151], a[232:235], a[8:11], v[148:151]\n"
"v_mfma_f32_16x16x32_bf16 v[140:143], a[232:235], a[4:7], v[140:143]\n"
"ds_read_b64_tr_b16 a[252:253], v203 offset:8192\n"
"v_mfma_f32_16x16x32_bf16 v[140:143], a[236:239], a[20:23], v[140:143]\n"
"ds_read_b64_tr_b16 a[254:255], v203 offset:12288\n"
"v_mfma_f32_16x16x32_bf16 v[132:135], a[236:239], a[16:19], v[132:135]\n"
"buffer_load_dwordx4 a[112:115], v218, %5, s65 offen offset:2048 sc1\n"
"v_mfma_f32_16x16x32_bf16 v[132:135], a[232:235], a[0:3], v[132:135]\n"
"s_waitcnt lgkmcnt(4)\n"
"v_mfma_f32_16x16x32_bf16 v[160:163], a[240:243], a[0:3], v[160:163]\n"
"s_waitcnt vmcnt(18)\n"
"s_barrier\n"
"ds_read_b64_tr_b16 a[224:225], v196 offset:16384\n"
"v_mfma_f32_16x16x32_bf16 v[160:163], a[244:247], a[16:19], v[160:163]\n"
"ds_read_b64_tr_b16 a[226:227], v196 offset:20480\n"
"v_mfma_f32_16x16x32_bf16 v[168:171], a[244:247], a[20:23], v[168:171]\n"
"buffer_load_dwordx4 a[116:119], v218, %5, s65 offen offset:3072 sc1\n"
"v_mfma_f32_16x16x32_bf16 v[168:171], a[240:243], a[4:7], v[168:171]\n"
"v_mfma_f32_16x16x32_bf16 v[176:179], a[240:243], a[8:11], v[176:179]\n"
"ds_read_b64_tr_b16 a[228:229], v196 offset:24576\n"
"v_mfma_f32_16x16x32_bf16 v[176:179], a[244:247], a[24:27], v[176:179]\n"
"ds_read_b64_tr_b16 a[230:231], v196 offset:28672\n"
"v_mfma_f32_16x16x32_bf16 v[184:187], a[244:247], a[28:31], v[184:187]\n"
"buffer_load_dwordx4 a[120:123], v219, %5, s65 offen offset:2048 sc1\n"
"v_mfma_f32_16x16x32_bf16 v[184:187], a[240:243], a[12:15], v[184:187]\n"
"s_waitcnt lgkmcnt(4)\n"
"v_mfma_f32_16x16x32_bf16 v[188:191], a[248:251], a[12:15], v[188:191]\n"
"ds_read_b64_tr_b16 a[232:233], v197 offset:16384\n"
"v_mfma_f32_16x16x32_bf16 v[188:191], a[252:255], a[28:31], v[188:191]\n"
"ds_read_b64_tr_b16 a[234:235], v197 offset:20480\n"
"v_mfma_f32_16x16x32_bf16 v[180:183], a[252:255], a[24:27], v[180:183]\n"
"buffer_load_dwordx4 a[124:127], v219, %5, s65 offen offset:3072 sc1\n"
"v_mfma_f32_16x16x32_bf16 v[180:183], a[248:251], a[8:11], v[180:183]\n"
"v_mfma_f32_16x16x32_bf16 v[172:175], a[248:251], a[4:7], v[172:175]\n"
"ds_read_b64_tr_b16 a[236:237], v197 offset:24576\n"
"v_mfma_f32_16x16x32_bf16 v[172:175], a[252:255], a[20:23], v[172:175]\n"
"ds_read_b64_tr_b16 a[238:239], v197 offset:28672\n"
"v_mfma_f32_16x16x32_bf16 v[164:167], a[252:255], a[16:19], v[164:167]\n"
"s_waitcnt vmcnt(12)\n"
"v_cmp_eq_u32 vcc, 0, v232\n"
"v_mfma_f32_16x16x32_bf16 v[164:167], a[248:251], a[0:3], v[164:167]\n"
"s_cbranch_vccnz LOS%=\n"
"LOB%=:\n"
"s_waitcnt vmcnt(25) lgkmcnt(4)\n"
"v_mfma_f32_16x16x32_bf16 v[64:67], a[224:227], a[32:35], v[64:67]\n"
"ds_read_b64_tr_b16 a[240:241], v198 offset:16384\n"
"v_mfma_f32_16x16x32_bf16 v[64:67], a[228:231], a[48:51], v[64:67]\n"
"ds_read_b64_tr_b16 a[242:243], v198 offset:20480\n"
"v_mfma_f32_16x16x32_bf16 v[72:75], a[228:231], a[52:55], v[72:75]\n"
"v_mfma_f32_16x16x32_bf16 v[72:75], a[224:227], a[36:39], v[72:75]\n"
"s_lshl_b32 s64, s68, 17\n"
"v_mfma_f32_16x16x32_bf16 v[80:83], a[224:227], a[40:43], v[80:83]\n"
"ds_read_b64_tr_b16 a[244:245], v198 offset:24576\n"
"s_add_u32 s64, s64, s67\n"
"v_mfma_f32_16x16x32_bf16 v[80:83], a[228:231], a[56:59], v[80:83]\n"
"ds_read_b64_tr_b16 a[246:247], v198 offset:28672\n"
"s_add_u32 m0, %11, 114688\n"
"v_mfma_f32_16x16x32_bf16 v[88:91], a[228:231], a[60:63], v[88:91]\n"
"buffer_load_dwordx4 v220, %6, s64 offen offset:0 lds\n"
"v_mfma_f32_16x16x32_bf16 v[88:91], a[224:227], a[44:47], v[88:91]\n"
"s_waitcnt lgkmcnt(4)\n"
"v_mfma_f32_16x16x32_bf16 v[92:95], a[232:235], a[44:47], v[92:95]\n"
"ds_read_b64_tr_b16 a[248:249], v199 offset:16384\n"
"s_add_u32 s74, s74, 1\n"
"v_mfma_f32_16x16x32_bf16 v[92:95], a[236:239], a[60:63], v[92:95]\n"
"ds_read_b64_tr_b16 a[250:251], v199 offset:20480\n"
"v_readlane_b32 s68, v192, s74\n"
"v_mfma_f32_16x16x32_bf16 v[84:87], a[236:239], a[56:59], v[84:87]\n"
"buffer_load_dwordx4 v221, %6, s64 offen offset:1024 lds\n"
"v_mfma_f32_16x16x32_bf16 v[84:87], a[232:235], a[40:43], v[84:87]\n"
"v_mfma_f32_16x16x32_bf16 v[76:79], a[232:235], a[36:39], v[76:79]\n"
"ds_read_b64_tr_b16 a[252:253], v199 offset:24576\n"
"v_mfma_f32_16x16x32_bf16 v[76:79], a[236:239], a[52:55], v[76:79]\n"
"ds_read_b64_tr_b16 a[254:255], v199 offset:28672\n"
"v_mfma_f32_16x16x32_bf16 v[68:71], a[236:239], a[48:51], v[68:71]\n"
"buffer_load_dwordx4 v222, %6, s64 offen offset:2048 lds\n"
"v_mfma_f32_16x16x32_bf16 v[68:71], a[232:235], a[32:35], v[68:71]\n"
"s_waitcnt lgkmcnt(4)\n"
"v_mfma_f32_16x16x32_bf16 v[96:99], a[240:243], a[32:35], v[96:99]\n"
"ds_read_b64_tr_b16 a[224:225], v200 offset:16384\n"
"v_mfma_f32_16x16x32_bf16 v[96:99], a[244:247], a[48:51], v[96:99]\n"
"ds_read_b64_tr_b16 a[226:227], v200 offset:20480\n"
"v_mfma_f32_16x16x32_bf16 v[104:107], a[244:247], a[52:55], v[104:107]\n"
"buffer_load_dwordx4 v223, %6, s64 offen offset:3072 lds\n"
"v_mfma_f32_16x16x32_bf16 v[104:107], a[240:243], a[36:39], v[104:107]\n"
"v_mfma_f32_16x16x32_bf16 v[112:115], a[240:243], a[40:43], v[112:115]\n"
"ds_read_b64_tr_b16 a[228:229], v200 offset:24576\n"
"v_mfma_f32_16x16x32_bf16 v[112:115], a[244:247], a[56:59], v[112:115]\n"
"ds_read_b64_tr_b16 a[230:231], v200 offset:28672\n"
"v_mfma_f32_16x16x32_bf16 v[120:123], a[244:247], a[60:63], v[120:123]\n"
"v_mfma_f32_16x16x32_bf16 v[120:123], a[240:243], a[44:47], v[120:123]\n"
"s_waitcnt lgkmcnt(4)\n"
"v_mfma_f32_16x16x32_bf16 v[124:127], a[248:251], a[44:47], v[124:127]\n"
"ds_read_b64_tr_b16 a[232:233], v201 offset:16384\n"
"v_mfma_f32_16x16x32_bf16 v[124:127], a[252:255], a[60:63], v[124:127]\n"
"ds_read_b64_tr_b16 a[234:235], v201 offset:20480\n"
"s_lshl_b32 s65, s76, 12\n"
"v_mfma_f32_16x16x32_bf16 v[116:119], a[252:255], a[56:59], v[116:119]\n"
"buffer_load_dwordx4 a[0:3], v218, %5, s65 offen offset:0 sc1\n"
"v_mfma_f32_16x16x32_bf16 v[116:119], a[248:251], a[40:43], v[116:119]\n"
"v_mfma_f32_16x16x32_bf16 v[108:111], a[248:251], a[36:39], v[108:111]\n"
"ds_read_b64_tr_b16 a[236:237], v201 offset:24576\n"
"s_add_u32 s75, s75, 1\n"
"v_mfma_f32_16x16x32_bf16 v[108:111], a[252:255], a[52:55], v[108:111]\n"
"ds_read_b64_tr_b16 a[238:239], v201 offset:28672\n"
"v_readlane_b32 s76, v192, s75\n"
"v_mfma_f32_16x16x32_bf16 v[100:103], a[252:255], a[48:51], v[100:103]\n"
"buffer_load_dwordx4 a[4:7], v218, %5, s65 offen offset:1024 sc1\n"
"v_mfma_f32_16x16x32_bf16 v[100:103], a[248:251], a[32:35], v[100:103]\n"
"s_waitcnt lgkmcnt(4)\n"
"v_mfma_f32_16x16x32_bf16 v[128:131], a[224:227], a[32:35], v[128:131]\n"
"ds_read_b64_tr_b16 a[240:241], v202 offset:16384\n"
"v_mfma_f32_16x16x32_bf16 v[128:131], a[228:231], a[48:51], v[128:131]\n"
"ds_read_b64_tr_b16 a[242:243], v202 offset:20480\n"
"v_mfma_f32_16x16x32_bf16 v[136:139], a[228:231], a[52:55], v[136:139]\n"
"buffer_load_dwordx4 a[8:11], v219, %5, s65 offen offset:0 sc1\n"
"v_mfma_f32_16x16x32_bf16 v[136:139], a[224:227], a[36:39], v[136:139]\n"
"v_mfma_f32_16x16x32_bf16 v[144:147], a[224:227], a[40:43], v[144:147]\n"
"ds_read_b64_tr_b16 a[244:245], v202 offset:24576\n"
"v_mfma_f32_16x16x32_bf16 v[144:147], a[228:231], a[56:59], v[144:147]\n"
"ds_read_b64_tr_b16 a[246:247], v202 offset:28672\n"
"v_mfma_f32_16x16x32_bf16 v[152:155], a[228:231], a[60:63], v[152:155]\n"
"buffer_load_dwordx4 a[12:15], v219, %5, s65 offen offset:1024 sc1\n"
"v_mfma_f32_16x16x32_bf16 v[152:155], a[224:227], a[44:47], v[152:155]\n"
"s_waitcnt lgkmcnt(4)\n"
"v_mfma_f32_16x16x32_bf16 v[156:159], a[232:235], a[44:47], v[156:159]\n"
"ds_read_b64_tr_b16 a[248:249], v203 offset:16384\n"
"v_mfma_f32_16x16x32_bf16 v[156:159], a[236:239], a[60:63], v[156:159]\n"
"ds_read_b64_tr_b16 a[250:251], v203 offset:20480\n"
"v_mfma_f32_16x16x32_bf16 v[148:151], a[236:239], a[56:59], v[148:151]\n"
"v_mfma_f32_16x16x32_bf16 v[148:151], a[232:235], a[40:43], v[148:151]\n"
"v_mfma_f32_16x16x32_bf16 v[140:143], a[232:235], a[36:39], v[140:143]\n"
"ds_read_b64_tr_b16 a[252:253], v203 offset:24576\n"
"v_mfma_f32_16x16x32_bf16 v[140:143], a[236:239], a[52:55], v[140:143]\n"
"ds_read_b64_tr_b16 a[254:255], v203 offset:28672\n"
"v_mfma_f32_16x16x32_bf16 v[132:135], a[236:239], a[48:51], v[132:135]\n"
"buffer_load_dwordx4 a[16:19], v218, %5, s65 offen offset:2048 sc1\n"
"v_mfma_f32_16x16x32_bf16 v[132:135], a[232:235], a[32:35], v[132:135]\n"
"s_waitcnt lgkmcnt(4)\n"
"v_mfma_f32_16x16x32_bf16 v[160:163], a[240:243], a[32:35], v[160:163]\n"
"s_waitcnt vmcnt(17)\n"
"s_barrier\n"
"ds_read_b64_tr_b16 a[224:225], v196 offset:32768\n"
"v_mfma_f32_16x16x32_bf16 v[160:163], a[244:247], a[48:51], v[160:163]\n"
"ds_read_b64_tr_b16 a[226:227], v196 offset:36864\n"
"v_mfma_f32_16x16x32_bf16 v[168:171], a[244:247], a[52:55], v[168:171]\n"
"buffer_load_dwordx4 a[20:23], v218, %5, s65 offen offset:3072 sc1\n"
"v_mfma_f32_16x16x32_bf16 v[168:171], a[240:243], a[36:39], v[168:171]\n"
"v_mfma_f32_16x16x32_bf16 v[176:179], a[240:243], a[40:43], v[176:179]\n"
"ds_read_b64_tr_b16 a[228:229], v196 offset:40960\n"
"v_mfma_f32_16x16x32_bf16 v[176:179], a[244:247], a[56:59], v[176:179]\n"
"ds_read_b64_tr_b16 a[230:231], v196 offset:45056\n"
"v_mfma_f32_16x16x32_bf16 v[184:187], a[244:247], a[60:63], v[184:187]\n"
"buffer_load_dwordx4 a[24:27], v219, %5, s65 offen offset:2048 sc1\n"
"v_mfma_f32_16x16x32_bf16 v[184:187], a[240:243], a[44:47], v[184:187]\n"
"s_waitcnt lgkmcnt(4)\n"
"v_mfma_f32_16x16x32_bf16 v[188:191], a[248:251], a[44:47], v[188:191]\n"
"ds_read_b64_tr_b16 a[232:233], v197 offset:32768\n"
"v_mfma_f32_16x16x32_bf16 v[188:191], a[252:255], a[60:63], v[188:191]\n"
"ds_read_b64_tr_b16 a[234:235], v197 offset:36864\n"
"v_mfma_f32_16x16x32_bf16 v[180:183], a[252:255], a[56:59], v[180:183]\n"
"buffer_load_dwordx4 a[28:31], v219, %5, s65 offen offset:3072 sc1\n"
"v_mfma_f32_16x16x32_bf16 v[180:183], a[248:251], a[40:43], v[180:183]\n"
"s_sub_u32 s66, s66, 1\n"
"v_mfma_f32_16x16x32_bf16 v[172:175], a[248:251], a[36:39], v[172:175]\n"
"ds_read_b64_tr_b16 a[236:237], v197 offset:40960\n"
"v_mfma_f32_16x16x32_bf16 v[172:175], a[252:255], a[52:55], v[172:175]\n"
"ds_read_b64_tr_b16 a[238:239], v197 offset:45056\n"
"v_mfma_f32_16x16x32_bf16 v[164:167], a[252:255], a[48:51], v[164:167]\n"
"v_mfma_f32_16x16x32_bf16 v[164:167], a[248:251], a[32:35], v[164:167]\n"
"s_cmp_lg_u32 s66, 0\n"
"s_cbranch_scc1 LOOP_C%=\n"
"s_waitcnt vmcnt(25) lgkmcnt(4)\n"
"v_mfma_f32_16x16x32_bf16 v[64:67], a[224:227], a[64:67], v[64:67]\n"
"ds_read_b64_tr_b16 a[240:241], v198 offset:32768\n"
"v_mfma_f32_16x16x32_bf16 v[64:67], a[228:231], a[80:83], v[64:67]\n"
"ds_read_b64_tr_b16 a[242:243], v198 offset:36864\n"
"v_mfma_f32_16x16x32_bf16 v[72:75], a[228:231], a[84:87], v[72:75]\n"
"v_mfma_f32_16x16x32_bf16 v[72:75], a[224:227], a[68:71], v[72:75]\n"
"s_lshl_b32 s64, s68, 17\n"
"v_mfma_f32_16x16x32_bf16 v[80:83], a[224:227], a[72:75], v[80:83]\n"
"ds_read_b64_tr_b16 a[244:245], v198 offset:40960\n"
"s_add_u32 s64, s64, s67\n"
"v_mfma_f32_16x16x32_bf16 v[80:83], a[228:231], a[88:91], v[80:83]\n"
"ds_read_b64_tr_b16 a[246:247], v198 offset:45056\n"
"s_add_u32 m0, %11, 65536\n"
"v_mfma_f32_16x16x32_bf16 v[88:91], a[228:231], a[92:95], v[88:91]\n"
"buffer_load_dwordx4 v220, %6, s64 offen offset:0 lds\n"
"v_mfma_f32_16x16x32_bf16 v[88:91], a[224:227], a[76:79], v[88:91]\n"
"s_waitcnt lgkmcnt(4)\n"
"v_mfma_f32_16x16x32_bf16 v[92:95], a[232:235], a[76:79], v[92:95]\n"
"ds_read_b64_tr_b16 a[248:249], v199 offset:32768\n"
"s_add_u32 s74, s74, 1\n"
"v_mfma_f32_16x16x32_bf16 v[92:95], a[236:239], a[92:95], v[92:95]\n"
"ds_read_b64_tr_b16 a[250:251], v199 offset:36864\n"
"v_readlane_b32 s68, v192, s74\n"
"v_mfma_f32_16x16x32_bf16 v[84:87], a[236:239], a[88:91], v[84:87]\n"
"buffer_load_dwordx4 v221, %6, s64 offen offset:1024 lds\n"
"v_mfma_f32_16x16x32_bf16 v[84:87], a[232:235], a[72:75], v[84:87]\n"
"v_mfma_f32_16x16x32_bf16 v[76:79], a[232:235], a[68:71], v[76:79]\n"
"ds_read_b64_tr_b16 a[252:253], v199 offset:40960\n"
"v_mfma_f32_16x16x32_bf16 v[76:79], a[236:239], a[84:87], v[76:79]\n"
"ds_read_b64_tr_b16 a[254:255], v199 offset:45056\n"
"v_mfma_f32_16x16x32_bf16 v[68:71], a[236:239], a[80:83], v[68:71]\n"
"buffer_load_dwordx4 v222, %6, s64 offen offset:2048 lds\n"
"v_mfma_f32_16x16x32_bf16 v[68:71], a[232:235], a[64:67], v[68:71]\n"
"s_waitcnt lgkmcnt(4)\n"
"v_mfma_f32_16x16x32_bf16 v[96:99], a[240:243], a[64:67], v[96:99]\n"
"ds_read_b64_tr_b16 a[224:225], v200 offset:32768\n"
"v_mfma_f32_16x16x32_bf16 v[96:99], a[244:247], a[80:83], v[96:99]\n"
"ds_read_b64_tr_b16 a[226:227], v200 offset:36864\n"
"v_mfma_f32_16x16x32_bf16 v[104:107], a[244:247], a[84:87], v[104:107]\n"
"buffer_load_dwordx4 v223, %6, s64 offen offset:3072 lds\n"
"v_mfma_f32_16x16x32_bf16 v[104:107], a[240:243], a[68:71], v[104:107]\n"
"v_mfma_f32_16x16x32_bf16 v[112:115], a[240:243], a[72:75], v[112:115]\n"
"ds_read_b64_tr_b16 a[228:229], v200 offset:40960\n"
"v_mfma_f32_16x16x32_bf16 v[112:115], a[244:247], a[88:91], v[112:115]\n"
"ds_read_b64_tr_b16 a[230:231], v200 offset:45056\n"
"v_mfma_f32_16x16x32_bf16 v[120:123], a[244:247], a[92:95], v[120:123]\n"
"v_mfma_f32_16x16x32_bf16 v[120:123], a[240:243], a[76:79], v[120:123]\n"
"s_waitcnt lgkmcnt(4)\n"
"v_mfma_f32_16x16x32_bf16 v[124:127], a[248:251], a[76:79], v[124:127]\n"
"ds_read_b64_tr_b16 a[232:233], v201 offset:32768\n"
"v_mfma_f32_16x16x32_bf16 v[124:127], a[252:255], a[92:95], v[124:127]\n"
"ds_read_b64_tr_b16 a[234:235], v201 offset:36864\n"
"s_lshl_b32 s65, s76, 12\n"
"v_mfma_f32_16x16x32_bf16 v[116:119], a[252:255], a[88:91], v[116:119]\n"
"buffer_load_dwordx4 a[32:35], v218, %5, s65 offen offset:0 sc1\n"
"v_mfma_f32_16x16x32_bf16 v[116:119], a[248:251], a[72:75], v[116:119]\n"
"v_mfma_f32_16x16x32_bf16 v[108:111], a[248:251], a[68:71], v[108:111]\n"
"ds_read_b64_tr_b16 a[236:237], v201 offset:40960\n"
"s_add_u32 s75, s75, 1\n"
"v_mfma_f32_16x16x32_bf16 v[108:111], a[252:255], a[84:87], v[108:111]\n"
"ds_read_b64_tr_b16 a[238:239], v201 offset:45056\n"
"v_readlane_b32 s76, v192, s75\n"
"v_mfma_f32_16x16x32_bf16 v[100:103], a[252:255], a[80:83], v[100:103]\n"
"buffer_load_dwordx4 a[36:39], v218, %5, s65 offen offset:1024 sc1\n"
"v_mfma_f32_16x16x32_bf16 v[100:103], a[248:251], a[64:67], v[100:103]\n"
"s_waitcnt lgkmcnt(4)\n"
"v_mfma_f32_16x16x32_bf16 v[128:131], a[224:227], a[64:67], v[128:131]\n"
"ds_read_b64_tr_b16 a[240:241], v202 offset:32768\n"
"v_mfma_f32_16x16x32_bf16 v[128:131], a[228:231], a[80:83], v[128:131]\n"
"ds_read_b64_tr_b16 a[242:243], v202 offset:36864\n"
"v_mfma_f32_16x16x32_bf16 v[136:139], a[228:231], a[84:87], v[136:139]\n"
"buffer_load_dwordx4 a[40:43], v219, %5, s65 offen offset:0 sc1\n"
"v_mfma_f32_16x16x32_bf16 v[136:139], a[224:227], a[68:71], v[136:139]\n"
"v_mfma_f32_16x16x32_bf16 v[144:147], a[224:227], a[72:75], v[144:147]\n"
"ds_read_b64_tr_b16 a[244:245], v202 offset:40960\n"
"v_mfma_f32_16x16x32_bf16 v[144:147], a[228:231], a[88:91], v[144:147]\n"
"ds_read_b64_tr_b16 a[246:247], v202 offset:45056\n"
"v_mfma_f32_16x16x32_bf16 v[152:155], a[228:231], a[92:95], v[152:155]\n"
"buffer_load_dwordx4 a[44:47], v219, %5, s65 offen offset:1024 sc1\n"
"v_mfma_f32_16x16x32_bf16 v[152:155], a[224:227], a[76:79], v[152:155]\n"
"s_waitcnt lgkmcnt(4)\n"
"v_mfma_f32_16x16x32_bf16 v[156:159], a[232:235], a[76:79], v[156:159]\n"
"ds_read_b64_tr_b16 a[248:249], v203 offset:32768\n"
"v_mfma_f32_16x16x32_bf16 v[156:159], a[236:239], a[92:95], v[156:159]\n"
"ds_read_b64_tr_b16 a[250:251], v203 offset:36864\n"
"v_mfma_f32_16x16x32_bf16 v[148:151], a[236:239], a[88:91], v[148:151]\n"
"v_mfma_f32_16x16x32_bf16 v[148:151], a[232:235], a[72:75], v[148:151]\n"
"v_mfma_f32_16x16x32_bf16 v[140:143], a[232:235], a[68:71], v[140:143]\n"
"ds_read_b64_tr_b16 a[252:253], v203 offset:40960\n"
"v_mfma_f32_16x16x32_bf16 v[140:143], a[236:239], a[84:87], v[140:143]\n"
"ds_read_b64_tr_b16 a[254:255], v203 offset:45056\n"
"v_mfma_f32_16x16x32_bf16 v[132:135], a[236:239], a[80:83], v[132:135]\n"
"buffer_load_dwordx4 a[48:51], v218, %5, s65 offen offset:2048 sc1\n"
"v_mfma_f32_16x16x32_bf16 v[132:135], a[232:235], a[64:67], v[132:135]\n"
"s_waitcnt lgkmcnt(4)\n"
"v_mfma_f32_16x16x32_bf16 v[160:163], a[240:243], a[64:67], v[160:163]\n"
"s_waitcnt vmcnt(17)\n"
"s_barrier\n"
"ds_read_b64_tr_b16 a[224:225], v196 offset:49152\n"
"v_mfma_f32_16x16x32_bf16 v[160:163], a[244:247], a[80:83], v[160:163]\n"
"ds_read_b64_tr_b16 a[226:227], v196 offset:53248\n"
"v_mfma_f32_16x16x32_bf16 v[168:171], a[244:247], a[84:87], v[168:171]\n"
"buffer_load_dwordx4 a[52:55], v218, %5, s65 offen offset:3072 sc1\n"
"v_mfma_f32_16x16x32_bf16 v[168:171], a[240:243], a[68:71], v[168:171]\n"
"v_mfma_f32_16x16x32_bf16 v[176:179], a[240:243], a[72:75], v[176:179]\n"
"ds_read_b64_tr_b16 a[228:229], v196 offset:57344\n"
"v_mfma_f32_16x16x32_bf16 v[176:179], a[244:247], a[88:91], v[176:179]\n"
"ds_read_b64_tr_b16 a[230:231], v196 offset:61440\n"
"v_mfma_f32_16x16x32_bf16 v[184:187], a[244:247], a[92:95], v[184:187]\n"
"buffer_load_dwordx4 a[56:59], v219, %5, s65 offen offset:2048 sc1\n"
"v_mfma_f32_16x16x32_bf16 v[184:187], a[240:243], a[76:79], v[184:187]\n"
"s_waitcnt lgkmcnt(4)\n"
"v_mfma_f32_16x16x32_bf16 v[188:191], a[248:251], a[76:79], v[188:191]\n"
"ds_read_b64_tr_b16 a[232:233], v197 offset:49152\n"
"v_mfma_f32_16x16x32_bf16 v[188:191], a[252:255], a[92:95], v[188:191]\n"
"ds_read_b64_tr_b16 a[234:235], v197 offset:53248\n"
"v_mfma_f32_16x16x32_bf16 v[180:183], a[252:255], a[88:91], v[180:183]\n"
"buffer_load_dwordx4 a[60:63], v219, %5, s65 offen offset:3072 sc1\n"
"v_mfma_f32_16x16x32_bf16 v[180:183], a[248:251], a[72:75], v[180:183]\n"
"v_mfma_f32_16x16x32_bf16 v[172:175], a[248:251], a[68:71], v[172:175]\n"
"ds_read_b64_tr_b16 a[236:237], v197 offset:57344\n"
"v_mfma_f32_16x16x32_bf16 v[172:175], a[252:255], a[84:87], v[172:175]\n"
"ds_read_b64_tr_b16 a[238:239], v197 offset:61440\n"
"v_mfma_f32_16x16x32_bf16 v[164:167], a[252:255], a[80:83], v[164:167]\n"
"v_mfma_f32_16x16x32_bf16 v[164:167], a[248:251], a[64:67], v[164:167]\n"
"s_waitcnt vmcnt(24) lgkmcnt(4)\n"
"v_mfma_f32_16x16x32_bf16 v[64:67], a[224:227], a[96:99], v[64:67]\n"
"ds_read_b64_tr_b16 a[240:241], v198 offset:49152\n"
"v_mfma_f32_16x16x32_bf16 v[64:67], a[228:231], a[112:115], v[64:67]\n"
"ds_read_b64_tr_b16 a[242:243], v198 offset:53248\n"
"v_mfma_f32_16x16x32_bf16 v[72:75], a[228:231], a[116:119], v[72:75]\n"
"v_mfma_f32_16x16x32_bf16 v[72:75], a[224:227], a[100:103], v[72:75]\n"
"s_lshl_b32 s64, s68, 17\n"
"v_mfma_f32_16x16x32_bf16 v[80:83], a[224:227], a[104:107], v[80:83]\n"
"ds_read_b64_tr_b16 a[244:245], v198 offset:57344\n"
"s_add_u32 s64, s64, s67\n"
"v_mfma_f32_16x16x32_bf16 v[80:83], a[228:231], a[120:123], v[80:83]\n"
"ds_read_b64_tr_b16 a[246:247], v198 offset:61440\n"
"s_add_u32 m0, %11, 81920\n"
"v_mfma_f32_16x16x32_bf16 v[88:91], a[228:231], a[124:127], v[88:91]\n"
"buffer_load_dwordx4 v220, %6, s64 offen offset:0 lds\n"
"v_mfma_f32_16x16x32_bf16 v[88:91], a[224:227], a[108:111], v[88:91]\n"
"s_waitcnt lgkmcnt(4)\n"
"v_mfma_f32_16x16x32_bf16 v[92:95], a[232:235], a[108:111], v[92:95]\n"
"ds_read_b64_tr_b16 a[248:249], v199 offset:49152\n"
"s_add_u32 s74, s74, 1\n"
"v_mfma_f32_16x16x32_bf16 v[92:95], a[236:239], a[124:127], v[92:95]\n"
"ds_read_b64_tr_b16 a[250:251], v199 offset:53248\n"
"v_readlane_b32 s68, v192, s74\n"
"v_mfma_f32_16x16x32_bf16 v[84:87], a[236:239], a[120:123], v[84:87]\n"
"buffer_load_dwordx4 v221, %6, s64 offen offset:1024 lds\n"
"v_mfma_f32_16x16x32_bf16 v[84:87], a[232:235], a[104:107], v[84:87]\n"
"v_mfma_f32_16x16x32_bf16 v[76:79], a[232:235], a[100:103], v[76:79]\n"
"ds_read_b64_tr_b16 a[252:253], v199 offset:57344\n"
"v_mfma_f32_16x16x32_bf16 v[76:79], a[236:239], a[116:119], v[76:79]\n"
"ds_read_b64_tr_b16 a[254:255], v199 offset:61440\n"
"v_mfma_f32_16x16x32_bf16 v[68:71], a[236:239], a[112:115], v[68:71]\n"
"buffer_load_dwordx4 v222, %6, s64 offen offset:2048 lds\n"
"v_mfma_f32_16x16x32_bf16 v[68:71], a[232:235], a[96:99], v[68:71]\n"
"s_waitcnt lgkmcnt(4)\n"
"v_mfma_f32_16x16x32_bf16 v[96:99], a[240:243], a[96:99], v[96:99]\n"
"ds_read_b64_tr_b16 a[224:225], v200 offset:49152\n"
"v_mfma_f32_16x16x32_bf16 v[96:99], a[244:247], a[112:115], v[96:99]\n"
"ds_read_b64_tr_b16 a[226:227], v200 offset:53248\n"
"v_mfma_f32_16x16x32_bf16 v[104:107], a[244:247], a[116:119], v[104:107]\n"
"buffer_load_dwordx4 v223, %6, s64 offen offset:3072 lds\n"
"v_mfma_f32_16x16x32_bf16 v[104:107], a[240:243], a[100:103], v[104:107]\n"
"v_mfma_f32_16x16x32_bf16 v[112:115], a[240:243], a[104:107], v[112:115]\n"
"ds_read_b64_tr_b16 a[228:229], v200 offset:57344\n"
"v_mfma_f32_16x16x32_bf16 v[112:115], a[244:247], a[120:123], v[112:115]\n"
"ds_read_b64_tr_b16 a[230:231], v200 offset:61440\n"
"v_mfma_f32_16x16x32_bf16 v[120:123], a[244:247], a[124:127], v[120:123]\n"
"v_mfma_f32_16x16x32_bf16 v[120:123], a[240:243], a[108:111], v[120:123]\n"
"s_waitcnt lgkmcnt(4)\n"
"v_mfma_f32_16x16x32_bf16 v[124:127], a[248:251], a[108:111], v[124:127]\n"
"ds_read_b64_tr_b16 a[232:233], v201 offset:49152\n"
"v_mfma_f32_16x16x32_bf16 v[124:127], a[252:255], a[124:127], v[124:127]\n"
"ds_read_b64_tr_b16 a[234:235], v201 offset:53248\n"
"s_lshl_b32 s65, s76, 12\n"
"v_mfma_f32_16x16x32_bf16 v[116:119], a[252:255], a[120:123], v[116:119]\n"
"buffer_load_dwordx4 a[64:67], v218, %5, s65 offen offset:0 sc1\n"
"v_mfma_f32_16x16x32_bf16 v[116:119], a[248:251], a[104:107], v[116:119]\n"
"v_mfma_f32_16x16x32_bf16 v[108:111], a[248:251], a[100:103], v[108:111]\n"
"ds_read_b64_tr_b16 a[236:237], v201 offset:57344\n"
"s_add_u32 s75, s75, 1\n"
"v_mfma_f32_16x16x32_bf16 v[108:111], a[252:255], a[116:119], v[108:111]\n"
"ds_read_b64_tr_b16 a[238:239], v201 offset:61440\n"
"v_readlane_b32 s76, v192, s75\n"
"v_mfma_f32_16x16x32_bf16 v[100:103], a[252:255], a[112:115], v[100:103]\n"
"buffer_load_dwordx4 a[68:71], v218, %5, s65 offen offset:1024 sc1\n"
"v_mfma_f32_16x16x32_bf16 v[100:103], a[248:251], a[96:99], v[100:103]\n"
"s_waitcnt lgkmcnt(4)\n"
"v_mfma_f32_16x16x32_bf16 v[128:131], a[224:227], a[96:99], v[128:131]\n"
"ds_read_b64_tr_b16 a[240:241], v202 offset:49152\n"
"v_mfma_f32_16x16x32_bf16 v[128:131], a[228:231], a[112:115], v[128:131]\n"
"ds_read_b64_tr_b16 a[242:243], v202 offset:53248\n"
"v_mfma_f32_16x16x32_bf16 v[136:139], a[228:231], a[116:119], v[136:139]\n"
"buffer_load_dwordx4 a[72:75], v219, %5, s65 offen offset:0 sc1\n"
"v_mfma_f32_16x16x32_bf16 v[136:139], a[224:227], a[100:103], v[136:139]\n"
"v_mfma_f32_16x16x32_bf16 v[144:147], a[224:227], a[104:107], v[144:147]\n"
"ds_read_b64_tr_b16 a[244:245], v202 offset:57344\n"
"v_mfma_f32_16x16x32_bf16 v[144:147], a[228:231], a[120:123], v[144:147]\n"
"ds_read_b64_tr_b16 a[246:247], v202 offset:61440\n"
"v_mfma_f32_16x16x32_bf16 v[152:155], a[228:231], a[124:127], v[152:155]\n"
"buffer_load_dwordx4 a[76:79], v219, %5, s65 offen offset:1024 sc1\n"
"v_mfma_f32_16x16x32_bf16 v[152:155], a[224:227], a[108:111], v[152:155]\n"
"s_waitcnt lgkmcnt(4)\n"
"v_mfma_f32_16x16x32_bf16 v[156:159], a[232:235], a[108:111], v[156:159]\n"
"ds_read_b64_tr_b16 a[248:249], v203 offset:49152\n"
"v_mfma_f32_16x16x32_bf16 v[156:159], a[236:239], a[124:127], v[156:159]\n"
"ds_read_b64_tr_b16 a[250:251], v203 offset:53248\n"
"v_mfma_f32_16x16x32_bf16 v[148:151], a[236:239], a[120:123], v[148:151]\n"
"v_mfma_f32_16x16x32_bf16 v[148:151], a[232:235], a[104:107], v[148:151]\n"
"v_mfma_f32_16x16x32_bf16 v[140:143], a[232:235], a[100:103], v[140:143]\n"
"ds_read_b64_tr_b16 a[252:253], v203 offset:57344\n"
"v_mfma_f32_16x16x32_bf16 v[140:143], a[236:239], a[116:119], v[140:143]\n"
"ds_read_b64_tr_b16 a[254:255], v203 offset:61440\n"
"v_mfma_f32_16x16x32_bf16 v[132:135], a[236:239], a[112:115], v[132:135]\n"
"buffer_load_dwordx4 a[80:83], v218, %5, s65 offen offset:2048 sc1\n"
"v_mfma_f32_16x16x32_bf16 v[132:135], a[232:235], a[96:99], v[132:135]\n"
"s_waitcnt lgkmcnt(4)\n"
"v_mfma_f32_16x16x32_bf16 v[160:163], a[240:243], a[96:99], v[160:163]\n"
"s_waitcnt vmcnt(17)\n"
"s_barrier\n"
"ds_read_b64_tr_b16 a[224:225], v196 offset:0\n"
"v_mfma_f32_16x16x32_bf16 v[160:163], a[244:247], a[112:115], v[160:163]\n"
"ds_read_b64_tr_b16 a[226:227], v196 offset:4096\n"
"v_mfma_f32_16x16x32_bf16 v[168:171], a[244:247], a[116:119], v[168:171]\n"
"buffer_load_dwordx4 a[84:87], v218, %5, s65 offen offset:3072 sc1\n"
"v_mfma_f32_16x16x32_bf16 v[168:171], a[240:243], a[100:103], v[168:171]\n"
"v_mfma_f32_16x16x32_bf16 v[176:179], a[240:243], a[104:107], v[176:179]\n"
"ds_read_b64_tr_b16 a[228:229], v196 offset:8192\n"
"v_mfma_f32_16x16x32_bf16 v[176:179], a[244:247], a[120:123], v[176:179]\n"
"ds_read_b64_tr_b16 a[230:231], v196 offset:12288\n"
"v_mfma_f32_16x16x32_bf16 v[184:187], a[244:247], a[124:127], v[184:187]\n"
"buffer_load_dwordx4 a[88:91], v219, %5, s65 offen offset:2048 sc1\n"
"v_mfma_f32_16x16x32_bf16 v[184:187], a[240:243], a[108:111], v[184:187]\n"
"s_waitcnt lgkmcnt(4)\n"
"v_mfma_f32_16x16x32_bf16 v[188:191], a[248:251], a[108:111], v[188:191]\n"
"ds_read_b64_tr_b16 a[232:233], v197 offset:0\n"
"v_mfma_f32_16x16x32_bf16 v[188:191], a[252:255], a[124:127], v[188:191]\n"
"ds_read_b64_tr_b16 a[234:235], v197 offset:4096\n"
"v_mfma_f32_16x16x32_bf16 v[180:183], a[252:255], a[120:123], v[180:183]\n"
"buffer_load_dwordx4 a[92:95], v219, %5, s65 offen offset:3072 sc1\n"
"v_mfma_f32_16x16x32_bf16 v[180:183], a[248:251], a[104:107], v[180:183]\n"
"v_mfma_f32_16x16x32_bf16 v[172:175], a[248:251], a[100:103], v[172:175]\n"
"ds_read_b64_tr_b16 a[236:237], v197 offset:8192\n"
"v_mfma_f32_16x16x32_bf16 v[172:175], a[252:255], a[116:119], v[172:175]\n"
"ds_read_b64_tr_b16 a[238:239], v197 offset:12288\n"
"v_mfma_f32_16x16x32_bf16 v[164:167], a[252:255], a[112:115], v[164:167]\n"
"v_mfma_f32_16x16x32_bf16 v[164:167], a[248:251], a[96:99], v[164:167]\n"
"s_waitcnt vmcnt(24) lgkmcnt(4)\n"
"v_mfma_f32_16x16x32_bf16 v[64:67], a[224:227], a[0:3], v[64:67]\n"
"ds_read_b64_tr_b16 a[240:241], v198 offset:0\n"
"s_lshl_b32 s64, s68, 17\n"
"s_add_u32 s64, s64, s67\n"
"v_mfma_f32_16x16x32_bf16 v[64:67], a[228:231], a[16:19], v[64:67]\n"
"ds_read_b64_tr_b16 a[242:243], v198 offset:4096\n"
"s_add_u32 m0, %11, 98304\n"
"v_mfma_f32_16x16x32_bf16 v[72:75], a[228:231], a[20:23], v[72:75]\n"
"buffer_load_dwordx4 v220, %6, s64 offen offset:0 lds\n"
"v_mfma_f32_16x16x32_bf16 v[72:75], a[224:227], a[4:7], v[72:75]\n"
"s_barrier\n"
"v_mfma_f32_16x16x32_bf16 v[80:83], a[224:227], a[8:11], v[80:83]\n"
"ds_read_b64_tr_b16 a[244:245], v198 offset:8192\n"
"s_add_u32 s74, s74, 1\n"
"v_mfma_f32_16x16x32_bf16 v[80:83], a[228:231], a[24:27], v[80:83]\n"
"ds_read_b64_tr_b16 a[246:247], v198 offset:12288\n"
"v_readlane_b32 s68, v192, s74\n"
"v_mfma_f32_16x16x32_bf16 v[88:91], a[228:231], a[28:31], v[88:91]\n"
"buffer_load_dwordx4 v221, %6, s64 offen offset:1024 lds\n"
"v_mfma_f32_16x16x32_bf16 v[88:91], a[224:227], a[12:15], v[88:91]\n"
"s_waitcnt lgkmcnt(4)\n"
"v_mfma_f32_16x16x32_bf16 v[92:95], a[232:235], a[12:15], v[92:95]\n"
"ds_read_b64_tr_b16 a[248:249], v199 offset:0\n"
"v_mfma_f32_16x16x32_bf16 v[92:95], a[236:239], a[28:31], v[92:95]\n"
"ds_read_b64_tr_b16 a[250:251], v199 offset:4096\n"
"v_mfma_f32_16x16x32_bf16 v[84:87], a[236:239], a[24:27], v[84:87]\n"
"buffer_load_dwordx4 v222, %6, s64 offen offset:2048 lds\n"
"v_mfma_f32_16x16x32_bf16 v[84:87], a[232:235], a[8:11], v[84:87]\n"
"v_mfma_f32_16x16x32_bf16 v[76:79], a[232:235], a[4:7], v[76:79]\n"
"ds_read_b64_tr_b16 a[252:253], v199 offset:8192\n"
"v_mfma_f32_16x16x32_bf16 v[76:79], a[236:239], a[20:23], v[76:79]\n"
"ds_read_b64_tr_b16 a[254:255], v199 offset:12288\n"
"v_mfma_f32_16x16x32_bf16 v[68:71], a[236:239], a[16:19], v[68:71]\n"
"buffer_load_dwordx4 v223, %6, s64 offen offset:3072 lds\n"
"v_mfma_f32_16x16x32_bf16 v[68:71], a[232:235], a[0:3], v[68:71]\n"
"s_lshl_b32 s64, s68, 17\n"
"s_waitcnt lgkmcnt(4)\n"
"v_mfma_f32_16x16x32_bf16 v[96:99], a[240:243], a[0:3], v[96:99]\n"
"ds_read_b64_tr_b16 a[224:225], v200 offset:0\n"
"s_add_u32 s64, s64, s67\n"
"v_mfma_f32_16x16x32_bf16 v[96:99], a[244:247], a[16:19], v[96:99]\n"
"ds_read_b64_tr_b16 a[226:227], v200 offset:4096\n"
"s_add_u32 m0, %11, 114688\n"
"v_mfma_f32_16x16x32_bf16 v[104:107], a[244:247], a[20:23], v[104:107]\n"
"buffer_load_dwordx4 v220, %6, s64 offen offset:0 lds\n"
"v_mfma_f32_16x16x32_bf16 v[104:107], a[240:243], a[4:7], v[104:107]\n"
"v_mfma_f32_16x16x32_bf16 v[112:115], a[240:243], a[8:11], v[112:115]\n"
"ds_read_b64_tr_b16 a[228:229], v200 offset:8192\n"
"s_add_u32 s74, s74, 1\n"
"v_mfma_f32_16x16x32_bf16 v[112:115], a[244:247], a[24:27], v[112:115]\n"
"ds_read_b64_tr_b16 a[230:231], v200 offset:12288\n"
"v_readlane_b32 s68, v192, s74\n"
"v_mfma_f32_16x16x32_bf16 v[120:123], a[244:247], a[28:31], v[120:123]\n"
"buffer_load_dwordx4 v221, %6, s64 offen offset:1024 lds\n"
"v_mfma_f32_16x16x32_bf16 v[120:123], a[240:243], a[12:15], v[120:123]\n"
"s_waitcnt lgkmcnt(4)\n"
"v_mfma_f32_16x16x32_bf16 v[124:127], a[248:251], a[12:15], v[124:127]\n"
"ds_read_b64_tr_b16 a[232:233], v201 offset:0\n"
"v_mfma_f32_16x16x32_bf16 v[124:127], a[252:255], a[28:31], v[124:127]\n"
"ds_read_b64_tr_b16 a[234:235], v201 offset:4096\n"
"v_mfma_f32_16x16x32_bf16 v[116:119], a[252:255], a[24:27], v[116:119]\n"
"buffer_load_dwordx4 v222, %6, s64 offen offset:2048 lds\n"
"v_mfma_f32_16x16x32_bf16 v[116:119], a[248:251], a[8:11], v[116:119]\n"
"v_mfma_f32_16x16x32_bf16 v[108:111], a[248:251], a[4:7], v[108:111]\n"
"ds_read_b64_tr_b16 a[236:237], v201 offset:8192\n"
"v_mfma_f32_16x16x32_bf16 v[108:111], a[252:255], a[20:23], v[108:111]\n"
"ds_read_b64_tr_b16 a[238:239], v201 offset:12288\n"
"v_mfma_f32_16x16x32_bf16 v[100:103], a[252:255], a[16:19], v[100:103]\n"
"buffer_load_dwordx4 v223, %6, s64 offen offset:3072 lds\n"
"v_mfma_f32_16x16x32_bf16 v[100:103], a[248:251], a[0:3], v[100:103]\n"
"s_waitcnt lgkmcnt(4)\n"
"v_mfma_f32_16x16x32_bf16 v[128:131], a[224:227], a[0:3], v[128:131]\n"
"s_waitcnt vmcnt(16)\n"
"s_barrier\n"
"ds_read_b64_tr_b16 a[240:241], v196 offset:16384\n"
"v_mfma_f32_16x16x32_bf16 v[128:131], a[228:231], a[16:19], v[128:131]\n"
"ds_read_b64_tr_b16 a[242:243], v196 offset:20480\n"
"s_lshl_b32 s65, s76, 12\n"
"v_mfma_f32_16x16x32_bf16 v[136:139], a[228:231], a[20:23], v[136:139]\n"
"buffer_load_dwordx4 a[96:99], v218, %5, s65 offen offset:0 sc1\n"
"v_mfma_f32_16x16x32_bf16 v[136:139], a[224:227], a[4:7], v[136:139]\n"
"v_mfma_f32_16x16x32_bf16 v[144:147], a[224:227], a[8:11], v[144:147]\n"
"ds_read_b64_tr_b16 a[244:245], v196 offset:24576\n"
"s_add_u32 s75, s75, 1\n"
"v_mfma_f32_16x16x32_bf16 v[144:147], a[228:231], a[24:27], v[144:147]\n"
"ds_read_b64_tr_b16 a[246:247], v196 offset:28672\n"
"v_readlane_b32 s76, v192, s75\n"
"v_mfma_f32_16x16x32_bf16 v[152:155], a[228:231], a[28:31], v[152:155]\n"
"buffer_load_dwordx4 a[100:103], v218, %5, s65 offen offset:1024 sc1\n"
"v_mfma_f32_16x16x32_bf16 v[152:155], a[224:227], a[12:15], v[152:155]\n"
"s_waitcnt lgkmcnt(4)\n"
"v_mfma_f32_16x16x32_bf16 v[156:159], a[232:235], a[12:15], v[156:159]\n"
"ds_read_b64_tr_b16 a[248:249], v197 offset:16384\n"
"v_mfma_f32_16x16x32_bf16 v[156:159], a[236:239], a[28:31], v[156:159]\n"
"ds_read_b64_tr_b16 a[250:251], v197 offset:20480\n"
"v_mfma_f32_16x16x32_bf16 v[148:151], a[236:239], a[24:27], v[148:151]\n"
"buffer_load_dwordx4 a[104:107], v219, %5, s65 offen offset:0 sc1\n"
"v_mfma_f32_16x16x32_bf16 v[148:151], a[232:235], a[8:11], v[148:151]\n"
"v_mfma_f32_16x16x32_bf16 v[140:143], a[232:235], a[4:7], v[140:143]\n"
"ds_read_b64_tr_b16 a[252:253], v197 offset:24576\n"
"v_mfma_f32_16x16x32_bf16 v[140:143], a[236:239], a[20:23], v[140:143]\n"
"ds_read_b64_tr_b16 a[254:255], v197 offset:28672\n"
"v_mfma_f32_16x16x32_bf16 v[132:135], a[236:239], a[16:19], v[132:135]\n"
"buffer_load_dwordx4 a[108:111], v219, %5, s65 offen offset:1024 sc1\n"
"v_mfma_f32_16x16x32_bf16 v[132:135], a[232:235], a[0:3], v[132:135]\n"
"s_waitcnt vmcnt(24) lgkmcnt(4)\n"
"v_mfma_f32_16x16x32_bf16 v[64:67], a[240:243], a[32:35], v[64:67]\n"
"ds_read_b64_tr_b16 a[224:225], v198 offset:16384\n"
"v_mfma_f32_16x16x32_bf16 v[64:67], a[244:247], a[48:51], v[64:67]\n"
"ds_read_b64_tr_b16 a[226:227], v198 offset:20480\n"
"v_mfma_f32_16x16x32_bf16 v[72:75], a[244:247], a[52:55], v[72:75]\n"
"buffer_load_dwordx4 a[112:115], v218, %5, s65 offen offset:2048 sc1\n"
"v_mfma_f32_16x16x32_bf16 v[72:75], a[240:243], a[36:39], v[72:75]\n"
"v_mfma_f32_16x16x32_bf16 v[80:83], a[240:243], a[40:43], v[80:83]\n"
"ds_read_b64_tr_b16 a[228:229], v198 offset:24576\n"
"v_mfma_f32_16x16x32_bf16 v[80:83], a[244:247], a[56:59], v[80:83]\n"
"ds_read_b64_tr_b16 a[230:231], v198 offset:28672\n"
"v_mfma_f32_16x16x32_bf16 v[88:91], a[244:247], a[60:63], v[88:91]\n"
"buffer_load_dwordx4 a[116:119], v218, %5, s65 offen offset:3072 sc1\n"
"v_mfma_f32_16x16x32_bf16 v[88:91], a[240:243], a[44:47], v[88:91]\n"
"s_waitcnt lgkmcnt(4)\n"
"v_mfma_f32_16x16x32_bf16 v[92:95], a[248:251], a[44:47], v[92:95]\n"
"ds_read_b64_tr_b16 a[232:233], v199 offset:16384\n"
"v_mfma_f32_16x16x32_bf16 v[92:95], a[252:255], a[60:63], v[92:95]\n"
"ds_read_b64_tr_b16 a[234:235], v199 offset:20480\n"
"v_mfma_f32_16x16x32_bf16 v[84:87], a[252:255], a[56:59], v[84:87]\n"
"buffer_load_dwordx4 a[120:123], v219, %5, s65 offen offset:2048 sc1\n"
"v_mfma_f32_16x16x32_bf16 v[84:87], a[248:251], a[40:43], v[84:87]\n"
"v_mfma_f32_16x16x32_bf16 v[76:79], a[248:251], a[36:39], v[76:79]\n"
"ds_read_b64_tr_b16 a[236:237], v199 offset:24576\n"
"v_mfma_f32_16x16x32_bf16 v[76:79], a[252:255], a[52:55], v[76:79]\n"
"ds_read_b64_tr_b16 a[238:239], v199 offset:28672\n"
"v_mfma_f32_16x16x32_bf16 v[68:71], a[252:255], a[48:51], v[68:71]\n"
"buffer_load_dwordx4 a[124:127], v219, %5, s65 offen offset:3072 sc1\n"
"v_mfma_f32_16x16x32_bf16 v[68:71], a[248:251], a[32:35], v[68:71]\n"
"s_waitcnt lgkmcnt(4)\n"
"v_mfma_f32_16x16x32_bf16 v[96:99], a[224:227], a[32:35], v[96:99]\n"
"s_waitcnt vmcnt(12)\n"
"s_barrier\n"
"ds_read_b64_tr_b16 a[240:241], v196 offset:32768\n"
"v_mfma_f32_16x16x32_bf16 v[96:99], a[228:231], a[48:51], v[96:99]\n"
"ds_read_b64_tr_b16 a[242:243], v196 offset:36864\n"
"v_mfma_f32_16x16x32_bf16 v[104:107], a[228:231], a[52:55], v[104:107]\n"
"v_mfma_f32_16x16x32_bf16 v[104:107], a[224:227], a[36:39], v[104:107]\n"
"v_add_u32 v219, 0x10000, %19\n"
"v_mfma_f32_16x16x32_bf16 v[112:115], a[224:227], a[40:43], v[112:115]\n"
"ds_read_b64_tr_b16 a[244:245], v196 offset:40960\n"
"v_mfma_f32_16x16x32_bf16 v[112:115], a[228:231], a[56:59], v[112:115]\n"
"ds_read_b64_tr_b16 a[246:247], v196 offset:45056\n"
"v_mfma_f32_16x16x32_bf16 v[120:123], a[228:231], a[60:63], v[120:123]\n"
"v_mfma_f32_16x16x32_bf16 v[120:123], a[224:227], a[44:47], v[120:123]\n"
"s_waitcnt lgkmcnt(4)\n"
"v_mfma_f32_16x16x32_bf16 v[124:127], a[232:235], a[44:47], v[124:127]\n"
"ds_read_b64_tr_b16 a[248:249], v197 offset:32768\n"
"v_mfma_f32_16x16x32_bf16 v[124:127], a[236:239], a[60:63], v[124:127]\n"
"ds_read_b64_tr_b16 a[250:251], v197 offset:36864\n"
"v_mfma_f32_16x16x32_bf16 v[116:119], a[236:239], a[56:59], v[116:119]\n"
"v_mfma_f32_16x16x32_bf16 v[116:119], a[232:235], a[40:43], v[116:119]\n"
"v_mfma_f32_16x16x32_bf16 v[108:111], a[232:235], a[36:39], v[108:111]\n"
"ds_read_b64_tr_b16 a[252:253], v197 offset:40960\n"
"v_mfma_f32_16x16x32_bf16 v[108:111], a[236:239], a[52:55], v[108:111]\n"
"ds_read_b64_tr_b16 a[254:255], v197 offset:45056\n"
"v_mfma_f32_16x16x32_bf16 v[100:103], a[236:239], a[48:51], v[100:103]\n"
"v_mfma_f32_16x16x32_bf16 v[100:103], a[232:235], a[32:35], v[100:103]\n"
"s_waitcnt vmcnt(16) lgkmcnt(4)\n"
"v_mfma_f32_16x16x32_bf16 v[64:67], a[240:243], a[64:67], v[64:67]\n"
"s_waitcnt vmcnt(8)\n"
"s_barrier\n"
"ds_read_b64_tr_b16 a[224:225], v196 offset:49152\n"
"v_mfma_f32_16x16x32_bf16 v[64:67], a[244:247], a[80:83], v[64:67]\n"
"ds_read_b64_tr_b16 a[226:227], v196 offset:53248\n"
"v_mfma_f32_16x16x32_bf16 v[72:75], a[244:247], a[84:87], v[72:75]\n"
"v_mfma_f32_16x16x32_bf16 v[72:75], a[240:243], a[68:71], v[72:75]\n"
"v_mfma_f32_16x16x32_bf16 v[80:83], a[240:243], a[72:75], v[80:83]\n"
"ds_read_b64_tr_b16 a[228:229], v196 offset:57344\n"
"v_mfma_f32_16x16x32_bf16 v[80:83], a[244:247], a[88:91], v[80:83]\n"
"ds_read_b64_tr_b16 a[230:231], v196 offset:61440\n"
"v_mfma_f32_16x16x32_bf16 v[88:91], a[244:247], a[92:95], v[88:91]\n"
"v_mfma_f32_16x16x32_bf16 v[88:91], a[240:243], a[76:79], v[88:91]\n"
"s_waitcnt lgkmcnt(4)\n"
"v_mfma_f32_16x16x32_bf16 v[92:95], a[248:251], a[76:79], v[92:95]\n"
"ds_read_b64_tr_b16 a[232:233], v197 offset:49152\n"
"v_mfma_f32_16x16x32_bf16 v[92:95], a[252:255], a[92:95], v[92:95]\n"
"ds_read_b64_tr_b16 a[234:235], v197 offset:53248\n"
"v_mfma_f32_16x16x32_bf16 v[84:87], a[252:255], a[88:91], v[84:87]\n"
"v_mfma_f32_16x16x32_bf16 v[84:87], a[248:251], a[72:75], v[84:87]\n"
"v_mfma_f32_16x16x32_bf16 v[76:79], a[248:251], a[68:71], v[76:79]\n"
"ds_read_b64_tr_b16 a[236:237], v197 offset:57344\n"
"v_mfma_f32_16x16x32_bf16 v[76:79], a[252:255], a[84:87], v[76:79]\n"
"ds_read_b64_tr_b16 a[238:239], v197 offset:61440\n"
"v_mfma_f32_16x16x32_bf16 v[68:71], a[252:255], a[80:83], v[68:71]\n"
"v_mfma_f32_16x16x32_bf16 v[68:71], a[248:251], a[64:67], v[68:71]\n"
"s_waitcnt vmcnt(0) lgkmcnt(4)\n"
"v_mfma_f32_16x16x32_bf16 v[64:67], a[224:227], a[96:99], v[64:67]\n"
"ds_read_b64_tr_b16 a[240:241], v198 offset:32768\n"
"v_mfma_f32_16x16x32_bf16 v[64:67], a[228:231], a[112:115], v[64:67]\n"
"ds_read_b64_tr_b16 a[242:243], v198 offset:36864\n"
"v_mfma_f32_16x16x32_bf16 v[72:75], a[228:231], a[116:119], v[72:75]\n"
"v_mfma_f32_16x16x32_bf16 v[72:75], a[224:227], a[100:103], v[72:75]\n"
"v_mfma_f32_16x16x32_bf16 v[80:83], a[224:227], a[104:107], v[80:83]\n"
"ds_read_b64_tr_b16 a[244:245], v198 offset:40960\n"
"v_mfma_f32_16x16x32_bf16 v[80:83], a[228:231], a[120:123], v[80:83]\n"
"ds_read_b64_tr_b16 a[246:247], v198 offset:45056\n"
"v_mfma_f32_16x16x32_bf16 v[88:91], a[228:231], a[124:127], v[88:91]\n"
"v_mfma_f32_16x16x32_bf16 v[88:91], a[224:227], a[108:111], v[88:91]\n"
"s_waitcnt lgkmcnt(4)\n"
"v_mfma_f32_16x16x32_bf16 v[92:95], a[232:235], a[108:111], v[92:95]\n"
"ds_read_b64_tr_b16 a[248:249], v199 offset:32768\n"
"v_mfma_f32_16x16x32_bf16 v[92:95], a[236:239], a[124:127], v[92:95]\n"
"ds_read_b64_tr_b16 a[250:251], v199 offset:36864\n"
"v_mfma_f32_16x16x32_bf16 v[84:87], a[236:239], a[120:123], v[84:87]\n"
"v_mfma_f32_16x16x32_bf16 v[84:87], a[232:235], a[104:107], v[84:87]\n"
"v_mfma_f32_16x16x32_bf16 v[76:79], a[232:235], a[100:103], v[76:79]\n"
"ds_read_b64_tr_b16 a[252:253], v199 offset:40960\n"
"v_mfma_f32_16x16x32_bf16 v[76:79], a[236:239], a[116:119], v[76:79]\n"
"ds_read_b64_tr_b16 a[254:255], v199 offset:45056\n"
"v_mfma_f32_16x16x32_bf16 v[68:71], a[236:239], a[112:115], v[68:71]\n"
"v_mfma_f32_16x16x32_bf16 v[68:71], a[232:235], a[96:99], v[68:71]\n"
"s_waitcnt lgkmcnt(4)\n"
"v_mfma_f32_16x16x32_bf16 v[96:99], a[240:243], a[64:67], v[96:99]\n"
"ds_read_b64_tr_b16 a[224:225], v198 offset:49152\n"
"v_mfma_f32_16x16x32_bf16 v[96:99], a[244:247], a[80:83], v[96:99]\n"
"ds_read_b64_tr_b16 a[226:227], v198 offset:53248\n"
"v_mfma_f32_16x16x32_bf16 v[104:107], a[244:247], a[84:87], v[104:107]\n"
"v_mfma_f32_16x16x32_bf16 v[104:107], a[240:243], a[68:71], v[104:107]\n"
"v_mfma_f32_16x16x32_bf16 v[112:115], a[240:243], a[72:75], v[112:115]\n"
"ds_read_b64_tr_b16 a[228:229], v198 offset:57344\n"
"v_mfma_f32_16x16x32_bf16 v[112:115], a[244:247], a[88:91], v[112:115]\n"
"ds_read_b64_tr_b16 a[230:231], v198 offset:61440\n"
"v_mfma_f32_16x16x32_bf16 v[120:123], a[244:247], a[92:95], v[120:123]\n"
"v_cvt_pk_bf16_f32 v88, v88, v89\n"
"v_cvt_pk_bf16_f32 v89, v90, v91\n"
"v_mfma_f32_16x16x32_bf16 v[120:123], a[240:243], a[76:79], v[120:123]\n"
"v_cvt_pk_bf16_f32 v90, v92, v93\n"
"v_cvt_pk_bf16_f32 v91, v94, v95\n"
"s_waitcnt lgkmcnt(4)\n"
"v_mfma_f32_16x16x32_bf16 v[124:127], a[248:251], a[76:79], v[124:127]\n"
"ds_read_b64_tr_b16 a[232:233], v199 offset:49152\n"
"v_mfma_f32_16x16x32_bf16 v[124:127], a[252:255], a[92:95], v[124:127]\n"
"ds_read_b64_tr_b16 a[234:235], v199 offset:53248\n"
"v_mfma_f32_16x16x32_bf16 v[116:119], a[252:255], a[88:91], v[116:119]\n"
"buffer_store_dwordx4 v[88:91], v219, %7, 0 offen offset:1024 sc1\n"
"v_mfma_f32_16x16x32_bf16 v[116:119], a[248:251], a[72:75], v[116:119]\n"
"v_mfma_f32_16x16x32_bf16 v[108:111], a[248:251], a[68:71], v[108:111]\n"
"ds_read_b64_tr_b16 a[236:237], v199 offset:57344\n"
"v_mfma_f32_16x16x32_bf16 v[108:111], a[252:255], a[84:87], v[108:111]\n"
"ds_read_b64_tr_b16 a[238:239], v199 offset:61440\n"
"v_mfma_f32_16x16x32_bf16 v[100:103], a[252:255], a[80:83], v[100:103]\n"
"v_cvt_pk_bf16_f32 v80, v80, v81\n"
"v_cvt_pk_bf16_f32 v81, v82, v83\n"
"v_mfma_f32_16x16x32_bf16 v[100:103], a[248:251], a[64:67], v[100:103]\n"
"v_cvt_pk_bf16_f32 v82, v84, v85\n"
"v_cvt_pk_bf16_f32 v83, v86, v87\n"
"s_waitcnt lgkmcnt(4)\n"
"v_mfma_f32_16x16x32_bf16 v[96:99], a[224:227], a[96:99], v[96:99]\n"
"ds_read_b64_tr_b16 a[240:241], v200 offset:16384\n"
"v_mfma_f32_16x16x32_bf16 v[96:99], a[228:231], a[112:115], v[96:99]\n"
"ds_read_b64_tr_b16 a[242:243], v200 offset:20480\n"
"v_mfma_f32_16x16x32_bf16 v[104:107], a[228:231], a[116:119], v[104:107]\n"
"buffer_store_dwordx4 v[80:83], v219, %7, 0 offen offset:0 sc1\n"
"v_mfma_f32_16x16x32_bf16 v[104:107], a[224:227], a[100:103], v[104:107]\n"
"v_mfma_f32_16x16x32_bf16 v[112:115], a[224:227], a[104:107], v[112:115]\n"
"ds_read_b64_tr_b16 a[244:245], v200 offset:24576\n"
"v_mfma_f32_16x16x32_bf16 v[112:115], a[228:231], a[120:123], v[112:115]\n"
"ds_read_b64_tr_b16 a[246:247], v200 offset:28672\n"
"v_mfma_f32_16x16x32_bf16 v[120:123], a[228:231], a[124:127], v[120:123]\n"
"v_cvt_pk_bf16_f32 v72, v72, v73\n"
"v_cvt_pk_bf16_f32 v73, v74, v75\n"
"v_mfma_f32_16x16x32_bf16 v[120:123], a[224:227], a[108:111], v[120:123]\n"
"v_cvt_pk_bf16_f32 v74, v76, v77\n"
"v_cvt_pk_bf16_f32 v75, v78, v79\n"
"s_waitcnt lgkmcnt(4)\n"
"v_mfma_f32_16x16x32_bf16 v[124:127], a[232:235], a[108:111], v[124:127]\n"
"ds_read_b64_tr_b16 a[248:249], v201 offset:16384\n"
"v_mfma_f32_16x16x32_bf16 v[124:127], a[236:239], a[124:127], v[124:127]\n"
"ds_read_b64_tr_b16 a[250:251], v201 offset:20480\n"
"v_mfma_f32_16x16x32_bf16 v[116:119], a[236:239], a[120:123], v[116:119]\n"
"buffer_store_dwordx4 v[72:75], v218, %7, 0 offen offset:1024 sc1\n"
"v_mfma_f32_16x16x32_bf16 v[116:119], a[232:235], a[104:107], v[116:119]\n"
"v_mfma_f32_16x16x32_bf16 v[108:111], a[232:235], a[100:103], v[108:111]\n"
"ds_read_b64_tr_b16 a[252:253], v201 offset:24576\n"
"v_mfma_f32_16x16x32_bf16 v[108:111], a[236:239], a[116:119], v[108:111]\n"
"ds_read_b64_tr_b16 a[254:255], v201 offset:28672\n"
"v_mfma_f32_16x16x32_bf16 v[100:103], a[236:239], a[112:115], v[100:103]\n"
"v_cvt_pk_bf16_f32 v64, v64, v65\n"
"v_cvt_pk_bf16_f32 v65, v66, v67\n"
"v_mfma_f32_16x16x32_bf16 v[100:103], a[232:235], a[96:99], v[100:103]\n"
"v_cvt_pk_bf16_f32 v66, v68, v69\n"
"v_cvt_pk_bf16_f32 v67, v70, v71\n"
"s_waitcnt lgkmcnt(4)\n"
"v_mfma_f32_16x16x32_bf16 v[128:131], a[240:243], a[32:35], v[128:131]\n"
"ds_read_b64_tr_b16 a[224:225], v200 offset:32768\n"
"v_mfma_f32_16x16x32_bf16 v[128:131], a[244:247], a[48:51], v[128:131]\n"
"ds_read_b64_tr_b16 a[226:227], v200 offset:36864\n"
"v_mfma_f32_16x16x32_bf16 v[136:139], a[244:247], a[52:55], v[136:139]\n"
"buffer_store_dwordx4 v[64:67], v218, %7, 0 offen offset:0 sc1\n"
"v_mfma_f32_16x16x32_bf16 v[136:139], a[240:243], a[36:39], v[136:139]\n"
"v_mfma_f32_16x16x32_bf16 v[144:147], a[240:243], a[40:43], v[144:147]\n"
"ds_read_b64_tr_b16 a[228:229], v200 offset:40960\n"
"v_mfma_f32_16x16x32_bf16 v[144:147], a[244:247], a[56:59], v[144:147]\n"
"ds_read_b64_tr_b16 a[230:231], v200 offset:45056\n"
"v_mfma_f32_16x16x32_bf16 v[152:155], a[244:247], a[60:63], v[152:155]\n"
"v_cvt_pk_bf16_f32 v120, v120, v121\n"
"v_cvt_pk_bf16_f32 v121, v122, v123\n"
"v_mfma_f32_16x16x32_bf16 v[152:155], a[240:243], a[44:47], v[152:155]\n"
"v_cvt_pk_bf16_f32 v122, v124, v125\n"
"v_cvt_pk_bf16_f32 v123, v126, v127\n"
"s_waitcnt lgkmcnt(4)\n"
"v_mfma_f32_16x16x32_bf16 v[156:159], a[248:251], a[44:47], v[156:159]\n"
"ds_read_b64_tr_b16 a[232:233], v201 offset:32768\n"
"v_mfma_f32_16x16x32_bf16 v[156:159], a[252:255], a[60:63], v[156:159]\n"
"ds_read_b64_tr_b16 a[234:235], v201 offset:36864\n"
"v_mfma_f32_16x16x32_bf16 v[148:151], a[252:255], a[56:59], v[148:151]\n"
"buffer_store_dwordx4 v[120:123], v219, %7, 0 offen offset:3072 sc1\n"
"v_mfma_f32_16x16x32_bf16 v[148:151], a[248:251], a[40:43], v[148:151]\n"
"v_mfma_f32_16x16x32_bf16 v[140:143], a[248:251], a[36:39], v[140:143]\n"
"ds_read_b64_tr_b16 a[236:237], v201 offset:40960\n"
"v_mfma_f32_16x16x32_bf16 v[140:143], a[252:255], a[52:55], v[140:143]\n"
"ds_read_b64_tr_b16 a[238:239], v201 offset:45056\n"
"v_mfma_f32_16x16x32_bf16 v[132:135], a[252:255], a[48:51], v[132:135]\n"
"v_mfma_f32_16x16x32_bf16 v[132:135], a[248:251], a[32:35], v[132:135]\n"
"s_waitcnt lgkmcnt(4)\n"
"v_mfma_f32_16x16x32_bf16 v[128:131], a[224:227], a[64:67], v[128:131]\n"
"ds_read_b64_tr_b16 a[240:241], v200 offset:49152\n"
"v_mfma_f32_16x16x32_bf16 v[128:131], a[228:231], a[80:83], v[128:131]\n"
"ds_read_b64_tr_b16 a[242:243], v200 offset:53248\n"
"v_mfma_f32_16x16x32_bf16 v[136:139], a[228:231], a[84:87], v[136:139]\n"
"v_cvt_pk_bf16_f32 v112, v112, v113\n"
"v_cvt_pk_bf16_f32 v113, v114, v115\n"
"v_mfma_f32_16x16x32_bf16 v[136:139], a[224:227], a[68:71], v[136:139]\n"
"v_cvt_pk_bf16_f32 v114, v116, v117\n"
"v_cvt_pk_bf16_f32 v115, v118, v119\n"
"v_mfma_f32_16x16x32_bf16 v[144:147], a[224:227], a[72:75], v[144:147]\n"
"ds_read_b64_tr_b16 a[244:245], v200 offset:57344\n"
"v_mfma_f32_16x16x32_bf16 v[144:147], a[228:231], a[88:91], v[144:147]\n"
"ds_read_b64_tr_b16 a[246:247], v200 offset:61440\n"
"v_mfma_f32_16x16x32_bf16 v[152:155], a[228:231], a[92:95], v[152:155]\n"
"buffer_store_dwordx4 v[112:115], v219, %7, 0 offen offset:2048 sc1\n"
"v_mfma_f32_16x16x32_bf16 v[152:155], a[224:227], a[76:79], v[152:155]\n"
"s_waitcnt lgkmcnt(4)\n"
"v_mfma_f32_16x16x32_bf16 v[156:159], a[232:235], a[76:79], v[156:159]\n"
"ds_read_b64_tr_b16 a[248:249], v201 offset:49152\n"
"v_mfma_f32_16x16x32_bf16 v[156:159], a[236:239], a[92:95], v[156:159]\n"
"ds_read_b64_tr_b16 a[250:251], v201 offset:53248\n"
"v_mfma_f32_16x16x32_bf16 v[148:151], a[236:239], a[88:91], v[148:151]\n"
"v_mfma_f32_16x16x32_bf16 v[148:151], a[232:235], a[72:75], v[148:151]\n"
"v_mfma_f32_16x16x32_bf16 v[140:143], a[232:235], a[68:71], v[140:143]\n"
"ds_read_b64_tr_b16 a[252:253], v201 offset:57344\n"
"v_mfma_f32_16x16x32_bf16 v[140:143], a[236:239], a[84:87], v[140:143]\n"
"ds_read_b64_tr_b16 a[254:255], v201 offset:61440\n"
"v_mfma_f32_16x16x32_bf16 v[132:135], a[236:239], a[80:83], v[132:135]\n"
"v_cvt_pk_bf16_f32 v104, v104, v105\n"
"v_cvt_pk_bf16_f32 v105, v106, v107\n"
"v_mfma_f32_16x16x32_bf16 v[132:135], a[232:235], a[64:67], v[132:135]\n"
"v_cvt_pk_bf16_f32 v106, v108, v109\n"
"v_cvt_pk_bf16_f32 v107, v110, v111\n"
"s_waitcnt lgkmcnt(4)\n"
"v_mfma_f32_16x16x32_bf16 v[128:131], a[240:243], a[96:99], v[128:131]\n"
"ds_read_b64_tr_b16 a[224:225], v202 offset:0\n"
"v_mfma_f32_16x16x32_bf16 v[128:131], a[244:247], a[112:115], v[128:131]\n"
"ds_read_b64_tr_b16 a[226:227], v202 offset:4096\n"
"v_mfma_f32_16x16x32_bf16 v[136:139], a[244:247], a[116:119], v[136:139]\n"
"buffer_store_dwordx4 v[104:107], v218, %7, 0 offen offset:3072 sc1\n"
"v_mfma_f32_16x16x32_bf16 v[136:139], a[240:243], a[100:103], v[136:139]\n"
"v_mfma_f32_16x16x32_bf16 v[144:147], a[240:243], a[104:107], v[144:147]\n"
"ds_read_b64_tr_b16 a[228:229], v202 offset:8192\n"
"v_mfma_f32_16x16x32_bf16 v[144:147], a[244:247], a[120:123], v[144:147]\n"
"ds_read_b64_tr_b16 a[230:231], v202 offset:12288\n"
"v_mfma_f32_16x16x32_bf16 v[152:155], a[244:247], a[124:127], v[152:155]\n"
"v_mfma_f32_16x16x32_bf16 v[152:155], a[240:243], a[108:111], v[152:155]\n"
"s_waitcnt lgkmcnt(4)\n"
"v_mfma_f32_16x16x32_bf16 v[156:159], a[248:251], a[108:111], v[156:159]\n"
"ds_read_b64_tr_b16 a[232:233], v203 offset:0\n"
"v_mfma_f32_16x16x32_bf16 v[156:159], a[252:255], a[124:127], v[156:159]\n"
"ds_read_b64_tr_b16 a[234:235], v203 offset:4096\n"
"v_mfma_f32_16x16x32_bf16 v[148:151], a[252:255], a[120:123], v[148:151]\n"
"v_cvt_pk_bf16_f32 v96, v96, v97\n"
"v_cvt_pk_bf16_f32 v97, v98, v99\n"
"v_mfma_f32_16x16x32_bf16 v[148:151], a[248:251], a[104:107], v[148:151]\n"
"v_cvt_pk_bf16_f32 v98, v100, v101\n"
"v_cvt_pk_bf16_f32 v99, v102, v103\n"
"v_mfma_f32_16x16x32_bf16 v[140:143], a[248:251], a[100:103], v[140:143]\n"
"ds_read_b64_tr_b16 a[236:237], v203 offset:8192\n"
"v_mfma_f32_16x16x32_bf16 v[140:143], a[252:255], a[116:119], v[140:143]\n"
"ds_read_b64_tr_b16 a[238:239], v203 offset:12288\n"
"v_mfma_f32_16x16x32_bf16 v[132:135], a[252:255], a[112:115], v[132:135]\n"
"buffer_store_dwordx4 v[96:99], v218, %7, 0 offen offset:2048 sc1\n"
"v_mfma_f32_16x16x32_bf16 v[132:135], a[248:251], a[96:99], v[132:135]\n"
"s_waitcnt lgkmcnt(4)\n"
"v_mfma_f32_16x16x32_bf16 v[160:163], a[224:227], a[0:3], v[160:163]\n"
"ds_read_b64_tr_b16 a[240:241], v202 offset:16384\n"
"v_mfma_f32_16x16x32_bf16 v[160:163], a[228:231], a[16:19], v[160:163]\n"
"ds_read_b64_tr_b16 a[242:243], v202 offset:20480\n"
"v_mfma_f32_16x16x32_bf16 v[168:171], a[228:231], a[20:23], v[168:171]\n"
"v_mfma_f32_16x16x32_bf16 v[168:171], a[224:227], a[4:7], v[168:171]\n"
"v_mfma_f32_16x16x32_bf16 v[176:179], a[224:227], a[8:11], v[176:179]\n"
"ds_read_b64_tr_b16 a[244:245], v202 offset:24576\n"
"v_mfma_f32_16x16x32_bf16 v[176:179], a[228:231], a[24:27], v[176:179]\n"
"ds_read_b64_tr_b16 a[246:247], v202 offset:28672\n"
"v_mfma_f32_16x16x32_bf16 v[184:187], a[228:231], a[28:31], v[184:187]\n"
"v_cvt_pk_bf16_f32 v152, v152, v153\n"
"v_cvt_pk_bf16_f32 v153, v154, v155\n"
"v_mfma_f32_16x16x32_bf16 v[184:187], a[224:227], a[12:15], v[184:187]\n"
"v_cvt_pk_bf16_f32 v154, v156, v157\n"
"v_cvt_pk_bf16_f32 v155, v158, v159\n"
"s_waitcnt lgkmcnt(4)\n"
"v_mfma_f32_16x16x32_bf16 v[188:191], a[232:235], a[12:15], v[188:191]\n"
"ds_read_b64_tr_b16 a[248:249], v203 offset:16384\n"
"v_mfma_f32_16x16x32_bf16 v[188:191], a[236:239], a[28:31], v[188:191]\n"
"ds_read_b64_tr_b16 a[250:251], v203 offset:20480\n"
"v_mfma_f32_16x16x32_bf16 v[180:183], a[236:239], a[24:27], v[180:183]\n"
"buffer_store_dwordx4 v[152:155], v219, %7, s70 offen offset:1024 sc1\n"
"v_mfma_f32_16x16x32_bf16 v[180:183], a[232:235], a[8:11], v[180:183]\n"
"v_mfma_f32_16x16x32_bf16 v[172:175], a[232:235], a[4:7], v[172:175]\n"
"ds_read_b64_tr_b16 a[252:253], v203 offset:24576\n"
"v_mfma_f32_16x16x32_bf16 v[172:175], a[236:239], a[20:23], v[172:175]\n"
"ds_read_b64_tr_b16 a[254:255], v203 offset:28672\n"
"v_mfma_f32_16x16x32_bf16 v[164:167], a[236:239], a[16:19], v[164:167]\n"
"v_mfma_f32_16x16x32_bf16 v[164:167], a[232:235], a[0:3], v[164:167]\n"
"s_waitcnt lgkmcnt(4)\n"
"v_mfma_f32_16x16x32_bf16 v[160:163], a[240:243], a[32:35], v[160:163]\n"
"ds_read_b64_tr_b16 a[224:225], v202 offset:32768\n"
"v_mfma_f32_16x16x32_bf16 v[160:163], a[244:247], a[48:51], v[160:163]\n"
"ds_read_b64_tr_b16 a[226:227], v202 offset:36864\n"
"v_mfma_f32_16x16x32_bf16 v[168:171], a[244:247], a[52:55], v[168:171]\n"
"v_mfma_f32_16x16x32_bf16 v[168:171], a[240:243], a[36:39], v[168:171]\n"
"v_mfma_f32_16x16x32_bf16 v[176:179], a[240:243], a[40:43], v[176:179]\n"
"ds_read_b64_tr_b16 a[228:229], v202 offset:40960\n"
"v_mfma_f32_16x16x32_bf16 v[176:179], a[244:247], a[56:59], v[176:179]\n"
"ds_read_b64_tr_b16 a[230:231], v202 offset:45056\n"
"v_mfma_f32_16x16x32_bf16 v[184:187], a[244:247], a[60:63], v[184:187]\n"
"v_cvt_pk_bf16_f32 v144, v144, v145\n"
"v_cvt_pk_bf16_f32 v145, v146, v147\n"
"v_mfma_f32_16x16x32_bf16 v[184:187], a[240:243], a[44:47], v[184:187]\n"
"v_cvt_pk_bf16_f32 v146, v148, v149\n"
"v_cvt_pk_bf16_f32 v147, v150, v151\n"
"s_waitcnt lgkmcnt(4)\n"
"v_mfma_f32_16x16x32_bf16 v[188:191], a[248:251], a[44:47], v[188:191]\n"
"ds_read_b64_tr_b16 a[232:233], v203 offset:32768\n"
"v_mfma_f32_16x16x32_bf16 v[188:191], a[252:255], a[60:63], v[188:191]\n"
"ds_read_b64_tr_b16 a[234:235], v203 offset:36864\n"
"v_mfma_f32_16x16x32_bf16 v[180:183], a[252:255], a[56:59], v[180:183]\n"
"buffer_store_dwordx4 v[144:147], v219, %7, s70 offen offset:0 sc1\n"
"v_mfma_f32_16x16x32_bf16 v[180:183], a[248:251], a[40:43], v[180:183]\n"
"v_mfma_f32_16x16x32_bf16 v[172:175], a[248:251], a[36:39], v[172:175]\n"
"ds_read_b64_tr_b16 a[236:237], v203 offset:40960\n"
"v_mfma_f32_16x16x32_bf16 v[172:175], a[252:255], a[52:55], v[172:175]\n"
"ds_read_b64_tr_b16 a[238:239], v203 offset:45056\n"
"v_mfma_f32_16x16x32_bf16 v[164:167], a[252:255], a[48:51], v[164:167]\n"
"v_mfma_f32_16x16x32_bf16 v[164:167], a[248:251], a[32:35], v[164:167]\n"
"s_waitcnt lgkmcnt(4)\n"
"v_mfma_f32_16x16x32_bf16 v[160:163], a[224:227], a[64:67], v[160:163]\n"
"ds_read_b64_tr_b16 a[240:241], v202 offset:49152\n"
"v_mfma_f32_16x16x32_bf16 v[160:163], a[228:231], a[80:83], v[160:163]\n"
"ds_read_b64_tr_b16 a[242:243], v202 offset:53248\n"
"v_mfma_f32_16x16x32_bf16 v[168:171], a[228:231], a[84:87], v[168:171]\n"
"v_mfma_f32_16x16x32_bf16 v[168:171], a[224:227], a[68:71], v[168:171]\n"
"v_mfma_f32_16x16x32_bf16 v[176:179], a[224:227], a[72:75], v[176:179]\n"
"ds_read_b64_tr_b16 a[244:245], v202 offset:57344\n"
"v_mfma_f32_16x16x32_bf16 v[176:179], a[228:231], a[88:91], v[176:179]\n"
"ds_read_b64_tr_b16 a[246:247], v202 offset:61440\n"
"v_mfma_f32_16x16x32_bf16 v[184:187], a[228:231], a[92:95], v[184:187]\n"
"v_cvt_pk_bf16_f32 v136, v136, v137\n"
"v_cvt_pk_bf16_f32 v137, v138, v139\n"
"v_mfma_f32_16x16x32_bf16 v[184:187], a[224:227], a[76:79], v[184:187]\n"
"v_cvt_pk_bf16_f32 v138, v140, v141\n"
"v_cvt_pk_bf16_f32 v139, v142, v143\n"
"s_waitcnt lgkmcnt(4)\n"
"v_mfma_f32_16x16x32_bf16 v[188:191], a[232:235], a[76:79], v[188:191]\n"
"ds_read_b64_tr_b16 a[248:249], v203 offset:49152\n"
"v_mfma_f32_16x16x32_bf16 v[188:191], a[236:239], a[92:95], v[188:191]\n"
"ds_read_b64_tr_b16 a[250:251], v203 offset:53248\n"
"v_mfma_f32_16x16x32_bf16 v[180:183], a[236:239], a[88:91], v[180:183]\n"
"buffer_store_dwordx4 v[136:139], v218, %7, s70 offen offset:1024 sc1\n"
"v_mfma_f32_16x16x32_bf16 v[180:183], a[232:235], a[72:75], v[180:183]\n"
"v_mfma_f32_16x16x32_bf16 v[172:175], a[232:235], a[68:71], v[172:175]\n"
"ds_read_b64_tr_b16 a[252:253], v203 offset:57344\n"
"v_mfma_f32_16x16x32_bf16 v[172:175], a[236:239], a[84:87], v[172:175]\n"
"ds_read_b64_tr_b16 a[254:255], v203 offset:61440\n"
"v_mfma_f32_16x16x32_bf16 v[164:167], a[236:239], a[80:83], v[164:167]\n"
"v_mfma_f32_16x16x32_bf16 v[164:167], a[232:235], a[64:67], v[164:167]\n"
"s_waitcnt lgkmcnt(4)\n"
"v_mfma_f32_16x16x32_bf16 v[160:163], a[240:243], a[96:99], v[160:163]\n"
"v_mfma_f32_16x16x32_bf16 v[160:163], a[244:247], a[112:115], v[160:163]\n"
"v_mfma_f32_16x16x32_bf16 v[168:171], a[244:247], a[116:119], v[168:171]\n"
"v_mfma_f32_16x16x32_bf16 v[168:171], a[240:243], a[100:103], v[168:171]\n"
"v_mfma_f32_16x16x32_bf16 v[176:179], a[240:243], a[104:107], v[176:179]\n"
"v_mfma_f32_16x16x32_bf16 v[176:179], a[244:247], a[120:123], v[176:179]\n"
"v_mfma_f32_16x16x32_bf16 v[184:187], a[244:247], a[124:127], v[184:187]\n"
"v_cvt_pk_bf16_f32 v128, v128, v129\n"
"v_cvt_pk_bf16_f32 v129, v130, v131\n"
"v_mfma_f32_16x16x32_bf16 v[184:187], a[240:243], a[108:111], v[184:187]\n"
"v_cvt_pk_bf16_f32 v130, v132, v133\n"
"v_cvt_pk_bf16_f32 v131, v134, v135\n"
"s_waitcnt lgkmcnt(0)\n"
"v_mfma_f32_16x16x32_bf16 v[188:191], a[248:251], a[108:111], v[188:191]\n"
"v_mfma_f32_16x16x32_bf16 v[188:191], a[252:255], a[124:127], v[188:191]\n"
"v_mfma_f32_16x16x32_bf16 v[180:183], a[252:255], a[120:123], v[180:183]\n"
"buffer_store_dwordx4 v[128:131], v218, %7, s70 offen offset:0 sc1\n"
"v_mfma_f32_16x16x32_bf16 v[180:183], a[248:251], a[104:107], v[180:183]\n"
"v_mfma_f32_16x16x32_bf16 v[172:175], a[248:251], a[100:103], v[172:175]\n"
"v_mfma_f32_16x16x32_bf16 v[172:175], a[252:255], a[116:119], v[172:175]\n"
"v_mfma_f32_16x16x32_bf16 v[164:167], a[252:255], a[112:115], v[164:167]\n"
"v_mfma_f32_16x16x32_bf16 v[164:167], a[248:251], a[96:99], v[164:167]\n"
"s_nop 4\n"
"v_cvt_pk_bf16_f32 v184, v184, v185\n"
"v_cvt_pk_bf16_f32 v185, v186, v187\n"
"v_cvt_pk_bf16_f32 v186, v188, v189\n"
"v_cvt_pk_bf16_f32 v187, v190, v191\n"
"v_cvt_pk_bf16_f32 v176, v176, v177\n"
"v_cvt_pk_bf16_f32 v177, v178, v179\n"
"v_cvt_pk_bf16_f32 v178, v180, v181\n"
"v_cvt_pk_bf16_f32 v179, v182, v183\n"
"buffer_store_dwordx4 v[184:187], v219, %7, s70 offen offset:3072 sc1\n"
"v_cvt_pk_bf16_f32 v168, v168, v169\n"
"v_cvt_pk_bf16_f32 v169, v170, v171\n"
"v_cvt_pk_bf16_f32 v170, v172, v173\n"
"v_cvt_pk_bf16_f32 v171, v174, v175\n"
"buffer_store_dwordx4 v[176:179], v219, %7, s70 offen offset:2048 sc1\n"
"v_cvt_pk_bf16_f32 v160, v160, v161\n"
"v_cvt_pk_bf16_f32 v161, v162, v163\n"
"v_cvt_pk_bf16_f32 v162, v164, v165\n"
"v_cvt_pk_bf16_f32 v163, v166, v167\n"
"buffer_store_dwordx4 v[168:171], v218, %7, s70 offen offset:3072 sc1\n"
"buffer_store_dwordx4 v[160:163], v218, %7, s70 offen offset:2048 sc1\n"
"s_branch DONE%=\n"
"FALLBACK%=:\n"
"s_waitcnt vmcnt(0) lgkmcnt(0)\n"
"s_mov_b32 s69, 1\n"
"v_xor_b32 v192, 0, %13\n"
"v_xor_b32 v193, 64, %13\n"
"v_xor_b32 v194, 128, %13\n"
"v_xor_b32 v195, 192, %13\n"
"v_xor_b32 v196, 0, %14\n"
"v_xor_b32 v197, 32, %14\n"
"v_xor_b32 v198, 64, %14\n"
"v_xor_b32 v199, 96, %14\n"
"v_xor_b32 v200, 128, %14\n"
"v_xor_b32 v201, 160, %14\n"
"v_xor_b32 v202, 192, %14\n"
"v_xor_b32 v203, 224, %14\n"
"v_mov_b32 v232, 0xff800000\n"
"v_mov_b32 v233, 0xff800000\n"
"v_mov_b32 v234, 0xff800000\n"
"v_mov_b32 v235, 0xff800000\n"
"s_mov_b32 s67, %11\n"
"s_mov_b32 s66, 32\n"
"s_mov_b32 m0, %11\n"
"s_waitcnt vmcnt(0) lgkmcnt(0)\n"
"LOOP_M%=:\n"
"s_barrier\n"
"buffer_load_dwordx4 v204, %2, s67 offen offset:0 lds\n"
"buffer_load_dwordx4 v205, %2, s67 offen offset:1024 lds\n"
"buffer_load_dwordx4 v206, %2, s67 offen offset:2048 lds\n"
"buffer_load_dwordx4 v207, %2, s67 offen offset:3072 lds\n"
"s_waitcnt vmcnt(0)\n"
"s_barrier\n"
"ds_read_b128 a[192:195], v192 offset:0\n"
"ds_read_b128 a[196:199], v192 offset:4096\n"
"ds_read_b128 a[200:203], v193 offset:0\n"
"ds_read_b128 a[204:207], v193 offset:4096\n"
"ds_read_b128 a[208:211], v194 offset:0\n"
"ds_read_b128 a[212:215], v194 offset:4096\n"
"ds_read_b128 a[216:219], v195 offset:0\n"
"ds_read_b128 a[220:223], v195 offset:4096\n"
"s_waitcnt lgkmcnt(0)\n"
"v_mfma_f32_16x16x32_bf16 v[0:3], a[192:195], v[128:131], 0\n"
"v_mfma_f32_16x16x32_bf16 v[0:3], a[200:203], v[132:135], v[0:3]\n"
"v_mfma_f32_16x16x32_bf16 v[0:3], a[208:211], v[136:139], v[0:3]\n"
"v_mfma_f32_16x16x32_bf16 v[0:3], a[216:219], v[140:143], v[0:3]\n"
"v_mfma_f32_16x16x32_bf16 v[8:11], a[216:219], v[156:159], 0\n"
"v_mfma_f32_16x16x32_bf16 v[8:11], a[208:211], v[152:155], v[8:11]\n"
"v_mfma_f32_16x16x32_bf16 v[8:11], a[200:203], v[148:151], v[8:11]\n"
"v_mfma_f32_16x16x32_bf16 v[8:11], a[192:195], v[144:147], v[8:11]\n"
"v_mfma_f32_16x16x32_bf16 v[16:19], a[192:195], v[160:163], 0\n"
"v_mfma_f32_16x16x32_bf16 v[16:19], a[200:203], v[164:167], v[16:19]\n"
"v_mfma_f32_16x16x32_bf16 v[16:19], a[208:211], v[168:171], v[16:19]\n"
"v_mfma_f32_16x16x32_bf16 v[16:19], a[216:219], v[172:175], v[16:19]\n"
"v_mfma_f32_16x16x32_bf16 v[24:27], a[216:219], v[188:191], 0\n"
"v_mfma_f32_16x16x32_bf16 v[24:27], a[208:211], v[184:187], v[24:27]\n"
"v_mfma_f32_16x16x32_bf16 v[24:27], a[200:203], v[180:183], v[24:27]\n"
"v_mfma_f32_16x16x32_bf16 v[24:27], a[192:195], v[176:179], v[24:27]\n"
"v_mfma_f32_16x16x32_bf16 v[28:31], a[196:199], v[176:179], 0\n"
"v_mfma_f32_16x16x32_bf16 v[28:31], a[204:207], v[180:183], v[28:31]\n"
"v_mfma_f32_16x16x32_bf16 v[28:31], a[212:215], v[184:187], v[28:31]\n"
"v_mfma_f32_16x16x32_bf16 v[28:31], a[220:223], v[188:191], v[28:31]\n"
"v_mfma_f32_16x16x32_bf16 v[20:23], a[220:223], v[172:175], 0\n"
"v_mfma_f32_16x16x32_bf16 v[20:23], a[212:215], v[168:171], v[20:23]\n"
"v_mfma_f32_16x16x32_bf16 v[20:23], a[204:207], v[164:167], v[20:23]\n"
"v_mfma_f32_16x16x32_bf16 v[20:23], a[196:199], v[160:163], v[20:23]\n"
"v_mfma_f32_16x16x32_bf16 v[12:15], a[196:199], v[144:147], 0\n"
"v_mfma_f32_16x16x32_bf16 v[12:15], a[204:207], v[148:151], v[12:15]\n"
"v_mfma_f32_16x16x32_bf16 v[12:15], a[212:215], v[152:155], v[12:15]\n"
"v_mfma_f32_16x16x32_bf16 v[12:15], a[220:223], v[156:159], v[12:15]\n"
"v_mfma_f32_16x16x32_bf16 v[4:7], a[220:223], v[140:143], 0\n"
"v_mfma_f32_16x16x32_bf16 v[4:7], a[212:215], v[136:139], v[4:7]\n"
"v_mfma_f32_16x16x32_bf16 v[4:7], a[204:207], v[132:135], v[4:7]\n"
"v_mfma_f32_16x16x32_bf16 v[4:7], a[196:199], v[128:131], v[4:7]\n"
"s_nop 15\n"
"v_max3_f32 v232, v0, v1, v232\n"
"v_max3_f32 v233, v8, v9, v233\n"
"v_max3_f32 v234, v16, v17, v234\n"
"v_max3_f32 v235, v24, v25, v235\n"
"v_max3_f32 v232, v2, v3, v232\n"
"v_max3_f32 v233, v10, v11, v233\n"
"v_max3_f32 v234, v18, v19, v234\n"
"v_max3_f32 v235, v26, v27, v235\n"
"v_max3_f32 v232, v4, v5, v232\n"
"v_max3_f32 v233, v12, v13, v233\n"
"v_max3_f32 v234, v20, v21, v234\n"
"v_max3_f32 v235, v28, v29, v235\n"
"v_max3_f32 v232, v6, v7, v232\n"
"v_max3_f32 v233, v14, v15, v233\n"
"v_max3_f32 v234, v22, v23, v234\n"
"v_max3_f32 v235, v30, v31, v235\n"
"ds_read_b128 a[192:195], v192 offset:8192\n"
"ds_read_b128 a[196:199], v192 offset:12288\n"
"ds_read_b128 a[200:203], v193 offset:8192\n"
"ds_read_b128 a[204:207], v193 offset:12288\n"
"ds_read_b128 a[208:211], v194 offset:8192\n"
"ds_read_b128 a[212:215], v194 offset:12288\n"
"ds_read_b128 a[216:219], v195 offset:8192\n"
"ds_read_b128 a[220:223], v195 offset:12288\n"
"s_waitcnt lgkmcnt(0)\n"
"v_mfma_f32_16x16x32_bf16 v[0:3], a[192:195], v[128:131], 0\n"
"v_mfma_f32_16x16x32_bf16 v[0:3], a[200:203], v[132:135], v[0:3]\n"
"v_mfma_f32_16x16x32_bf16 v[0:3], a[208:211], v[136:139], v[0:3]\n"
"v_mfma_f32_16x16x32_bf16 v[0:3], a[216:219], v[140:143], v[0:3]\n"
"v_mfma_f32_16x16x32_bf16 v[8:11], a[216:219], v[156:159], 0\n"
"v_mfma_f32_16x16x32_bf16 v[8:11], a[208:211], v[152:155], v[8:11]\n"
"v_mfma_f32_16x16x32_bf16 v[8:11], a[200:203], v[148:151], v[8:11]\n"
"v_mfma_f32_16x16x32_bf16 v[8:11], a[192:195], v[144:147], v[8:11]\n"
"v_mfma_f32_16x16x32_bf16 v[16:19], a[192:195], v[160:163], 0\n"
"v_mfma_f32_16x16x32_bf16 v[16:19], a[200:203], v[164:167], v[16:19]\n"
"v_mfma_f32_16x16x32_bf16 v[16:19], a[208:211], v[168:171], v[16:19]\n"
"v_mfma_f32_16x16x32_bf16 v[16:19], a[216:219], v[172:175], v[16:19]\n"
"v_mfma_f32_16x16x32_bf16 v[24:27], a[216:219], v[188:191], 0\n"
"v_mfma_f32_16x16x32_bf16 v[24:27], a[208:211], v[184:187], v[24:27]\n"
"v_mfma_f32_16x16x32_bf16 v[24:27], a[200:203], v[180:183], v[24:27]\n"
"v_mfma_f32_16x16x32_bf16 v[24:27], a[192:195], v[176:179], v[24:27]\n"
"v_mfma_f32_16x16x32_bf16 v[28:31], a[196:199], v[176:179], 0\n"
"v_mfma_f32_16x16x32_bf16 v[28:31], a[204:207], v[180:183], v[28:31]\n"
"v_mfma_f32_16x16x32_bf16 v[28:31], a[212:215], v[184:187], v[28:31]\n"
"v_mfma_f32_16x16x32_bf16 v[28:31], a[220:223], v[188:191], v[28:31]\n"
"v_mfma_f32_16x16x32_bf16 v[20:23], a[220:223], v[172:175], 0\n"
"v_mfma_f32_16x16x32_bf16 v[20:23], a[212:215], v[168:171], v[20:23]\n"
"v_mfma_f32_16x16x32_bf16 v[20:23], a[204:207], v[164:167], v[20:23]\n"
"v_mfma_f32_16x16x32_bf16 v[20:23], a[196:199], v[160:163], v[20:23]\n"
"v_mfma_f32_16x16x32_bf16 v[12:15], a[196:199], v[144:147], 0\n"
"v_mfma_f32_16x16x32_bf16 v[12:15], a[204:207], v[148:151], v[12:15]\n"
"v_mfma_f32_16x16x32_bf16 v[12:15], a[212:215], v[152:155], v[12:15]\n"
"v_mfma_f32_16x16x32_bf16 v[12:15], a[220:223], v[156:159], v[12:15]\n"
"v_mfma_f32_16x16x32_bf16 v[4:7], a[220:223], v[140:143], 0\n"
"v_mfma_f32_16x16x32_bf16 v[4:7], a[212:215], v[136:139], v[4:7]\n"
"v_mfma_f32_16x16x32_bf16 v[4:7], a[204:207], v[132:135], v[4:7]\n"
"v_mfma_f32_16x16x32_bf16 v[4:7], a[196:199], v[128:131], v[4:7]\n"
"s_nop 15\n"
"v_max3_f32 v232, v0, v1, v232\n"
"v_max3_f32 v233, v8, v9, v233\n"
"v_max3_f32 v234, v16, v17, v234\n"
"v_max3_f32 v235, v24, v25, v235\n"
"v_max3_f32 v232, v2, v3, v232\n"
"v_max3_f32 v233, v10, v11, v233\n"
"v_max3_f32 v234, v18, v19, v234\n"
"v_max3_f32 v235, v26, v27, v235\n"
"v_max3_f32 v232, v4, v5, v232\n"
"v_max3_f32 v233, v12, v13, v233\n"
"v_max3_f32 v234, v20, v21, v234\n"
"v_max3_f32 v235, v28, v29, v235\n"
"v_max3_f32 v232, v6, v7, v232\n"
"v_max3_f32 v233, v14, v15, v233\n"
"v_max3_f32 v234, v22, v23, v234\n"
"v_max3_f32 v235, v30, v31, v235\n"
"s_add_u32 s67, s67, 16384\n"
"s_sub_u32 s66, s66, 1\n"
"s_cmp_lg_u32 s66, 0\n"
"s_cbranch_scc1 LOOP_M%=\n"
"v_mov_b32 v236, v232\n"
"v_mov_b32 v237, v233\n"
"v_mov_b32 v238, v234\n"
"v_mov_b32 v239, v235\n"
"s_nop 1\n"
"v_permlane16_swap_b32 v232, v236\n"
"v_permlane16_swap_b32 v233, v237\n"
"v_permlane16_swap_b32 v234, v238\n"
"v_permlane16_swap_b32 v235, v239\n"
"v_max_f32 v232, v232, v236\n"
"v_max_f32 v233, v233, v237\n"
"v_max_f32 v234, v234, v238\n"
"v_max_f32 v235, v235, v239\n"
"v_mov_b32 v236, v232\n"
"v_mov_b32 v237, v233\n"
"v_mov_b32 v238, v234\n"
"v_mov_b32 v239, v235\n"
"s_nop 1\n"
"v_permlane32_swap_b32 v232, v236\n"
"v_permlane32_swap_b32 v233, v237\n"
"v_permlane32_swap_b32 v234, v238\n"
"v_permlane32_swap_b32 v235, v239\n"
"v_max_f32 v232, v232, v236\n"
"v_max_f32 v233, v233, v237\n"
"v_max_f32 v234, v234, v238\n"
"v_max_f32 v235, v235, v239\n"
"v_sub_f32 v224, 0, v232\n"
"v_sub_f32 v225, 0, v233\n"
"v_sub_f32 v226, 0, v234\n"
"v_sub_f32 v227, 0, v235\n"
"s_barrier\n"
"s_add_u32 s72, %11, 0\n"
"s_add_u32 m0, %11, 0\n"
"s_nop 0\n"
"buffer_load_dwordx4 v204, %2, s72 offen offset:0 lds\n"
"buffer_load_dwordx4 v205, %2, s72 offen offset:1024 lds\n"
"buffer_load_dwordx4 v206, %2, s72 offen offset:2048 lds\n"
"buffer_load_dwordx4 v207, %2, s72 offen offset:3072 lds\n"
"s_add_u32 s72, %11, 16384\n"
"s_add_u32 m0, %11, 16384\n"
"s_nop 0\n"
"buffer_load_dwordx4 v204, %2, s72 offen offset:0 lds\n"
"buffer_load_dwordx4 v205, %2, s72 offen offset:1024 lds\n"
"buffer_load_dwordx4 v206, %2, s72 offen offset:2048 lds\n"
"buffer_load_dwordx4 v207, %2, s72 offen offset:3072 lds\n"
"s_add_u32 s72, %11, 32768\n"
"s_add_u32 m0, %11, 32768\n"
"s_nop 0\n"
"buffer_load_dwordx4 v204, %2, s72 offen offset:0 lds\n"
"buffer_load_dwordx4 v205, %2, s72 offen offset:1024 lds\n"
"buffer_load_dwordx4 v206, %2, s72 offen offset:2048 lds\n"
"buffer_load_dwordx4 v207, %2, s72 offen offset:3072 lds\n"
"s_add_u32 s72, %11, 49152\n"
"s_add_u32 m0, %11, 49152\n"
"s_nop 0\n"
"buffer_load_dwordx4 v204, %2, s72 offen offset:0 lds\n"
"buffer_load_dwordx4 v205, %2, s72 offen offset:1024 lds\n"
"buffer_load_dwordx4 v206, %2, s72 offen offset:2048 lds\n"
"buffer_load_dwordx4 v207, %2, s72 offen offset:3072 lds\n"
"s_add_u32 s72, %11, 32768\n"
"s_add_u32 m0, %11, 65536\n"
"s_nop 0\n"
"buffer_load_dwordx4 v208, %3, s72 offen offset:0 lds\n"
"buffer_load_dwordx4 v209, %3, s72 offen offset:1024 lds\n"
"buffer_load_dwordx4 v208, %3, s72 offen offset:2048 lds\n"
"buffer_load_dwordx4 v209, %3, s72 offen offset:3072 lds\n"
"s_add_u32 s72, %11, 49152\n"
"s_add_u32 m0, %11, 81920\n"
"s_nop 0\n"
"buffer_load_dwordx4 v208, %3, s72 offen offset:0 lds\n"
"buffer_load_dwordx4 v209, %3, s72 offen offset:1024 lds\n"
"buffer_load_dwordx4 v208, %3, s72 offen offset:2048 lds\n"
"buffer_load_dwordx4 v209, %3, s72 offen offset:3072 lds\n"
"s_add_u32 s72, %11, 65536\n"
"s_add_u32 m0, %11, 98304\n"
"s_nop 0\n"
"buffer_load_dwordx4 v208, %3, s72 offen offset:0 lds\n"
"buffer_load_dwordx4 v209, %3, s72 offen offset:1024 lds\n"
"buffer_load_dwordx4 v208, %3, s72 offen offset:2048 lds\n"
"buffer_load_dwordx4 v209, %3, s72 offen offset:3072 lds\n"
"v_accvgpr_write_b32 a0, 0\n"
"v_accvgpr_write_b32 a1, 0\n"
"v_accvgpr_write_b32 a2, 0\n"
"v_accvgpr_write_b32 a3, 0\n"
"v_accvgpr_write_b32 a4, 0\n"
"v_accvgpr_write_b32 a5, 0\n"
"v_accvgpr_write_b32 a6, 0\n"
"v_accvgpr_write_b32 a7, 0\n"
"v_accvgpr_write_b32 a8, 0\n"
"v_accvgpr_write_b32 a9, 0\n"
"v_accvgpr_write_b32 a10, 0\n"
"v_accvgpr_write_b32 a11, 0\n"
"v_accvgpr_write_b32 a12, 0\n"
"v_accvgpr_write_b32 a13, 0\n"
"v_accvgpr_write_b32 a14, 0\n"
"v_accvgpr_write_b32 a15, 0\n"
"v_accvgpr_write_b32 a16, 0\n"
"v_accvgpr_write_b32 a17, 0\n"
"v_accvgpr_write_b32 a18, 0\n"
"v_accvgpr_write_b32 a19, 0\n"
"v_accvgpr_write_b32 a20, 0\n"
"v_accvgpr_write_b32 a21, 0\n"
"v_accvgpr_write_b32 a22, 0\n"
"v_accvgpr_write_b32 a23, 0\n"
"v_accvgpr_write_b32 a24, 0\n"
"v_accvgpr_write_b32 a25, 0\n"
"v_accvgpr_write_b32 a26, 0\n"
"v_accvgpr_write_b32 a27, 0\n"
"v_accvgpr_write_b32 a28, 0\n"
"v_accvgpr_write_b32 a29, 0\n"
"v_accvgpr_write_b32 a30, 0\n"
"v_accvgpr_write_b32 a31, 0\n"
"v_accvgpr_write_b32 a32, 0\n"
"v_accvgpr_write_b32 a33, 0\n"
"v_accvgpr_write_b32 a34, 0\n"
"v_accvgpr_write_b32 a35, 0\n"
"v_accvgpr_write_b32 a36, 0\n"
"v_accvgpr_write_b32 a37, 0\n"
"v_accvgpr_write_b32 a38, 0\n"
"v_accvgpr_write_b32 a39, 0\n"
"v_accvgpr_write_b32 a40, 0\n"
"v_accvgpr_write_b32 a41, 0\n"
"v_accvgpr_write_b32 a42, 0\n"
"v_accvgpr_write_b32 a43, 0\n"
"v_accvgpr_write_b32 a44, 0\n"
"v_accvgpr_write_b32 a45, 0\n"
"v_accvgpr_write_b32 a46, 0\n"
"v_accvgpr_write_b32 a47, 0\n"
"v_accvgpr_write_b32 a48, 0\n"
"v_accvgpr_write_b32 a49, 0\n"
"v_accvgpr_write_b32 a50, 0\n"
"v_accvgpr_write_b32 a51, 0\n"
"v_accvgpr_write_b32 a52, 0\n"
"v_accvgpr_write_b32 a53, 0\n"
"v_accvgpr_write_b32 a54, 0\n"
"v_accvgpr_write_b32 a55, 0\n"
"v_accvgpr_write_b32 a56, 0\n"
"v_accvgpr_write_b32 a57, 0\n"
"v_accvgpr_write_b32 a58, 0\n"
"v_accvgpr_write_b32 a59, 0\n"
"v_accvgpr_write_b32 a60, 0\n"
"v_accvgpr_write_b32 a61, 0\n"
"v_accvgpr_write_b32 a62, 0\n"
"v_accvgpr_write_b32 a63, 0\n"
"v_accvgpr_write_b32 a64, 0\n"
"v_accvgpr_write_b32 a65, 0\n"
"v_accvgpr_write_b32 a66, 0\n"
"v_accvgpr_write_b32 a67, 0\n"
"v_accvgpr_write_b32 a68, 0\n"
"v_accvgpr_write_b32 a69, 0\n"
"v_accvgpr_write_b32 a70, 0\n"
"v_accvgpr_write_b32 a71, 0\n"
"v_accvgpr_write_b32 a72, 0\n"
"v_accvgpr_write_b32 a73, 0\n"
"v_accvgpr_write_b32 a74, 0\n"
"v_accvgpr_write_b32 a75, 0\n"
"v_accvgpr_write_b32 a76, 0\n"
"v_accvgpr_write_b32 a77, 0\n"
"v_accvgpr_write_b32 a78, 0\n"
"v_accvgpr_write_b32 a79, 0\n"
"v_accvgpr_write_b32 a80, 0\n"
"v_accvgpr_write_b32 a81, 0\n"
"v_accvgpr_write_b32 a82, 0\n"
"v_accvgpr_write_b32 a83, 0\n"
"v_accvgpr_write_b32 a84, 0\n"
"v_accvgpr_write_b32 a85, 0\n"
"v_accvgpr_write_b32 a86, 0\n"
"v_accvgpr_write_b32 a87, 0\n"
"v_accvgpr_write_b32 a88, 0\n"
"v_accvgpr_write_b32 a89, 0\n"
"v_accvgpr_write_b32 a90, 0\n"
"v_accvgpr_write_b32 a91, 0\n"
"v_accvgpr_write_b32 a92, 0\n"
"v_accvgpr_write_b32 a93, 0\n"
"v_accvgpr_write_b32 a94, 0\n"
"v_accvgpr_write_b32 a95, 0\n"
"v_accvgpr_write_b32 a96, 0\n"
"v_accvgpr_write_b32 a97, 0\n"
"v_accvgpr_write_b32 a98, 0\n"
"v_accvgpr_write_b32 a99, 0\n"
"v_accvgpr_write_b32 a100, 0\n"
"v_accvgpr_write_b32 a101, 0\n"
"v_accvgpr_write_b32 a102, 0\n"
"v_accvgpr_write_b32 a103, 0\n"
"v_accvgpr_write_b32 a104, 0\n"
"v_accvgpr_write_b32 a105, 0\n"
"v_accvgpr_write_b32 a106, 0\n"
"v_accvgpr_write_b32 a107, 0\n"
"v_accvgpr_write_b32 a108, 0\n"
"v_accvgpr_write_b32 a109, 0\n"
"v_accvgpr_write_b32 a110, 0\n"
"v_accvgpr_write_b32 a111, 0\n"
"v_accvgpr_write_b32 a112, 0\n"
"v_accvgpr_write_b32 a113, 0\n"
"v_accvgpr_write_b32 a114, 0\n"
"v_accvgpr_write_b32 a115, 0\n"
"v_accvgpr_write_b32 a116, 0\n"
"v_accvgpr_write_b32 a117, 0\n"
"v_accvgpr_write_b32 a118, 0\n"
"v_accvgpr_write_b32 a119, 0\n"
"v_accvgpr_write_b32 a120, 0\n"
"v_accvgpr_write_b32 a121, 0\n"
"v_accvgpr_write_b32 a122, 0\n"
"v_accvgpr_write_b32 a123, 0\n"
"v_accvgpr_write_b32 a124, 0\n"
"v_accvgpr_write_b32 a125, 0\n"
"v_accvgpr_write_b32 a126, 0\n"
"v_accvgpr_write_b32 a127, 0\n"
"v_accvgpr_write_b32 a128, 0\n"
"v_accvgpr_write_b32 a129, 0\n"
"v_accvgpr_write_b32 a130, 0\n"
"v_accvgpr_write_b32 a131, 0\n"
"v_accvgpr_write_b32 a132, 0\n"
"v_accvgpr_write_b32 a133, 0\n"
"v_accvgpr_write_b32 a134, 0\n"
"v_accvgpr_write_b32 a135, 0\n"
"v_accvgpr_write_b32 a136, 0\n"
"v_accvgpr_write_b32 a137, 0\n"
"v_accvgpr_write_b32 a138, 0\n"
"v_accvgpr_write_b32 a139, 0\n"
"v_accvgpr_write_b32 a140, 0\n"
"v_accvgpr_write_b32 a141, 0\n"
"v_accvgpr_write_b32 a142, 0\n"
"v_accvgpr_write_b32 a143, 0\n"
"s_waitcnt vmcnt(0)\n"
"s_barrier\n"
"ds_read_b128 a[192:195], v192 offset:0\n"
"ds_read_b128 a[196:199], v192 offset:4096\n"
"ds_read_b128 a[200:203], v193 offset:0\n"
"ds_read_b128 a[204:207], v193 offset:4096\n"
"ds_read_b128 a[208:211], v194 offset:0\n"
"ds_read_b128 a[212:215], v194 offset:4096\n"
"ds_read_b128 a[216:219], v195 offset:0\n"
"ds_read_b128 a[220:223], v195 offset:4096\n"
"ds_read_b128 a[224:227], v192 offset:8192\n"
"ds_read_b128 a[228:231], v192 offset:12288\n"
"ds_read_b128 a[232:235], v193 offset:8192\n"
"ds_read_b128 a[236:239], v193 offset:12288\n"
"ds_read_b128 a[240:243], v194 offset:8192\n"
"ds_read_b128 a[244:247], v194 offset:12288\n"
"ds_read_b128 a[248:251], v195 offset:8192\n"
"ds_read_b128 a[252:255], v195 offset:12288\n"
"s_waitcnt lgkmcnt(0)\n"
"s_barrier\n"
"s_add_u32 s72, %11, 65536\n"
"s_add_u32 m0, %11, 0\n"
"s_nop 0\n"
"buffer_load_dwordx4 v204, %2, s72 offen offset:0 lds\n"
"buffer_load_dwordx4 v205, %2, s72 offen offset:1024 lds\n"
"buffer_load_dwordx4 v206, %2, s72 offen offset:2048 lds\n"
"buffer_load_dwordx4 v207, %2, s72 offen offset:3072 lds\n"
"v_mfma_f32_16x16x32_bf16 v[0:3], a[192:195], v[128:131], 0\n"
"v_mfma_f32_16x16x32_bf16 v[0:3], a[200:203], v[132:135], v[0:3]\n"
"v_mfma_f32_16x16x32_bf16 v[0:3], a[208:211], v[136:139], v[0:3]\n"
"v_mfma_f32_16x16x32_bf16 v[0:3], a[216:219], v[140:143], v[0:3]\n"
"v_mfma_f32_16x16x32_bf16 v[8:11], a[216:219], v[156:159], 0\n"
"v_mfma_f32_16x16x32_bf16 v[8:11], a[208:211], v[152:155], v[8:11]\n"
"v_mfma_f32_16x16x32_bf16 v[8:11], a[200:203], v[148:151], v[8:11]\n"
"v_mfma_f32_16x16x32_bf16 v[8:11], a[192:195], v[144:147], v[8:11]\n"
"v_mfma_f32_16x16x32_bf16 v[16:19], a[192:195], v[160:163], 0\n"
"v_mfma_f32_16x16x32_bf16 v[16:19], a[200:203], v[164:167], v[16:19]\n"
"v_mfma_f32_16x16x32_bf16 v[16:19], a[208:211], v[168:171], v[16:19]\n"
"v_mfma_f32_16x16x32_bf16 v[16:19], a[216:219], v[172:175], v[16:19]\n"
"v_mfma_f32_16x16x32_bf16 v[24:27], a[216:219], v[188:191], 0\n"
"v_mfma_f32_16x16x32_bf16 v[24:27], a[208:211], v[184:187], v[24:27]\n"
"v_mfma_f32_16x16x32_bf16 v[24:27], a[200:203], v[180:183], v[24:27]\n"
"v_mfma_f32_16x16x32_bf16 v[24:27], a[192:195], v[176:179], v[24:27]\n"
"v_mfma_f32_16x16x32_bf16 v[28:31], a[196:199], v[176:179], 0\n"
"v_mfma_f32_16x16x32_bf16 v[28:31], a[204:207], v[180:183], v[28:31]\n"
"v_mfma_f32_16x16x32_bf16 v[28:31], a[212:215], v[184:187], v[28:31]\n"
"v_mfma_f32_16x16x32_bf16 v[28:31], a[220:223], v[188:191], v[28:31]\n"
"v_mfma_f32_16x16x32_bf16 v[20:23], a[220:223], v[172:175], 0\n"
"v_mfma_f32_16x16x32_bf16 v[20:23], a[212:215], v[168:171], v[20:23]\n"
"v_mfma_f32_16x16x32_bf16 v[20:23], a[204:207], v[164:167], v[20:23]\n"
"v_mfma_f32_16x16x32_bf16 v[20:23], a[196:199], v[160:163], v[20:23]\n"
"v_mfma_f32_16x16x32_bf16 v[12:15], a[196:199], v[144:147], 0\n"
"v_mfma_f32_16x16x32_bf16 v[12:15], a[204:207], v[148:151], v[12:15]\n"
"v_mfma_f32_16x16x32_bf16 v[12:15], a[212:215], v[152:155], v[12:15]\n"
"v_mfma_f32_16x16x32_bf16 v[12:15], a[220:223], v[156:159], v[12:15]\n"
"v_mfma_f32_16x16x32_bf16 v[4:7], a[220:223], v[140:143], 0\n"
"v_mfma_f32_16x16x32_bf16 v[4:7], a[212:215], v[136:139], v[4:7]\n"
"v_mfma_f32_16x16x32_bf16 v[4:7], a[204:207], v[132:135], v[4:7]\n"
"v_mfma_f32_16x16x32_bf16 v[4:7], a[196:199], v[128:131], v[4:7]\n"
"v_mfma_f32_16x16x32_bf16 v[32:35], a[224:227], v[128:131], 0\n"
"v_mfma_f32_16x16x32_bf16 v[32:35], a[232:235], v[132:135], v[32:35]\n"
"v_mfma_f32_16x16x32_bf16 v[32:35], a[240:243], v[136:139], v[32:35]\n"
"v_mfma_f32_16x16x32_bf16 v[32:35], a[248:251], v[140:143], v[32:35]\n"
"v_mfma_f32_16x16x32_bf16 v[40:43], a[248:251], v[156:159], 0\n"
"v_mfma_f32_16x16x32_bf16 v[40:43], a[240:243], v[152:155], v[40:43]\n"
"v_mfma_f32_16x16x32_bf16 v[40:43], a[232:235], v[148:151], v[40:43]\n"
"v_mfma_f32_16x16x32_bf16 v[40:43], a[224:227], v[144:147], v[40:43]\n"
"v_mfma_f32_16x16x32_bf16 v[48:51], a[224:227], v[160:163], 0\n"
"v_mfma_f32_16x16x32_bf16 v[48:51], a[232:235], v[164:167], v[48:51]\n"
"v_mfma_f32_16x16x32_bf16 v[48:51], a[240:243], v[168:171], v[48:51]\n"
"v_mfma_f32_16x16x32_bf16 v[48:51], a[248:251], v[172:175], v[48:51]\n"
"v_mfma_f32_16x16x32_bf16 v[56:59], a[248:251], v[188:191], 0\n"
"v_mfma_f32_16x16x32_bf16 v[56:59], a[240:243], v[184:187], v[56:59]\n"
"v_mfma_f32_16x16x32_bf16 v[56:59], a[232:235], v[180:183], v[56:59]\n"
"v_mfma_f32_16x16x32_bf16 v[56:59], a[224:227], v[176:179], v[56:59]\n"
"v_mfma_f32_16x16x32_bf16 v[60:63], a[228:231], v[176:179], 0\n"
"v_mfma_f32_16x16x32_bf16 v[60:63], a[236:239], v[180:183], v[60:63]\n"
"v_mfma_f32_16x16x32_bf16 v[60:63], a[244:247], v[184:187], v[60:63]\n"
"v_mfma_f32_16x16x32_bf16 v[60:63], a[252:255], v[188:191], v[60:63]\n"
"v_mfma_f32_16x16x32_bf16 v[52:55], a[252:255], v[172:175], 0\n"
"v_mfma_f32_16x16x32_bf16 v[52:55], a[244:247], v[168:171], v[52:55]\n"
"v_mfma_f32_16x16x32_bf16 v[52:55], a[236:239], v[164:167], v[52:55]\n"
"v_mfma_f32_16x16x32_bf16 v[52:55], a[228:231], v[160:163], v[52:55]\n"
"v_mfma_f32_16x16x32_bf16 v[44:47], a[228:231], v[144:147], 0\n"
"v_mfma_f32_16x16x32_bf16 v[44:47], a[236:239], v[148:151], v[44:47]\n"
"v_mfma_f32_16x16x32_bf16 v[44:47], a[244:247], v[152:155], v[44:47]\n"
"v_mfma_f32_16x16x32_bf16 v[44:47], a[252:255], v[156:159], v[44:47]\n"
"v_mfma_f32_16x16x32_bf16 v[36:39], a[252:255], v[140:143], 0\n"
"v_mfma_f32_16x16x32_bf16 v[36:39], a[244:247], v[136:139], v[36:39]\n"
"v_mfma_f32_16x16x32_bf16 v[36:39], a[236:239], v[132:135], v[36:39]\n"
"v_mfma_f32_16x16x32_bf16 v[36:39], a[228:231], v[128:131], v[36:39]\n"
"ds_read_b128 a[192:195], v192 offset:16384\n"
"ds_read_b128 a[196:199], v192 offset:20480\n"
"ds_read_b128 a[200:203], v193 offset:16384\n"
"ds_read_b128 a[204:207], v193 offset:20480\n"
"ds_read_b128 a[208:211], v194 offset:16384\n"
"ds_read_b128 a[212:215], v194 offset:20480\n"
"ds_read_b128 a[216:219], v195 offset:16384\n"
"ds_read_b128 a[220:223], v195 offset:20480\n"
"s_nop 15\n"
"s_branch HAVE_NEGM%=\n"
"LOS%=:\n"
"s_mov_b32 s71, 0x40000\n"
"v_bfe_u32 v233, %19, 2, 5\n"
"LOP%=:\n"
"global_load_dword v232, v233, %10 offset:32 sc1\n"
"s_waitcnt vmcnt(0)\n"
"v_cmp_eq_u32 vcc, 0, v232\n"
"s_sub_u32 s71, s71, 1\n"
"s_cbranch_scc1 LOPX%=\n"
"s_cmp_eq_u64 vcc, 0\n"
"s_cbranch_scc1 LOPX%=\n"
"s_sleep 2\n"
"s_branch LOP%=\n"
"LOPX%=:\n"
"s_branch LOB%=\n"
"DONE%=:\n"
"s_nop 1\n"
    :
    : "s"(srdH), "s"(srdW), "s"(srdK), "s"(srdV), "s"(srdO), "s"(srdOX), "s"(srdWO), "s"(srdP), "s"(srdW1), "s"(cH), "s"(cO), "s"(w4k), "s"(jj), "v"(vK0), "v"(vV0), "v"(vW0), "v"(vKD), "v"(vVD), "v"(vWD), "v"(vL16), "v"(vPA), "v"(vPS)
    : "v0", "v1", "v2", "v3", "v4", "v5", "v6", "v7", "v8", "v9", "v10", "v11", "v12", "v13", "v14", "v15", "v16", "v17", "v18", "v19", "v20", "v21", "v22", "v23", "v24", "v25", "v26", "v27", "v28", "v29", "v30", "v31", "v32", "v33", "v34", "v35", "v36", "v37", "v38", "v39", "v40", "v41", "v42", "v43", "v44", "v45", "v46", "v47", "v48", "v49", "v50", "v51", "v52", "v53", "v54", "v55", "v56", "v57", "v58", "v59", "v60", "v61", "v62", "v63", "v64", "v65", "v66", "v67", "v68", "v69", "v70", "v71", "v72", "v73", "v74", "v75", "v76", "v77", "v78", "v79", "v80", "v81", "v82", "v83", "v84", "v85", "v86", "v87", "v88", "v89", "v90", "v91", "v92", "v93", "v94", "v95", "v96", "v97", "v98", "v99", "v100", "v101", "v102", "v103", "v104", "v105", "v106", "v107", "v108", "v109", "v110", "v111", "v112", "v113", "v114", "v115", "v116", "v117", "v118", "v119", "v120", "v121", "v122", "v123", "v124", "v125", "v126", "v127", "v128", "v129", "v130", "v131", "v132", "v133", "v134", "v135", "v136", "v137", "v138", "v139", "v140", "v141", "v142", "v143", "v144", "v145", "v146", "v147", "v148", "v149", "v150", "v151", "v152", "v153", "v154", "v155", "v156", "v157", "v158", "v159", "v160", "v161", "v162", "v163", "v164", "v165", "v166", "v167", "v168", "v169", "v170", "v171", "v172", "v173", "v174", "v175", "v176", "v177", "v178", "v179", "v180", "v181", "v182", "v183", "v184", "v185", "v186", "v187", "v188", "v189", "v190", "v191", "v192", "v193", "v194", "v195", "v196", "v197", "v198", "v199", "v200", "v201", "v202", "v203", "v204", "v205", "v206", "v207", "v208", "v209", "v210", "v211", "v212", "v213", "v214", "v215", "v216", "v217", "v218", "v219", "v220", "v221", "v222", "v223", "v224", "v225", "v226", "v227", "v228", "v229", "v230", "v231", "v232", "v233", "v234", "v235", "v236", "v237", "v238", "v239", "a0", "a1", "a2", "a3", "a4", "a5", "a6", "a7", "a8", "a9", "a10", "a11", "a12", "a13", "a14", "a15", "a16", "a17", "a18", "a19", "a20", "a21", "a22", "a23", "a24", "a25", "a26", "a27", "a28", "a29", "a30", "a31", "a32", "a33", "a34", "a35", "a36", "a37", "a38", "a39", "a40", "a41", "a42", "a43", "a44", "a45", "a46", "a47", "a48", "a49", "a50", "a51", "a52", "a53", "a54", "a55", "a56", "a57", "a58", "a59", "a60", "a61", "a62", "a63", "a64", "a65", "a66", "a67", "a68", "a69", "a70", "a71", "a72", "a73", "a74", "a75", "a76", "a77", "a78", "a79", "a80", "a81", "a82", "a83", "a84", "a85", "a86", "a87", "a88", "a89", "a90", "a91", "a92", "a93", "a94", "a95", "a96", "a97", "a98", "a99", "a100", "a101", "a102", "a103", "a104", "a105", "a106", "a107", "a108", "a109", "a110", "a111", "a112", "a113", "a114", "a115", "a116", "a117", "a118", "a119", "a120", "a121", "a122", "a123", "a124", "a125", "a126", "a127", "a128", "a129", "a130", "a131", "a132", "a133", "a134", "a135", "a136", "a137", "a138", "a139", "a140", "a141", "a142", "a143", "a144", "a145", "a146", "a147", "a148", "a149", "a150", "a151", "a152", "a153", "a154", "a155", "a156", "a157", "a158", "a159", "a160", "a161", "a162", "a163", "a164", "a165", "a166", "a167", "a168", "a169", "a170", "a171", "a172", "a173", "a174", "a175", "a176", "a177", "a178", "a179", "a180", "a181", "a182", "a183", "a184", "a185", "a186", "a187", "a188", "a189", "a190", "a191", "a192", "a193", "a194", "a195", "a196", "a197", "a198", "a199", "a200", "a201", "a202", "a203", "a204", "a205", "a206", "a207", "a208", "a209", "a210", "a211", "a212", "a213", "a214", "a215", "a216", "a217", "a218", "a219", "a220", "a221", "a222", "a223", "a224", "a225", "a226", "a227", "a228", "a229", "a230", "a231", "a232", "a233", "a234", "a235", "a236", "a237", "a238", "a239", "a240", "a241", "a242", "a243", "a244", "a245", "a246", "a247", "a248", "a249", "a250", "a251", "a252", "a253", "a254", "a255", "s64", "s65", "s66", "s67", "s68", "s69", "s70", "s71", "s72", "s73", "s74", "s75", "s76", "s77", "vcc", "scc", "memory");
  }
  unsigned t = threadIdx.x;
  asm volatile("" : "+v"(t));
  const unsigned lane = t & 63u, gq = lane >> 4, q = (lane >> 2) & 3u, p = lane & 3u, rr = 4u * gq + q;
  const unsigned row = row0 + (t & 7u), c0 = t >> 3;
  const unsigned poff = ((row >> 4) & 1u) * 1024u + (row & 15u) * 16u + (c0 >> 2) * 2048u + (c0 & 3u) * 256u;
  f32x16 fa[2];
  u32x16 xq;
  unsigned cv0;
  const unsigned cgp = jj * 4u + sk;
  const unsigned fo = sk * 32u + (lane & 31u);
  {
    const unsigned odd = head & 1u;
    unsigned vD0, vD1, vD2, vD3;
    {
      const __amdgpu_buffer_rsrc_t srdP = SRD(ws + WS_P1 + (size_t)(row0 >> 5) * 65536u, (12u << 20) + 65536u);
      const __amdgpu_buffer_rsrc_t srdYw = SRD(ws + WS_Y + (size_t)(row0 >> 5) * 65536u, 65536);
      unsigned cv = __hip_atomic_load((gu32*)(cq + 256u + fo), RLX_AGENT);
      {
        const __amdgpu_buffer_rsrc_t srdM2 = SRD(ws + WS_M2 + qblk * 8192u, 8192);
        const u32x4 m0 = ld_sc1(srdM2, t * 16u, 0), m1 = ld_sc1(srdM2, t * 16u, 4096);
        *(u32x4*)(lds + L_PAR + 4096u + t * 16u) = m0; *(u32x4*)(lds + L_PAR + 8192u + t * 16u) = m1;
      }
      asm volatile("s_waitcnt lgkmcnt(0)" ::: "memory");
      PUBLISH(cq + 256u + head);
      u32x4 pp[4][4] = {};
      unsigned todo = 15u;
      {
        const unsigned nz = (unsigned)__builtin_amdgcn_ballot_w64(cv != 0u);
        asm volatile("" ::: "memory");
#pragma unroll
        for (int k = 0; k < 4; ++k)
          if ((todo & (1u << k)) && ((nz >> (8 * k)) & 255u) == 255u) {
#pragma unroll
            for (int i = 0; i < 4; ++i) pp[i][k] = ld_sc1(srdP, poff + i * 16384u, k << 22);
            todo &= ~(1u << k);
          }
        if (todo) { __builtin_amdgcn_s_sleep(SLP); cv = __hip_atomic_load((gu32*)(cq + 256u + fo), RLX_AGENT); }
      }
      asm volatile("" ::: "memory");
      f32x2 v[16], ga[16], sb[16], cb[16];
#pragma unroll
      for (int i = 0; i < 4; ++i) {
        const u32x4 xv = *(const u32x4*)(lds + L_X + (i * 256u + t) * 16u), g4 = *(const u32x4*)(lds + L_PAR + 4096u + (c0 + 32u * i) * 16u);
        const u32x4 s4 = *(const u32x4*)(lds + L_PAR + 6144u + (c0 + 32u * i) * 16u), c4 = *(const u32x4*)(lds + L_PAR + 8192u + (c0 + 32u * i) * 16u);
#pragma unroll
        for (int e = 0; e < 4; ++e) { v[4 * i + e] = up2(xv[e]); ga[4 * i + e] = up2(g4[e]); sb[4 * i + e] = up2(s4[e]); cb[4 * i + e] = up2(c4[e]) + 1.f; }
      }
      { const u32x4 pd = *(const u32x4*)(lds + L_PAR + t * 16u); vD0 = pd[0]; vD1 = pd[1]; vD2 = pd[2]; vD3 = pd[3]; }
#pragma unroll
      for (int i = 0; i < 16; ++i) { PIN(v[i]); PIN(ga[i]); PIN(sb[i]); PIN(cb[i]); }
      PIN(vD0); PIN(vD1); PIN(vD2); PIN(vD3);
#pragma nounroll
      for (unsigned spins = 0; todo != 0u && spins < (1u << 18); ++spins)
      {
        const unsigned nz = (unsigned)__builtin_amdgcn_ballot_w64(cv != 0u);
        asm volatile("" ::: "memory");
#pragma unroll
        for (int k = 0; k < 4; ++k)
          if ((todo & (1u << k)) && ((nz >> (8 * k)) & 255u) == 255u) {
#pragma unroll
            for (int i = 0; i < 4; ++i) pp[i][k] = ld_sc1(srdP, poff + i * 16384u, k << 22);
            todo &= ~(1u << k);
          }
        if (todo) { __builtin_amdgcn_s_sleep(SLP); cv = __hip_atomic_load((gu32*)(cq + 256u + fo), RLX_AGENT); }
      }
#pragma unroll
      for (int i = 0; i < 16; ++i) v[i] = fma2(ga[i], add2(add2(add2(up2(pp[i >> 2][0][i & 3]), pp[i >> 2][1][i & 3]), pp[i >> 2][2][i & 3]), pp[i >> 2][3][i & 3]), v[i]);
      u32x4 x1p[4];
#pragma unroll
      for (int i = 0; i < 16; ++i) x1p[i >> 2][i & 3] = pk2(v[i]);
      {
        const __amdgpu_buffer_rsrc_t srdX1 = SRD(ws + WS_X1 + (size_t)(row0 >> 5) * 65536u, 65536);
#pragma unroll
        for (int i = 0; i < 4; ++i) st_sc1(x1p[i], srdX1, poff + i * 16384u);
      }
      float mean, rstd;
      row_stats(v, (f32x2*)(lds + L_ST), t, mean, rstd);
      {
        const float rb = -mean * rstd;
#pragma unroll
        for (int i = 0; i < 16; ++i) v[i] = fma2(fma2(v[i], bc2(rstd), bc2(rb)), cb[i], sb[i]);
      }
      row_stats(v, (f32x2*)(lds + L_ST + 256), t, mean, rstd);
      const float rb = -mean * rstd;
#pragma unroll
      for (int i = 0; i < 4; ++i) {
        u32x4 o;
#pragma unroll
        for (int e = 0; e < 4; ++e) o[e] = pk2(fma2(v[4 * i + e], bc2(rstd), bc2(rb)));
        st_sc1(o, srdYw, poff + i * 16384u);
      }
      PUBLISH(cq + 384u + head);
    }
    const unsigned kl = (7u * jj + 1u) >> 1, k0 = 28u * sk + kl, k6 = (odd ? k0 - 1u : k0 + 3u) * 2048u + odd * 512u;
    const __amdgpu_buffer_rsrc_t srdY = SRD(ws + WS_Y + (size_t)mb0 * 65536u, 131072);
    const __amdgpu_buffer_rsrc_t srdB = SRD(b1 + head * 112u, 224);
    const __amdgpu_buffer_rsrc_t srdG = SRD(ws + WS_H1 + (size_t)mb0 * 262144u, 2u * 262144u);
    const __amdgpu_buffer_rsrc_t srdGX = SRD(ws + WS_H1 + (size_t)mb0 * 262144u + sk * 57344u, 262144 + 57344);
    const __amdgpu_buffer_rsrc_t srdW2 = SRD(w2 + (size_t)sk * (896u * 2048u) + jj * 256u, 896u * 2048u - jj * 256u);
    const __amdgpu_buffer_rsrc_t srdP = SRD(ws + WS_P2 + (size_t)sk * (4u << 20) + (size_t)mb0 * 65536u + jj * 8192u, 65536 + 8192);
    const __amdgpu_buffer_rsrc_t srdX1 = SRD(ws + WS_X1 + (size_t)mb0 * 65536u + cgp * 2048u, 65536u + 2048u);
    unsigned* cY = cq + 384u + sk * 32u + w * 8u;
    unsigned* cG = cq + 512u + sk * 32u;
    unsigned* cF = cq + 640u + sk * 32u + jj;
    const unsigned vRE = rr * 256u + 32u * (rr & 7u) + 8u * p, vRF = (65536u + rr * 256u + 32u * (rr & 7u) + 8u * p) ^ ((sk ^ 3u) << 6), px = (sk ^ 3u) << 11, vL16 = lane * 16u;
    const unsigned vS6 = (gq & 1u) * 1024u + (gq >> 1) * 256u + (lane & 15u) * 16u;
    unsigned vSQ;
    {
      const unsigned e1 = (lane - 3u) >> 1, o1 = e1 + (e1 >= jj), e2 = lane - 17u, o2 = e2 + (e2 >= jj);
      vSQ = lane < 3u ? kl + lane : lane < 17u ? ((7u * o1 + 1u) >> 1) + ((lane - 3u) & 1u) : lane < 24u ? ((7u * o2 + 1u) >> 1) + 2u : 7u * ((lane - 24u) & 3u) + 3u;
    }
    const unsigned vFD = 2048u * gq + 16u * ((lane & 15u) ^ (gq << 1)), vB0 = odd * 32u + 8u * gq, vB6 = (odd ? 0u : 192u) + 8u * gq;
  asm volatile(
"s_waitcnt lgkmcnt(0)\n"
"buffer_load_dwordx2 v[128:129], %30, %6, 0 offen offset:0\n"
"buffer_load_dwordx2 v[130:131], %30, %6, 0 offen offset:32\n"
"buffer_load_dwordx2 v[132:133], %30, %6, 0 offen offset:64\n"
"buffer_load_dwordx2 v[134:135], %30, %6, 0 offen offset:96\n"
"buffer_load_dwordx2 v[136:137], %30, %6, 0 offen offset:128\n"
"buffer_load_dwordx2 v[138:139], %30, %6, 0 offen offset:160\n"
"buffer_load_dwordx2 v[140:141], %31, %6, 0 offen\n"
"s_mov_b32 s64, 1376256\n"
"s_mov_b32 s65, 0\n"
"s_mov_b32 s72, 4096\n"
"v_mov_b32 v209, %25\n"
"v_mov_b32 v210, %26\n"
"v_mov_b32 v211, %27\n"
"v_mov_b32 v212, %28\n"
"v_xor_b32 v184, 0, %22\n"
"v_xor_b32 v185, 32, %22\n"
"v_xor_b32 v186, 64, %22\n"
"v_xor_b32 v187, 96, %22\n"
"v_xor_b32 v188, 128, %22\n"
"v_xor_b32 v189, 160, %22\n"
"v_xor_b32 v190, 192, %22\n"
"v_mov_b32 v207, %24\n"
"v_add_u32 v208, 65536, %24\n"
"v_xor_b32 v225, 128, %29\n"
"v_add_u32 v225, 7168, v225\n"
"v_xor_b32 v227, 128, %29\n"
"v_add_u32 v227, 21504, v227\n"
"v_add_u32 v226, 14336, %29\n"
"v_add_u32 v224, 0, %29\n"
"v_xor_b32 v214, 0, %23\n"
"v_xor_b32 v215, 32, %23\n"
"v_xor_b32 v216, 64, %23\n"
"v_xor_b32 v217, 96, %23\n"
"v_xor_b32 v218, 128, %23\n"
"v_xor_b32 v219, 160, %23\n"
"v_xor_b32 v220, 192, %23\n"
"v_xor_b32 v221, 224, %23\n"
"v_mov_b32 v222, %24\n"
"v_add_u32 v223, 262144, %24\n"
"v_mov_b32 v200, 0xbdd2d3e8\n"
"v_mov_b32 v201, 0xbdd2d3e8\n"
"v_mov_b32 v202, 0xc0135761\n"
"v_mov_b32 v203, 0xc0135761\n"
"v_mov_b32 v204, 0x3f800000\n"
"v_mov_b32 v205, 0x3f800000\n"
"s_waitcnt vmcnt(19)\n"
"s_barrier\n"
"ds_read_b64_tr_b16 a[224:225], v184 offset:0\n"
"ds_read_b64_tr_b16 a[226:227], v184 offset:4096\n"
"ds_read_b64_tr_b16 a[228:229], v184 offset:8192\n"
"ds_read_b64_tr_b16 a[230:231], v184 offset:12288\n"
"ds_read_b64_tr_b16 a[232:233], v185 offset:0\n"
"ds_read_b64_tr_b16 a[234:235], v185 offset:4096\n"
"ds_read_b64_tr_b16 a[236:237], v185 offset:8192\n"
"ds_read_b64_tr_b16 a[238:239], v185 offset:12288\n"
"s_mov_b32 s67, 0x40000\n"
"v_bfe_u32 v229, %24, 2, 5\n"
"PY%=:\n"
"global_load_dword v228, v229, %12 offset:0 sc1\n"
"s_waitcnt vmcnt(0)\n"
"v_cmp_eq_u32 vcc, 0, v228\n"
"s_sub_u32 s67, s67, 1\n"
"s_cbranch_scc1 PYX%=\n"
"s_cmp_eq_u64 vcc, 0\n"
"s_cbranch_scc1 PYX%=\n"
"s_sleep 2\n"
"s_branch PY%=\n"
"PYX%=:\n"
"buffer_load_dwordx4 a[0:3], v207, %4, s65 offen offset:0 sc1\n"
"buffer_load_dwordx4 a[4:7], v207, %4, s65 offen offset:1024 sc1\n"
"buffer_load_dwordx4 a[8:11], v208, %4, s65 offen offset:0 sc1\n"
"buffer_load_dwordx4 a[12:15], v208, %4, s65 offen offset:1024 sc1\n"
"buffer_load_dwordx4 a[16:19], v207, %4, s65 offen offset:2048 sc1\n"
"buffer_load_dwordx4 a[20:23], v207, %4, s65 offen offset:3072 sc1\n"
"buffer_load_dwordx4 a[24:27], v208, %4, s65 offen offset:2048 sc1\n"
"buffer_load_dwordx4 a[28:31], v208, %4, s65 offen offset:3072 sc1\n"
"s_add_u32 s65, s65, 4096\n"
"buffer_load_dwordx4 a[32:35], v207, %4, s65 offen offset:0 sc1\n"
"buffer_load_dwordx4 a[36:39], v207, %4, s65 offen offset:1024 sc1\n"
"buffer_load_dwordx4 a[40:43], v208, %4, s65 offen offset:0 sc1\n"
"buffer_load_dwordx4 a[44:47], v208, %4, s65 offen offset:1024 sc1\n"
"buffer_load_dwordx4 a[48:51], v207, %4, s65 offen offset:2048 sc1\n"
"buffer_load_dwordx4 a[52:55], v207, %4, s65 offen offset:3072 sc1\n"
"buffer_load_dwordx4 a[56:59], v208, %4, s65 offen offset:2048 sc1\n"
"buffer_load_dwordx4 a[60:63], v208, %4, s65 offen offset:3072 sc1\n"
"v_lshlrev_b32 v0, 16, v128\n"
"v_and_b32 v1, 0xffff0000, v128\n"
"v_lshlrev_b32 v2, 16, v129\n"
"v_and_b32 v3, 0xffff0000, v129\n"
"v_pk_mul_f32 v[8:9], v[0:1], v[204:205]\n"
"v_pk_mul_f32 v[10:11], v[2:3], v[204:205]\n"
"v_pk_mul_f32 v[16:17], v[0:1], v[204:205]\n"
"v_pk_mul_f32 v[18:19], v[2:3], v[204:205]\n"
"v_pk_mul_f32 v[24:25], v[0:1], v[204:205]\n"
"v_pk_mul_f32 v[26:27], v[2:3], v[204:205]\n"
"v_lshlrev_b32 v4, 16, v130\n"
"v_and_b32 v5, 0xffff0000, v130\n"
"v_lshlrev_b32 v6, 16, v131\n"
"v_and_b32 v7, 0xffff0000, v131\n"
"v_pk_mul_f32 v[12:13], v[4:5], v[204:205]\n"
"v_pk_mul_f32 v[14:15], v[6:7], v[204:205]\n"
"v_pk_mul_f32 v[20:21], v[4:5], v[204:205]\n"
"v_pk_mul_f32 v[22:23], v[6:7], v[204:205]\n"
"v_pk_mul_f32 v[28:29], v[4:5], v[204:205]\n"
"v_pk_mul_f32 v[30:31], v[6:7], v[204:205]\n"
"v_lshlrev_b32 v32, 16, v132\n"
"v_and_b32 v33, 0xffff0000, v132\n"
"v_lshlrev_b32 v34, 16, v133\n"
"v_and_b32 v35, 0xffff0000, v133\n"
"v_pk_mul_f32 v[40:41], v[32:33], v[204:205]\n"
"v_pk_mul_f32 v[42:43], v[34:35], v[204:205]\n"
"v_pk_mul_f32 v[48:49], v[32:33], v[204:205]\n"
"v_pk_mul_f32 v[50:51], v[34:35], v[204:205]\n"
"v_pk_mul_f32 v[56:57], v[32:33], v[204:205]\n"
"v_pk_mul_f32 v[58:59], v[34:35], v[204:205]\n"
"v_lshlrev_b32 v36, 16, v134\n"
"v_and_b32 v37, 0xffff0000, v134\n"
"v_lshlrev_b32 v38, 16, v135\n"
"v_and_b32 v39, 0xffff0000, v135\n"
"v_pk_mul_f32 v[44:45], v[36:37], v[204:205]\n"
"v_pk_mul_f32 v[46:47], v[38:39], v[204:205]\n"
"v_pk_mul_f32 v[52:53], v[36:37], v[204:205]\n"
"v_pk_mul_f32 v[54:55], v[38:39], v[204:205]\n"
"v_pk_mul_f32 v[60:61], v[36:37], v[204:205]\n"
"v_pk_mul_f32 v[62:63], v[38:39], v[204:205]\n"
"v_lshlrev_b32 v64, 16, v136\n"
"v_and_b32 v65, 0xffff0000, v136\n"
"v_lshlrev_b32 v66, 16, v137\n"
"v_and_b32 v67, 0xffff0000, v137\n"
"v_pk_mul_f32 v[72:73], v[64:65], v[204:205]\n"
"v_pk_mul_f32 v[74:75], v[66:67], v[204:205]\n"
"v_pk_mul_f32 v[80:81], v[64:65], v[204:205]\n"
"v_pk_mul_f32 v[82:83], v[66:67], v[204:205]\n"
"v_pk_mul_f32 v[88:89], v[64:65], v[204:205]\n"
"v_pk_mul_f32 v[90:91], v[66:67], v[204:205]\n"
"v_lshlrev_b32 v68, 16, v138\n"
"v_and_b32 v69, 0xffff0000, v138\n"
"v_lshlrev_b32 v70, 16, v139\n"
"v_and_b32 v71, 0xffff0000, v139\n"
"v_pk_mul_f32 v[76:77], v[68:69], v[204:205]\n"
"v_pk_mul_f32 v[78:79], v[70:71], v[204:205]\n"
"v_pk_mul_f32 v[84:85], v[68:69], v[204:205]\n"
"v_pk_mul_f32 v[86:87], v[70:71], v[204:205]\n"
"v_pk_mul_f32 v[92:93], v[68:69], v[204:205]\n"
"v_pk_mul_f32 v[94:95], v[70:71], v[204:205]\n"
"v_lshlrev_b32 v96, 16, v140\n"
"v_and_b32 v97, 0xffff0000, v140\n"
"v_lshlrev_b32 v98, 16, v141\n"
"v_and_b32 v99, 0xffff0000, v141\n"
"v_pk_mul_f32 v[100:101], v[96:97], v[204:205]\n"
"v_pk_mul_f32 v[102:103], v[98:99], v[204:205]\n"
"v_pk_mul_f32 v[104:105], v[96:97], v[204:205]\n"
"v_pk_mul_f32 v[106:107], v[98:99], v[204:205]\n"
"v_pk_mul_f32 v[108:109], v[96:97], v[204:205]\n"
"v_pk_mul_f32 v[110:111], v[98:99], v[204:205]\n"
"s_waitcnt vmcnt(8) lgkmcnt(4)\n"
"v_mfma_f32_16x16x32_bf16 v[0:3], a[224:227], a[0:3], v[0:3]\n"
"ds_read_b64_tr_b16 a[240:241], v186 offset:0\n"
"v_mfma_f32_16x16x32_bf16 v[0:3], a[228:231], a[16:19], v[0:3]\n"
"ds_read_b64_tr_b16 a[242:243], v186 offset:4096\n"
"v_mfma_f32_16x16x32_bf16 v[8:11], a[228:231], a[20:23], v[8:11]\n"
"s_add_u32 s65, s65, 4096\n"
"v_mfma_f32_16x16x32_bf16 v[8:11], a[224:227], a[4:7], v[8:11]\n"
"buffer_load_dwordx4 a[64:67], v207, %4, s65 offen offset:0 sc1\n"
"v_mfma_f32_16x16x32_bf16 v[16:19], a[224:227], a[8:11], v[16:19]\n"
"ds_read_b64_tr_b16 a[244:245], v186 offset:8192\n"
"v_mfma_f32_16x16x32_bf16 v[16:19], a[228:231], a[24:27], v[16:19]\n"
"ds_read_b64_tr_b16 a[246:247], v186 offset:12288\n"
"v_mfma_f32_16x16x32_bf16 v[24:27], a[228:231], a[28:31], v[24:27]\n"
"buffer_load_dwordx4 a[68:71], v207, %4, s65 offen offset:1024 sc1\n"
"v_mfma_f32_16x16x32_bf16 v[24:27], a[224:227], a[12:15], v[24:27]\n"
"s_waitcnt lgkmcnt(4)\n"
"v_mfma_f32_16x16x32_bf16 v[28:31], a[232:235], a[12:15], v[28:31]\n"
"ds_read_b64_tr_b16 a[248:249], v187 offset:0\n"
"v_mfma_f32_16x16x32_bf16 v[28:31], a[236:239], a[28:31], v[28:31]\n"
"ds_read_b64_tr_b16 a[250:251], v187 offset:4096\n"
"buffer_load_dwordx4 a[72:75], v208, %4, s65 offen offset:0 sc1\n"
"v_mfma_f32_16x16x32_bf16 v[20:23], a[236:239], a[24:27], v[20:23]\n"
"v_mfma_f32_16x16x32_bf16 v[20:23], a[232:235], a[8:11], v[20:23]\n"
"v_mfma_f32_16x16x32_bf16 v[12:15], a[232:235], a[4:7], v[12:15]\n"
"ds_read_b64_tr_b16 a[252:253], v187 offset:8192\n"
"buffer_load_dwordx4 a[76:79], v208, %4, s65 offen offset:1024 sc1\n"
"v_mfma_f32_16x16x32_bf16 v[12:15], a[236:239], a[20:23], v[12:15]\n"
"ds_read_b64_tr_b16 a[254:255], v187 offset:12288\n"
"v_mfma_f32_16x16x32_bf16 v[4:7], a[236:239], a[16:19], v[4:7]\n"
"v_mfma_f32_16x16x32_bf16 v[4:7], a[232:235], a[0:3], v[4:7]\n"
"buffer_load_dwordx4 a[80:83], v207, %4, s65 offen offset:2048 sc1\n"
"s_waitcnt lgkmcnt(4)\n"
"v_mfma_f32_16x16x32_bf16 v[32:35], a[240:243], a[0:3], v[32:35]\n"
"ds_read_b64_tr_b16 a[224:225], v188 offset:0\n"
"v_mfma_f32_16x16x32_bf16 v[32:35], a[244:247], a[16:19], v[32:35]\n"
"ds_read_b64_tr_b16 a[226:227], v188 offset:4096\n"
"v_mfma_f32_16x16x32_bf16 v[40:43], a[244:247], a[20:23], v[40:43]\n"
"buffer_load_dwordx4 a[84:87], v207, %4, s65 offen offset:3072 sc1\n"
"v_mfma_f32_16x16x32_bf16 v[40:43], a[240:243], a[4:7], v[40:43]\n"
"v_mfma_f32_16x16x32_bf16 v[48:51], a[240:243], a[8:11], v[48:51]\n"
"ds_read_b64_tr_b16 a[228:229], v188 offset:8192\n"
"v_mfma_f32_16x16x32_bf16 v[48:51], a[244:247], a[24:27], v[48:51]\n"
"ds_read_b64_tr_b16 a[230:231], v188 offset:12288\n"
"buffer_load_dwordx4 a[88:91], v208, %4, s65 offen offset:2048 sc1\n"
"v_mfma_f32_16x16x32_bf16 v[56:59], a[244:247], a[28:31], v[56:59]\n"
"v_mfma_f32_16x16x32_bf16 v[56:59], a[240:243], a[12:15], v[56:59]\n"
"s_waitcnt lgkmcnt(4)\n"
"v_mfma_f32_16x16x32_bf16 v[60:63], a[248:251], a[12:15], v[60:63]\n"
"ds_read_b64_tr_b16 a[232:233], v189 offset:0\n"
"buffer_load_dwordx4 a[92:95], v208, %4, s65 offen offset:3072 sc1\n"
"v_mfma_f32_16x16x32_bf16 v[60:63], a[252:255], a[28:31], v[60:63]\n"
"ds_read_b64_tr_b16 a[234:235], v189 offset:4096\n"
"v_mfma_f32_16x16x32_bf16 v[52:55], a[252:255], a[24:27], v[52:55]\n"
"s_add_u32 s65, s65, 4096\n"
"v_mfma_f32_16x16x32_bf16 v[52:55], a[248:251], a[8:11], v[52:55]\n"
"buffer_load_dwordx4 a[96:99], v207, %4, s65 offen offset:0 sc1\n"
"v_mfma_f32_16x16x32_bf16 v[44:47], a[248:251], a[4:7], v[44:47]\n"
"ds_read_b64_tr_b16 a[236:237], v189 offset:8192\n"
"v_mfma_f32_16x16x32_bf16 v[44:47], a[252:255], a[20:23], v[44:47]\n"
"ds_read_b64_tr_b16 a[238:239], v189 offset:12288\n"
"v_mfma_f32_16x16x32_bf16 v[36:39], a[252:255], a[16:19], v[36:39]\n"
"buffer_load_dwordx4 a[100:103], v207, %4, s65 offen offset:1024 sc1\n"
"v_mfma_f32_16x16x32_bf16 v[36:39], a[248:251], a[0:3], v[36:39]\n"
"s_waitcnt lgkmcnt(4)\n"
"v_mfma_f32_16x16x32_bf16 v[64:67], a[224:227], a[0:3], v[64:67]\n"
"ds_read_b64_tr_b16 a[240:241], v190 offset:0\n"
"v_mfma_f32_16x16x32_bf16 v[64:67], a[228:231], a[16:19], v[64:67]\n"
"ds_read_b64_tr_b16 a[242:243], v190 offset:4096\n"
"buffer_load_dwordx4 a[104:107], v208, %4, s65 offen offset:0 sc1\n"
"v_mfma_f32_16x16x32_bf16 v[72:75], a[228:231], a[20:23], v[72:75]\n"
"v_mfma_f32_16x16x32_bf16 v[72:75], a[224:227], a[4:7], v[72:75]\n"
"v_mfma_f32_16x16x32_bf16 v[80:83], a[224:227], a[8:11], v[80:83]\n"
"ds_read_b64_tr_b16 a[244:245], v190 offset:8192\n"
"buffer_load_dwordx4 a[108:111], v208, %4, s65 offen offset:1024 sc1\n"
"v_mfma_f32_16x16x32_bf16 v[80:83], a[228:231], a[24:27], v[80:83]\n"
"ds_read_b64_tr_b16 a[246:247], v190 offset:12288\n"
"v_mfma_f32_16x16x32_bf16 v[88:91], a[228:231], a[28:31], v[88:91]\n"
"v_mfma_f32_16x16x32_bf16 v[88:91], a[224:227], a[12:15], v[88:91]\n"
"buffer_load_dwordx4 a[112:115], v207, %4, s65 offen offset:2048 sc1\n"
"s_waitcnt lgkmcnt(4)\n"
"v_mfma_f32_16x16x32_bf16 v[92:95], a[232:235], a[12:15], v[92:95]\n"
"s_waitcnt vmcnt(45)\n"
"s_barrier\n"
"ds_read_b64_tr_b16 a[248:249], v184 offset:16384\n"
"v_mfma_f32_16x16x32_bf16 v[92:95], a[236:239], a[28:31], v[92:95]\n"
"ds_read_b64_tr_b16 a[250:251], v184 offset:20480\n"
"v_mfma_f32_16x16x32_bf16 v[84:87], a[236:239], a[24:27], v[84:87]\n"
"buffer_load_dwordx4 a[116:119], v207, %4, s65 offen offset:3072 sc1\n"
"v_mfma_f32_16x16x32_bf16 v[84:87], a[232:235], a[8:11], v[84:87]\n"
"v_mfma_f32_16x16x32_bf16 v[76:79], a[232:235], a[4:7], v[76:79]\n"
"ds_read_b64_tr_b16 a[252:253], v184 offset:24576\n"
"v_mfma_f32_16x16x32_bf16 v[76:79], a[236:239], a[20:23], v[76:79]\n"
"ds_read_b64_tr_b16 a[254:255], v184 offset:28672\n"
"buffer_load_dwordx4 a[120:123], v208, %4, s65 offen offset:2048 sc1\n"
"v_mfma_f32_16x16x32_bf16 v[68:71], a[236:239], a[16:19], v[68:71]\n"
"v_mfma_f32_16x16x32_bf16 v[68:71], a[232:235], a[0:3], v[68:71]\n"
"s_waitcnt lgkmcnt(4)\n"
"v_mfma_f32_16x16x32_bf16 v[96:99], a[240:243], a[0:3], v[96:99]\n"
"ds_read_b64_tr_b16 a[224:225], v185 offset:16384\n"
"buffer_load_dwordx4 a[124:127], v208, %4, s65 offen offset:3072 sc1\n"
"v_mfma_f32_16x16x32_bf16 v[96:99], a[244:247], a[16:19], v[96:99]\n"
"ds_read_b64_tr_b16 a[226:227], v185 offset:20480\n"
"v_mfma_f32_16x16x32_bf16 v[100:103], a[244:247], a[20:23], v[100:103]\n"
"v_mfma_f32_16x16x32_bf16 v[100:103], a[240:243], a[4:7], v[100:103]\n"
"v_mfma_f32_16x16x32_bf16 v[104:107], a[240:243], a[8:11], v[104:107]\n"
"ds_read_b64_tr_b16 a[228:229], v185 offset:24576\n"
"v_mfma_f32_16x16x32_bf16 v[104:107], a[244:247], a[24:27], v[104:107]\n"
"ds_read_b64_tr_b16 a[230:231], v185 offset:28672\n"
"v_mfma_f32_16x16x32_bf16 v[108:111], a[244:247], a[28:31], v[108:111]\n"
"v_mfma_f32_16x16x32_bf16 v[108:111], a[240:243], a[12:15], v[108:111]\n"
"s_waitcnt vmcnt(16) lgkmcnt(4)\n"
"v_mfma_f32_16x16x32_bf16 v[24:27], a[248:251], a[44:47], v[24:27]\n"
"ds_read_b64_tr_b16 a[232:233], v186 offset:16384\n"
"v_mfma_f32_16x16x32_bf16 v[24:27], a[252:255], a[60:63], v[24:27]\n"
"ds_read_b64_tr_b16 a[234:235], v186 offset:20480\n"
"v_mfma_f32_16x16x32_bf16 v[16:19], a[252:255], a[56:59], v[16:19]\n"
"v_mfma_f32_16x16x32_bf16 v[16:19], a[248:251], a[40:43], v[16:19]\n"
"v_mfma_f32_16x16x32_bf16 v[8:11], a[248:251], a[36:39], v[8:11]\n"
"ds_read_b64_tr_b16 a[236:237], v186 offset:24576\n"
"v_mfma_f32_16x16x32_bf16 v[8:11], a[252:255], a[52:55], v[8:11]\n"
"ds_read_b64_tr_b16 a[238:239], v186 offset:28672\n"
"s_add_u32 s65, s65, 4096\n"
"v_mfma_f32_16x16x32_bf16 v[0:3], a[252:255], a[48:51], v[0:3]\n"
"buffer_load_dwordx4 a[0:3], v207, %4, s65 offen offset:0 sc1\n"
"v_mfma_f32_16x16x32_bf16 v[0:3], a[248:251], a[32:35], v[0:3]\n"
"s_waitcnt lgkmcnt(4)\n"
"v_mfma_f32_16x16x32_bf16 v[4:7], a[224:227], a[32:35], v[4:7]\n"
"ds_read_b64_tr_b16 a[240:241], v187 offset:16384\n"
"v_mfma_f32_16x16x32_bf16 v[4:7], a[228:231], a[48:51], v[4:7]\n"
"ds_read_b64_tr_b16 a[242:243], v187 offset:20480\n"
"v_mfma_f32_16x16x32_bf16 v[12:15], a[228:231], a[52:55], v[12:15]\n"
"buffer_load_dwordx4 a[4:7], v207, %4, s65 offen offset:1024 sc1\n"
"v_mfma_f32_16x16x32_bf16 v[12:15], a[224:227], a[36:39], v[12:15]\n"
"v_mfma_f32_16x16x32_bf16 v[20:23], a[224:227], a[40:43], v[20:23]\n"
"ds_read_b64_tr_b16 a[244:245], v187 offset:24576\n"
"v_mfma_f32_16x16x32_bf16 v[20:23], a[228:231], a[56:59], v[20:23]\n"
"ds_read_b64_tr_b16 a[246:247], v187 offset:28672\n"
"v_mfma_f32_16x16x32_bf16 v[28:31], a[228:231], a[60:63], v[28:31]\n"
"buffer_load_dwordx4 a[8:11], v208, %4, s65 offen offset:0 sc1\n"
"v_mfma_f32_16x16x32_bf16 v[28:31], a[224:227], a[44:47], v[28:31]\n"
"s_waitcnt lgkmcnt(4)\n"
"v_mfma_f32_16x16x32_bf16 v[56:59], a[232:235], a[44:47], v[56:59]\n"
"ds_read_b64_tr_b16 a[248:249], v188 offset:16384\n"
"v_mfma_f32_16x16x32_bf16 v[56:59], a[236:239], a[60:63], v[56:59]\n"
"ds_read_b64_tr_b16 a[250:251], v188 offset:20480\n"
"v_mfma_f32_16x16x32_bf16 v[48:51], a[236:239], a[56:59], v[48:51]\n"
"buffer_load_dwordx4 a[12:15], v208, %4, s65 offen offset:1024 sc1\n"
"v_mfma_f32_16x16x32_bf16 v[48:51], a[232:235], a[40:43], v[48:51]\n"
"v_mfma_f32_16x16x32_bf16 v[40:43], a[232:235], a[36:39], v[40:43]\n"
"ds_read_b64_tr_b16 a[252:253], v188 offset:24576\n"
"v_mfma_f32_16x16x32_bf16 v[40:43], a[236:239], a[52:55], v[40:43]\n"
"ds_read_b64_tr_b16 a[254:255], v188 offset:28672\n"
"v_mfma_f32_16x16x32_bf16 v[32:35], a[236:239], a[48:51], v[32:35]\n"
"buffer_load_dwordx4 a[16:19], v207, %4, s65 offen offset:2048 sc1\n"
"v_mfma_f32_16x16x32_bf16 v[32:35], a[232:235], a[32:35], v[32:35]\n"
"s_waitcnt lgkmcnt(4)\n"
"v_mfma_f32_16x16x32_bf16 v[36:39], a[240:243], a[32:35], v[36:39]\n"
"ds_read_b64_tr_b16 a[224:225], v189 offset:16384\n"
"v_mfma_f32_16x16x32_bf16 v[36:39], a[244:247], a[48:51], v[36:39]\n"
"ds_read_b64_tr_b16 a[226:227], v189 offset:20480\n"
"v_mfma_f32_16x16x32_bf16 v[44:47], a[244:247], a[52:55], v[44:47]\n"
"buffer_load_dwordx4 a[20:23], v207, %4, s65 offen offset:3072 sc1\n"
"v_mfma_f32_16x16x32_bf16 v[44:47], a[240:243], a[36:39], v[44:47]\n"
"v_mfma_f32_16x16x32_bf16 v[52:55], a[240:243], a[40:43], v[52:55]\n"
"ds_read_b64_tr_b16 a[228:229], v189 offset:24576\n"
"v_mfma_f32_16x16x32_bf16 v[52:55], a[244:247], a[56:59], v[52:55]\n"
"ds_read_b64_tr_b16 a[230:231], v189 offset:28672\n"
"v_mfma_f32_16x16x32_bf16 v[60:63], a[244:247], a[60:63], v[60:63]\n"
"buffer_load_dwordx4 a[24:27], v208, %4, s65 offen offset:2048 sc1\n"
"v_mfma_f32_16x16x32_bf16 v[60:63], a[240:243], a[44:47], v[60:63]\n"
"s_waitcnt lgkmcnt(4)\n"
"v_mfma_f32_16x16x32_bf16 v[88:91], a[248:251], a[44:47], v[88:91]\n"
"ds_read_b64_tr_b16 a[232:233], v190 offset:16384\n"
"v_mfma_f32_16x16x32_bf16 v[88:91], a[252:255], a[60:63], v[88:91]\n"
"ds_read_b64_tr_b16 a[234:235], v190 offset:20480\n"
"v_mfma_f32_16x16x32_bf16 v[80:83], a[252:255], a[56:59], v[80:83]\n"
"buffer_load_dwordx4 a[28:31], v208, %4, s65 offen offset:3072 sc1\n"
"v_mfma_f32_16x16x32_bf16 v[80:83], a[248:251], a[40:43], v[80:83]\n"
"v_mfma_f32_16x16x32_bf16 v[72:75], a[248:251], a[36:39], v[72:75]\n"
"ds_read_b64_tr_b16 a[236:237], v190 offset:24576\n"
"v_mfma_f32_16x16x32_bf16 v[72:75], a[252:255], a[52:55], v[72:75]\n"
"ds_read_b64_tr_b16 a[238:239], v190 offset:28672\n"
"v_mfma_f32_16x16x32_bf16 v[64:67], a[252:255], a[48:51], v[64:67]\n"
"v_mfma_f32_16x16x32_bf16 v[64:67], a[248:251], a[32:35], v[64:67]\n"
"s_waitcnt lgkmcnt(4)\n"
"v_mfma_f32_16x16x32_bf16 v[68:71], a[224:227], a[32:35], v[68:71]\n"
"s_waitcnt vmcnt(52)\n"
"s_barrier\n"
"ds_read_b64_tr_b16 a[240:241], v184 offset:32768\n"
"v_mfma_f32_16x16x32_bf16 v[68:71], a[228:231], a[48:51], v[68:71]\n"
"ds_read_b64_tr_b16 a[242:243], v184 offset:36864\n"
"v_mfma_f32_16x16x32_bf16 v[76:79], a[228:231], a[52:55], v[76:79]\n"
"v_mfma_f32_16x16x32_bf16 v[76:79], a[224:227], a[36:39], v[76:79]\n"
"v_mfma_f32_16x16x32_bf16 v[84:87], a[224:227], a[40:43], v[84:87]\n"
"ds_read_b64_tr_b16 a[244:245], v184 offset:40960\n"
"v_mfma_f32_16x16x32_bf16 v[84:87], a[228:231], a[56:59], v[84:87]\n"
"ds_read_b64_tr_b16 a[246:247], v184 offset:45056\n"
"v_mfma_f32_16x16x32_bf16 v[92:95], a[228:231], a[60:63], v[92:95]\n"
"v_mfma_f32_16x16x32_bf16 v[92:95], a[224:227], a[44:47], v[92:95]\n"
"s_waitcnt lgkmcnt(4)\n"
"v_mfma_f32_16x16x32_bf16 v[108:111], a[232:235], a[44:47], v[108:111]\n"
"ds_read_b64_tr_b16 a[248:249], v185 offset:32768\n"
"v_mfma_f32_16x16x32_bf16 v[108:111], a[236:239], a[60:63], v[108:111]\n"
"ds_read_b64_tr_b16 a[250:251], v185 offset:36864\n"
"v_mfma_f32_16x16x32_bf16 v[104:107], a[236:239], a[56:59], v[104:107]\n"
"v_mfma_f32_16x16x32_bf16 v[104:107], a[232:235], a[40:43], v[104:107]\n"
"v_mfma_f32_16x16x32_bf16 v[100:103], a[232:235], a[36:39], v[100:103]\n"
"ds_read_b64_tr_b16 a[252:253], v185 offset:40960\n"
"v_mfma_f32_16x16x32_bf16 v[100:103], a[236:239], a[52:55], v[100:103]\n"
"ds_read_b64_tr_b16 a[254:255], v185 offset:45056\n"
"v_mfma_f32_16x16x32_bf16 v[96:99], a[236:239], a[48:51], v[96:99]\n"
"v_mfma_f32_16x16x32_bf16 v[96:99], a[232:235], a[32:35], v[96:99]\n"
"s_mov_b32 s66, 2\n"
"LOOP_E%=:\n"
"s_waitcnt vmcnt(16) lgkmcnt(4)\n"
"v_mfma_f32_16x16x32_bf16 v[0:3], a[240:243], a[64:67], v[0:3]\n"
"ds_read_b64_tr_b16 a[224:225], v186 offset:32768\n"
"v_mfma_f32_16x16x32_bf16 v[0:3], a[244:247], a[80:83], v[0:3]\n"
"ds_read_b64_tr_b16 a[226:227], v186 offset:36864\n"
"v_mfma_f32_16x16x32_bf16 v[8:11], a[244:247], a[84:87], v[8:11]\n"
"v_mfma_f32_16x16x32_bf16 v[8:11], a[240:243], a[68:71], v[8:11]\n"
"v_mfma_f32_16x16x32_bf16 v[16:19], a[240:243], a[72:75], v[16:19]\n"
"ds_read_b64_tr_b16 a[228:229], v186 offset:40960\n"
"s_add_u32 s64, s64, 458752\n"
"v_mfma_f32_16x16x32_bf16 v[16:19], a[244:247], a[88:91], v[16:19]\n"
"ds_read_b64_tr_b16 a[230:231], v186 offset:45056\n"
"s_add_u32 m0, %15, 0\n"
"v_mfma_f32_16x16x32_bf16 v[24:27], a[244:247], a[92:95], v[24:27]\n"
"buffer_load_dwordx4 v209, %5, s64 offen offset:0 lds\n"
"v_mfma_f32_16x16x32_bf16 v[24:27], a[240:243], a[76:79], v[24:27]\n"
"s_waitcnt lgkmcnt(4)\n"
"v_mfma_f32_16x16x32_bf16 v[28:31], a[248:251], a[76:79], v[28:31]\n"
"ds_read_b64_tr_b16 a[232:233], v187 offset:32768\n"
"v_mfma_f32_16x16x32_bf16 v[28:31], a[252:255], a[92:95], v[28:31]\n"
"ds_read_b64_tr_b16 a[234:235], v187 offset:36864\n"
"v_mfma_f32_16x16x32_bf16 v[20:23], a[252:255], a[88:91], v[20:23]\n"
"buffer_load_dwordx4 v210, %5, s64 offen offset:1024 lds\n"
"v_mfma_f32_16x16x32_bf16 v[20:23], a[248:251], a[72:75], v[20:23]\n"
"v_mfma_f32_16x16x32_bf16 v[12:15], a[248:251], a[68:71], v[12:15]\n"
"ds_read_b64_tr_b16 a[236:237], v187 offset:40960\n"
"v_mfma_f32_16x16x32_bf16 v[12:15], a[252:255], a[84:87], v[12:15]\n"
"ds_read_b64_tr_b16 a[238:239], v187 offset:45056\n"
"v_mfma_f32_16x16x32_bf16 v[4:7], a[252:255], a[80:83], v[4:7]\n"
"buffer_load_dwordx4 v211, %5, s64 offen offset:2048 lds\n"
"v_mfma_f32_16x16x32_bf16 v[4:7], a[248:251], a[64:67], v[4:7]\n"
"s_waitcnt lgkmcnt(4)\n"
"v_mfma_f32_16x16x32_bf16 v[32:35], a[224:227], a[64:67], v[32:35]\n"
"ds_read_b64_tr_b16 a[240:241], v188 offset:32768\n"
"v_mfma_f32_16x16x32_bf16 v[32:35], a[228:231], a[80:83], v[32:35]\n"
"ds_read_b64_tr_b16 a[242:243], v188 offset:36864\n"
"v_mfma_f32_16x16x32_bf16 v[40:43], a[228:231], a[84:87], v[40:43]\n"
"buffer_load_dwordx4 v212, %5, s64 offen offset:3072 lds\n"
"v_mfma_f32_16x16x32_bf16 v[40:43], a[224:227], a[68:71], v[40:43]\n"
"v_mfma_f32_16x16x32_bf16 v[48:51], a[224:227], a[72:75], v[48:51]\n"
"ds_read_b64_tr_b16 a[244:245], v188 offset:40960\n"
"v_mfma_f32_16x16x32_bf16 v[48:51], a[228:231], a[88:91], v[48:51]\n"
"ds_read_b64_tr_b16 a[246:247], v188 offset:45056\n"
"s_add_u32 s65, s65, 4096\n"
"v_mfma_f32_16x16x32_bf16 v[56:59], a[228:231], a[92:95], v[56:59]\n"
"buffer_load_dwordx4 a[32:35], v207, %4, s65 offen offset:0 sc1\n"
"v_mfma_f32_16x16x32_bf16 v[56:59], a[224:227], a[76:79], v[56:59]\n"
"s_waitcnt lgkmcnt(4)\n"
"v_mfma_f32_16x16x32_bf16 v[60:63], a[232:235], a[76:79], v[60:63]\n"
"ds_read_b64_tr_b16 a[248:249], v189 offset:32768\n"
"v_mfma_f32_16x16x32_bf16 v[60:63], a[236:239], a[92:95], v[60:63]\n"
"ds_read_b64_tr_b16 a[250:251], v189 offset:36864\n"
"v_mfma_f32_16x16x32_bf16 v[52:55], a[236:239], a[88:91], v[52:55]\n"
"buffer_load_dwordx4 a[36:39], v207, %4, s65 offen offset:1024 sc1\n"
"v_mfma_f32_16x16x32_bf16 v[52:55], a[232:235], a[72:75], v[52:55]\n"
"v_mfma_f32_16x16x32_bf16 v[44:47], a[232:235], a[68:71], v[44:47]\n"
"ds_read_b64_tr_b16 a[252:253], v189 offset:40960\n"
"v_mfma_f32_16x16x32_bf16 v[44:47], a[236:239], a[84:87], v[44:47]\n"
"ds_read_b64_tr_b16 a[254:255], v189 offset:45056\n"
"v_mfma_f32_16x16x32_bf16 v[36:39], a[236:239], a[80:83], v[36:39]\n"
"buffer_load_dwordx4 a[40:43], v208, %4, s65 offen offset:0 sc1\n"
"v_mfma_f32_16x16x32_bf16 v[36:39], a[232:235], a[64:67], v[36:39]\n"
"s_waitcnt lgkmcnt(4)\n"
"v_mfma_f32_16x16x32_bf16 v[64:67], a[240:243], a[64:67], v[64:67]\n"
"ds_read_b64_tr_b16 a[224:225], v190 offset:32768\n"
"v_mfma_f32_16x16x32_bf16 v[64:67], a[244:247], a[80:83], v[64:67]\n"
"ds_read_b64_tr_b16 a[226:227], v190 offset:36864\n"
"v_mfma_f32_16x16x32_bf16 v[72:75], a[244:247], a[84:87], v[72:75]\n"
"buffer_load_dwordx4 a[44:47], v208, %4, s65 offen offset:1024 sc1\n"
"v_mfma_f32_16x16x32_bf16 v[72:75], a[240:243], a[68:71], v[72:75]\n"
"v_mfma_f32_16x16x32_bf16 v[80:83], a[240:243], a[72:75], v[80:83]\n"
"ds_read_b64_tr_b16 a[228:229], v190 offset:40960\n"
"v_mfma_f32_16x16x32_bf16 v[80:83], a[244:247], a[88:91], v[80:83]\n"
"ds_read_b64_tr_b16 a[230:231], v190 offset:45056\n"
"v_mfma_f32_16x16x32_bf16 v[88:91], a[244:247], a[92:95], v[88:91]\n"
"buffer_load_dwordx4 a[48:51], v207, %4, s65 offen offset:2048 sc1\n"
"v_mfma_f32_16x16x32_bf16 v[88:91], a[240:243], a[76:79], v[88:91]\n"
"s_waitcnt lgkmcnt(4)\n"
"v_mfma_f32_16x16x32_bf16 v[92:95], a[248:251], a[76:79], v[92:95]\n"
"s_waitcnt vmcnt(17)\n"
"s_barrier\n"
"ds_read_b64_tr_b16 a[232:233], v184 offset:49152\n"
"v_mfma_f32_16x16x32_bf16 v[92:95], a[252:255], a[92:95], v[92:95]\n"
"ds_read_b64_tr_b16 a[234:235], v184 offset:53248\n"
"v_mfma_f32_16x16x32_bf16 v[84:87], a[252:255], a[88:91], v[84:87]\n"
"buffer_load_dwordx4 a[52:55], v207, %4, s65 offen offset:3072 sc1\n"
"v_mfma_f32_16x16x32_bf16 v[84:87], a[248:251], a[72:75], v[84:87]\n"
"v_mfma_f32_16x16x32_bf16 v[76:79], a[248:251], a[68:71], v[76:79]\n"
"ds_read_b64_tr_b16 a[236:237], v184 offset:57344\n"
"v_mfma_f32_16x16x32_bf16 v[76:79], a[252:255], a[84:87], v[76:79]\n"
"ds_read_b64_tr_b16 a[238:239], v184 offset:61440\n"
"v_mfma_f32_16x16x32_bf16 v[68:71], a[252:255], a[80:83], v[68:71]\n"
"buffer_load_dwordx4 a[56:59], v208, %4, s65 offen offset:2048 sc1\n"
"v_mfma_f32_16x16x32_bf16 v[68:71], a[248:251], a[64:67], v[68:71]\n"
"s_waitcnt lgkmcnt(4)\n"
"v_mfma_f32_16x16x32_bf16 v[96:99], a[224:227], a[64:67], v[96:99]\n"
"ds_read_b64_tr_b16 a[240:241], v185 offset:49152\n"
"v_mfma_f32_16x16x32_bf16 v[96:99], a[228:231], a[80:83], v[96:99]\n"
"ds_read_b64_tr_b16 a[242:243], v185 offset:53248\n"
"v_mfma_f32_16x16x32_bf16 v[100:103], a[228:231], a[84:87], v[100:103]\n"
"buffer_load_dwordx4 a[60:63], v208, %4, s65 offen offset:3072 sc1\n"
"v_mfma_f32_16x16x32_bf16 v[100:103], a[224:227], a[68:71], v[100:103]\n"
"v_mfma_f32_16x16x32_bf16 v[104:107], a[224:227], a[72:75], v[104:107]\n"
"ds_read_b64_tr_b16 a[244:245], v185 offset:57344\n"
"v_mfma_f32_16x16x32_bf16 v[104:107], a[228:231], a[88:91], v[104:107]\n"
"ds_read_b64_tr_b16 a[246:247], v185 offset:61440\n"
"v_mfma_f32_16x16x32_bf16 v[108:111], a[228:231], a[92:95], v[108:111]\n"
"v_mfma_f32_16x16x32_bf16 v[108:111], a[224:227], a[76:79], v[108:111]\n"
"s_waitcnt vmcnt(20) lgkmcnt(4)\n"
"v_mfma_f32_16x16x32_bf16 v[24:27], a[232:235], a[108:111], v[24:27]\n"
"ds_read_b64_tr_b16 a[248:249], v186 offset:49152\n"
"v_mfma_f32_16x16x32_bf16 v[24:27], a[236:239], a[124:127], v[24:27]\n"
"ds_read_b64_tr_b16 a[250:251], v186 offset:53248\n"
"v_mfma_f32_16x16x32_bf16 v[16:19], a[236:239], a[120:123], v[16:19]\n"
"v_mfma_f32_16x16x32_bf16 v[16:19], a[232:235], a[104:107], v[16:19]\n"
"v_mfma_f32_16x16x32_bf16 v[8:11], a[232:235], a[100:103], v[8:11]\n"
"ds_read_b64_tr_b16 a[252:253], v186 offset:57344\n"
"s_add_u32 s64, s64, 458752\n"
"v_mfma_f32_16x16x32_bf16 v[8:11], a[236:239], a[116:119], v[8:11]\n"
"ds_read_b64_tr_b16 a[254:255], v186 offset:61440\n"
"s_add_u32 m0, %15, 16384\n"
"v_mfma_f32_16x16x32_bf16 v[0:3], a[236:239], a[112:115], v[0:3]\n"
"buffer_load_dwordx4 v209, %5, s64 offen offset:0 lds\n"
"v_mfma_f32_16x16x32_bf16 v[0:3], a[232:235], a[96:99], v[0:3]\n"
"s_waitcnt lgkmcnt(4)\n"
"v_mfma_f32_16x16x32_bf16 v[4:7], a[240:243], a[96:99], v[4:7]\n"
"ds_read_b64_tr_b16 a[224:225], v187 offset:49152\n"
"v_mfma_f32_16x16x32_bf16 v[4:7], a[244:247], a[112:115], v[4:7]\n"
"ds_read_b64_tr_b16 a[226:227], v187 offset:53248\n"
"v_mfma_f32_16x16x32_bf16 v[12:15], a[244:247], a[116:119], v[12:15]\n"
"buffer_load_dwordx4 v210, %5, s64 offen offset:1024 lds\n"
"v_mfma_f32_16x16x32_bf16 v[12:15], a[240:243], a[100:103], v[12:15]\n"
"v_mfma_f32_16x16x32_bf16 v[20:23], a[240:243], a[104:107], v[20:23]\n"
"ds_read_b64_tr_b16 a[228:229], v187 offset:57344\n"
"v_mfma_f32_16x16x32_bf16 v[20:23], a[244:247], a[120:123], v[20:23]\n"
"ds_read_b64_tr_b16 a[230:231], v187 offset:61440\n"
"v_mfma_f32_16x16x32_bf16 v[28:31], a[244:247], a[124:127], v[28:31]\n"
"buffer_load_dwordx4 v211, %5, s64 offen offset:2048 lds\n"
"v_mfma_f32_16x16x32_bf16 v[28:31], a[240:243], a[108:111], v[28:31]\n"
"s_waitcnt lgkmcnt(4)\n"
"v_mfma_f32_16x16x32_bf16 v[56:59], a[248:251], a[108:111], v[56:59]\n"
"ds_read_b64_tr_b16 a[232:233], v188 offset:49152\n"
"v_mfma_f32_16x16x32_bf16 v[56:59], a[252:255], a[124:127], v[56:59]\n"
"ds_read_b64_tr_b16 a[234:235], v188 offset:53248\n"
"v_mfma_f32_16x16x32_bf16 v[48:51], a[252:255], a[120:123], v[48:51]\n"
"buffer_load_dwordx4 v212, %5, s64 offen offset:3072 lds\n"
"v_mfma_f32_16x16x32_bf16 v[48:51], a[248:251], a[104:107], v[48:51]\n"
"v_mfma_f32_16x16x32_bf16 v[40:43], a[248:251], a[100:103], v[40:43]\n"
"ds_read_b64_tr_b16 a[236:237], v188 offset:57344\n"
"v_mfma_f32_16x16x32_bf16 v[40:43], a[252:255], a[116:119], v[40:43]\n"
"ds_read_b64_tr_b16 a[238:239], v188 offset:61440\n"
"s_add_u32 s65, s65, 4096\n"
"v_mfma_f32_16x16x32_bf16 v[32:35], a[252:255], a[112:115], v[32:35]\n"
"buffer_load_dwordx4 a[64:67], v207, %4, s65 offen offset:0 sc1\n"
"v_mfma_f32_16x16x32_bf16 v[32:35], a[248:251], a[96:99], v[32:35]\n"
"s_waitcnt lgkmcnt(4)\n"
"v_mfma_f32_16x16x32_bf16 v[36:39], a[224:227], a[96:99], v[36:39]\n"
"ds_read_b64_tr_b16 a[240:241], v189 offset:49152\n"
"v_mfma_f32_16x16x32_bf16 v[36:39], a[228:231], a[112:115], v[36:39]\n"
"ds_read_b64_tr_b16 a[242:243], v189 offset:53248\n"
"v_mfma_f32_16x16x32_bf16 v[44:47], a[228:231], a[116:119], v[44:47]\n"
"buffer_load_dwordx4 a[68:71], v207, %4, s65 offen offset:1024 sc1\n"
"v_mfma_f32_16x16x32_bf16 v[44:47], a[224:227], a[100:103], v[44:47]\n"
"v_mfma_f32_16x16x32_bf16 v[52:55], a[224:227], a[104:107], v[52:55]\n"
"ds_read_b64_tr_b16 a[244:245], v189 offset:57344\n"
"v_mfma_f32_16x16x32_bf16 v[52:55], a[228:231], a[120:123], v[52:55]\n"
"ds_read_b64_tr_b16 a[246:247], v189 offset:61440\n"
"v_mfma_f32_16x16x32_bf16 v[60:63], a[228:231], a[124:127], v[60:63]\n"
"buffer_load_dwordx4 a[72:75], v208, %4, s65 offen offset:0 sc1\n"
"v_mfma_f32_16x16x32_bf16 v[60:63], a[224:227], a[108:111], v[60:63]\n"
"s_waitcnt lgkmcnt(4)\n"
"v_mfma_f32_16x16x32_bf16 v[88:91], a[232:235], a[108:111], v[88:91]\n"
"ds_read_b64_tr_b16 a[248:249], v190 offset:49152\n"
"v_mfma_f32_16x16x32_bf16 v[88:91], a[236:239], a[124:127], v[88:91]\n"
"ds_read_b64_tr_b16 a[250:251], v190 offset:53248\n"
"v_mfma_f32_16x16x32_bf16 v[80:83], a[236:239], a[120:123], v[80:83]\n"
"buffer_load_dwordx4 a[76:79], v208, %4, s65 offen offset:1024 sc1\n"
"v_mfma_f32_16x16x32_bf16 v[80:83], a[232:235], a[104:107], v[80:83]\n"
"v_mfma_f32_16x16x32_bf16 v[72:75], a[232:235], a[100:103], v[72:75]\n"
"ds_read_b64_tr_b16 a[252:253], v190 offset:57344\n"
"v_mfma_f32_16x16x32_bf16 v[72:75], a[236:239], a[116:119], v[72:75]\n"
"ds_read_b64_tr_b16 a[254:255], v190 offset:61440\n"
"v_mfma_f32_16x16x32_bf16 v[64:67], a[236:239], a[112:115], v[64:67]\n"
"buffer_load_dwordx4 a[80:83], v207, %4, s65 offen offset:2048 sc1\n"
"v_mfma_f32_16x16x32_bf16 v[64:67], a[232:235], a[96:99], v[64:67]\n"
"s_waitcnt lgkmcnt(4)\n"
"v_mfma_f32_16x16x32_bf16 v[68:71], a[240:243], a[96:99], v[68:71]\n"
"s_waitcnt vmcnt(17)\n"
"s_barrier\n"
"ds_read_b64_tr_b16 a[224:225], v184 offset:0\n"
"v_mfma_f32_16x16x32_bf16 v[68:71], a[244:247], a[112:115], v[68:71]\n"
"ds_read_b64_tr_b16 a[226:227], v184 offset:4096\n"
"v_mfma_f32_16x16x32_bf16 v[76:79], a[244:247], a[116:119], v[76:79]\n"
"buffer_load_dwordx4 a[84:87], v207, %4, s65 offen offset:3072 sc1\n"
"v_mfma_f32_16x16x32_bf16 v[76:79], a[240:243], a[100:103], v[76:79]\n"
"v_mfma_f32_16x16x32_bf16 v[84:87], a[240:243], a[104:107], v[84:87]\n"
"ds_read_b64_tr_b16 a[228:229], v184 offset:8192\n"
"v_mfma_f32_16x16x32_bf16 v[84:87], a[244:247], a[120:123], v[84:87]\n"
"ds_read_b64_tr_b16 a[230:231], v184 offset:12288\n"
"v_mfma_f32_16x16x32_bf16 v[92:95], a[244:247], a[124:127], v[92:95]\n"
"buffer_load_dwordx4 a[88:91], v208, %4, s65 offen offset:2048 sc1\n"
"v_mfma_f32_16x16x32_bf16 v[92:95], a[240:243], a[108:111], v[92:95]\n"
"s_waitcnt lgkmcnt(4)\n"
"v_mfma_f32_16x16x32_bf16 v[108:111], a[248:251], a[108:111], v[108:111]\n"
"ds_read_b64_tr_b16 a[232:233], v185 offset:0\n"
"v_mfma_f32_16x16x32_bf16 v[108:111], a[252:255], a[124:127], v[108:111]\n"
"ds_read_b64_tr_b16 a[234:235], v185 offset:4096\n"
"v_mfma_f32_16x16x32_bf16 v[104:107], a[252:255], a[120:123], v[104:107]\n"
"buffer_load_dwordx4 a[92:95], v208, %4, s65 offen offset:3072 sc1\n"
"v_mfma_f32_16x16x32_bf16 v[104:107], a[248:251], a[104:107], v[104:107]\n"
"v_mfma_f32_16x16x32_bf16 v[100:103], a[248:251], a[100:103], v[100:103]\n"
"ds_read_b64_tr_b16 a[236:237], v185 offset:8192\n"
"v_mfma_f32_16x16x32_bf16 v[100:103], a[252:255], a[116:119], v[100:103]\n"
"ds_read_b64_tr_b16 a[238:239], v185 offset:12288\n"
"v_mfma_f32_16x16x32_bf16 v[96:99], a[252:255], a[112:115], v[96:99]\n"
"v_mfma_f32_16x16x32_bf16 v[96:99], a[248:251], a[96:99], v[96:99]\n"
"s_waitcnt vmcnt(24) lgkmcnt(4)\n"
"v_mfma_f32_16x16x32_bf16 v[0:3], a[224:227], a[0:3], v[0:3]\n"
"ds_read_b64_tr_b16 a[240:241], v186 offset:0\n"
"v_mfma_f32_16x16x32_bf16 v[0:3], a[228:231], a[16:19], v[0:3]\n"
"ds_read_b64_tr_b16 a[242:243], v186 offset:4096\n"
"v_mfma_f32_16x16x32_bf16 v[8:11], a[228:231], a[20:23], v[8:11]\n"
"v_mfma_f32_16x16x32_bf16 v[8:11], a[224:227], a[4:7], v[8:11]\n"
"v_mfma_f32_16x16x32_bf16 v[16:19], a[224:227], a[8:11], v[16:19]\n"
"ds_read_b64_tr_b16 a[244:245], v186 offset:8192\n"
"s_add_u32 s64, s64, 458752\n"
"v_mfma_f32_16x16x32_bf16 v[16:19], a[228:231], a[24:27], v[16:19]\n"
"ds_read_b64_tr_b16 a[246:247], v186 offset:12288\n"
"s_add_u32 m0, %15, 32768\n"
"v_mfma_f32_16x16x32_bf16 v[24:27], a[228:231], a[28:31], v[24:27]\n"
"buffer_load_dwordx4 v209, %5, s64 offen offset:0 lds\n"
"v_mfma_f32_16x16x32_bf16 v[24:27], a[224:227], a[12:15], v[24:27]\n"
"s_waitcnt lgkmcnt(4)\n"
"v_mfma_f32_16x16x32_bf16 v[28:31], a[232:235], a[12:15], v[28:31]\n"
"ds_read_b64_tr_b16 a[248:249], v187 offset:0\n"
"v_mfma_f32_16x16x32_bf16 v[28:31], a[236:239], a[28:31], v[28:31]\n"
"ds_read_b64_tr_b16 a[250:251], v187 offset:4096\n"
"v_mfma_f32_16x16x32_bf16 v[20:23], a[236:239], a[24:27], v[20:23]\n"
"buffer_load_dwordx4 v210, %5, s64 offen offset:1024 lds\n"
"v_mfma_f32_16x16x32_bf16 v[20:23], a[232:235], a[8:11], v[20:23]\n"
"v_mfma_f32_16x16x32_bf16 v[12:15], a[232:235], a[4:7], v[12:15]\n"
"ds_read_b64_tr_b16 a[252:253], v187 offset:8192\n"
"v_mfma_f32_16x16x32_bf16 v[12:15], a[236:239], a[20:23], v[12:15]\n"
"ds_read_b64_tr_b16 a[254:255], v187 offset:12288\n"
"v_mfma_f32_16x16x32_bf16 v[4:7], a[236:239], a[16:19], v[4:7]\n"
"buffer_load_dwordx4 v211, %5, s64 offen offset:2048 lds\n"
"v_mfma_f32_16x16x32_bf16 v[4:7], a[232:235], a[0:3], v[4:7]\n"
"s_waitcnt lgkmcnt(4)\n"
"v_mfma_f32_16x16x32_bf16 v[32:35], a[240:243], a[0:3], v[32:35]\n"
"ds_read_b64_tr_b16 a[224:225], v188 offset:0\n"
"v_mfma_f32_16x16x32_bf16 v[32:35], a[244:247], a[16:19], v[32:35]\n"
"ds_read_b64_tr_b16 a[226:227], v188 offset:4096\n"
"v_mfma_f32_16x16x32_bf16 v[40:43], a[244:247], a[20:23], v[40:43]\n"
"buffer_load_dwordx4 v212, %5, s64 offen offset:3072 lds\n"
"v_mfma_f32_16x16x32_bf16 v[40:43], a[240:243], a[4:7], v[40:43]\n"
"v_mfma_f32_16x16x32_bf16 v[48:51], a[240:243], a[8:11], v[48:51]\n"
"ds_read_b64_tr_b16 a[228:229], v188 offset:8192\n"
"v_mfma_f32_16x16x32_bf16 v[48:51], a[244:247], a[24:27], v[48:51]\n"
"ds_read_b64_tr_b16 a[230:231], v188 offset:12288\n"
"s_add_u32 s65, s65, 4096\n"
"v_mfma_f32_16x16x32_bf16 v[56:59], a[244:247], a[28:31], v[56:59]\n"
"buffer_load_dwordx4 a[96:99], v207, %4, s65 offen offset:0 sc1\n"
"v_mfma_f32_16x16x32_bf16 v[56:59], a[240:243], a[12:15], v[56:59]\n"
"s_waitcnt lgkmcnt(4)\n"
"v_mfma_f32_16x16x32_bf16 v[60:63], a[248:251], a[12:15], v[60:63]\n"
"ds_read_b64_tr_b16 a[232:233], v189 offset:0\n"
"v_mfma_f32_16x16x32_bf16 v[60:63], a[252:255], a[28:31], v[60:63]\n"
"ds_read_b64_tr_b16 a[234:235], v189 offset:4096\n"
"v_mfma_f32_16x16x32_bf16 v[52:55], a[252:255], a[24:27], v[52:55]\n"
"buffer_load_dwordx4 a[100:103], v207, %4, s65 offen offset:1024 sc1\n"
"v_mfma_f32_16x16x32_bf16 v[52:55], a[248:251], a[8:11], v[52:55]\n"
"v_mfma_f32_16x16x32_bf16 v[44:47], a[248:251], a[4:7], v[44:47]\n"
"ds_read_b64_tr_b16 a[236:237], v189 offset:8192\n"
"v_mfma_f32_16x16x32_bf16 v[44:47], a[252:255], a[20:23], v[44:47]\n"
"ds_read_b64_tr_b16 a[238:239], v189 offset:12288\n"
"v_mfma_f32_16x16x32_bf16 v[36:39], a[252:255], a[16:19], v[36:39]\n"
"buffer_load_dwordx4 a[104:107], v208, %4, s65 offen offset:0 sc1\n"
"v_mfma_f32_16x16x32_bf16 v[36:39], a[248:251], a[0:3], v[36:39]\n"
"s_waitcnt lgkmcnt(4)\n"
"v_mfma_f32_16x16x32_bf16 v[64:67], a[224:227], a[0:3], v[64:67]\n"
"ds_read_b64_tr_b16 a[240:241], v190 offset:0\n"
"v_mfma_f32_16x16x32_bf16 v[64:67], a[228:231], a[16:19], v[64:67]\n"
"ds_read_b64_tr_b16 a[242:243], v190 offset:4096\n"
"v_mfma_f32_16x16x32_bf16 v[72:75], a[228:231], a[20:23], v[72:75]\n"
"buffer_load_dwordx4 a[108:111], v208, %4, s65 offen offset:1024 sc1\n"
"v_mfma_f32_16x16x32_bf16 v[72:75], a[224:227], a[4:7], v[72:75]\n"
"v_mfma_f32_16x16x32_bf16 v[80:83], a[224:227], a[8:11], v[80:83]\n"
"ds_read_b64_tr_b16 a[244:245], v190 offset:8192\n"
"v_mfma_f32_16x16x32_bf16 v[80:83], a[228:231], a[24:27], v[80:83]\n"
"ds_read_b64_tr_b16 a[246:247], v190 offset:12288\n"
"v_mfma_f32_16x16x32_bf16 v[88:91], a[228:231], a[28:31], v[88:91]\n"
"buffer_load_dwordx4 a[112:115], v207, %4, s65 offen offset:2048 sc1\n"
"v_mfma_f32_16x16x32_bf16 v[88:91], a[224:227], a[12:15], v[88:91]\n"
"s_waitcnt lgkmcnt(4)\n"
"v_mfma_f32_16x16x32_bf16 v[92:95], a[232:235], a[12:15], v[92:95]\n"
"s_waitcnt vmcnt(17)\n"
"s_barrier\n"
"ds_read_b64_tr_b16 a[248:249], v184 offset:16384\n"
"v_mfma_f32_16x16x32_bf16 v[92:95], a[236:239], a[28:31], v[92:95]\n"
"ds_read_b64_tr_b16 a[250:251], v184 offset:20480\n"
"v_mfma_f32_16x16x32_bf16 v[84:87], a[236:239], a[24:27], v[84:87]\n"
"buffer_load_dwordx4 a[116:119], v207, %4, s65 offen offset:3072 sc1\n"
"v_mfma_f32_16x16x32_bf16 v[84:87], a[232:235], a[8:11], v[84:87]\n"
"v_mfma_f32_16x16x32_bf16 v[76:79], a[232:235], a[4:7], v[76:79]\n"
"ds_read_b64_tr_b16 a[252:253], v184 offset:24576\n"
"v_mfma_f32_16x16x32_bf16 v[76:79], a[236:239], a[20:23], v[76:79]\n"
"ds_read_b64_tr_b16 a[254:255], v184 offset:28672\n"
"v_mfma_f32_16x16x32_bf16 v[68:71], a[236:239], a[16:19], v[68:71]\n"
"buffer_load_dwordx4 a[120:123], v208, %4, s65 offen offset:2048 sc1\n"
"v_mfma_f32_16x16x32_bf16 v[68:71], a[232:235], a[0:3], v[68:71]\n"
"s_waitcnt lgkmcnt(4)\n"
"v_mfma_f32_16x16x32_bf16 v[96:99], a[240:243], a[0:3], v[96:99]\n"
"ds_read_b64_tr_b16 a[224:225], v185 offset:16384\n"
"v_mfma_f32_16x16x32_bf16 v[96:99], a[244:247], a[16:19], v[96:99]\n"
"ds_read_b64_tr_b16 a[226:227], v185 offset:20480\n"
"v_mfma_f32_16x16x32_bf16 v[100:103], a[244:247], a[20:23], v[100:103]\n"
"buffer_load_dwordx4 a[124:127], v208, %4, s65 offen offset:3072 sc1\n"
"v_mfma_f32_16x16x32_bf16 v[100:103], a[240:243], a[4:7], v[100:103]\n"
"v_mfma_f32_16x16x32_bf16 v[104:107], a[240:243], a[8:11], v[104:107]\n"
"ds_read_b64_tr_b16 a[228:229], v185 offset:24576\n"
"v_mfma_f32_16x16x32_bf16 v[104:107], a[244:247], a[24:27], v[104:107]\n"
"ds_read_b64_tr_b16 a[230:231], v185 offset:28672\n"
"v_mfma_f32_16x16x32_bf16 v[108:111], a[244:247], a[28:31], v[108:111]\n"
"v_mfma_f32_16x16x32_bf16 v[108:111], a[240:243], a[12:15], v[108:111]\n"
"s_waitcnt vmcnt(24) lgkmcnt(4)\n"
"v_mfma_f32_16x16x32_bf16 v[24:27], a[248:251], a[44:47], v[24:27]\n"
"ds_read_b64_tr_b16 a[232:233], v186 offset:16384\n"
"v_mfma_f32_16x16x32_bf16 v[24:27], a[252:255], a[60:63], v[24:27]\n"
"ds_read_b64_tr_b16 a[234:235], v186 offset:20480\n"
"v_mfma_f32_16x16x32_bf16 v[16:19], a[252:255], a[56:59], v[16:19]\n"
"v_mfma_f32_16x16x32_bf16 v[16:19], a[248:251], a[40:43], v[16:19]\n"
"v_mfma_f32_16x16x32_bf16 v[8:11], a[248:251], a[36:39], v[8:11]\n"
"ds_read_b64_tr_b16 a[236:237], v186 offset:24576\n"
"s_add_u32 s64, s64, 458752\n"
"v_mfma_f32_16x16x32_bf16 v[8:11], a[252:255], a[52:55], v[8:11]\n"
"ds_read_b64_tr_b16 a[238:239], v186 offset:28672\n"
"s_add_u32 m0, %15, 49152\n"
"v_mfma_f32_16x16x32_bf16 v[0:3], a[252:255], a[48:51], v[0:3]\n"
"buffer_load_dwordx4 v209, %5, s64 offen offset:0 lds\n"
"v_mfma_f32_16x16x32_bf16 v[0:3], a[248:251], a[32:35], v[0:3]\n"
"s_waitcnt lgkmcnt(4)\n"
"v_mfma_f32_16x16x32_bf16 v[4:7], a[224:227], a[32:35], v[4:7]\n"
"ds_read_b64_tr_b16 a[240:241], v187 offset:16384\n"
"v_mfma_f32_16x16x32_bf16 v[4:7], a[228:231], a[48:51], v[4:7]\n"
"ds_read_b64_tr_b16 a[242:243], v187 offset:20480\n"
"v_mfma_f32_16x16x32_bf16 v[12:15], a[228:231], a[52:55], v[12:15]\n"
"buffer_load_dwordx4 v210, %5, s64 offen offset:1024 lds\n"
"v_mfma_f32_16x16x32_bf16 v[12:15], a[224:227], a[36:39], v[12:15]\n"
"v_mfma_f32_16x16x32_bf16 v[20:23], a[224:227], a[40:43], v[20:23]\n"
"ds_read_b64_tr_b16 a[244:245], v187 offset:24576\n"
"v_mfma_f32_16x16x32_bf16 v[20:23], a[228:231], a[56:59], v[20:23]\n"
"ds_read_b64_tr_b16 a[246:247], v187 offset:28672\n"
"v_mfma_f32_16x16x32_bf16 v[28:31], a[228:231], a[60:63], v[28:31]\n"
"buffer_load_dwordx4 v211, %5, s64 offen offset:2048 lds\n"
"v_mfma_f32_16x16x32_bf16 v[28:31], a[224:227], a[44:47], v[28:31]\n"
"s_waitcnt lgkmcnt(4)\n"
"v_mfma_f32_16x16x32_bf16 v[56:59], a[232:235], a[44:47], v[56:59]\n"
"ds_read_b64_tr_b16 a[248:249], v188 offset:16384\n"
"v_mfma_f32_16x16x32_bf16 v[56:59], a[236:239], a[60:63], v[56:59]\n"
"ds_read_b64_tr_b16 a[250:251], v188 offset:20480\n"
"v_mfma_f32_16x16x32_bf16 v[48:51], a[236:239], a[56:59], v[48:51]\n"
"buffer_load_dwordx4 v212, %5, s64 offen offset:3072 lds\n"
"v_mfma_f32_16x16x32_bf16 v[48:51], a[232:235], a[40:43], v[48:51]\n"
"v_mfma_f32_16x16x32_bf16 v[40:43], a[232:235], a[36:39], v[40:43]\n"
"ds_read_b64_tr_b16 a[252:253], v188 offset:24576\n"
"v_mfma_f32_16x16x32_bf16 v[40:43], a[236:239], a[52:55], v[40:43]\n"
"ds_read_b64_tr_b16 a[254:255], v188 offset:28672\n"
"s_add_u32 s65, s65, 4096\n"
"v_mfma_f32_16x16x32_bf16 v[32:35], a[236:239], a[48:51], v[32:35]\n"
"buffer_load_dwordx4 a[0:3], v207, %4, s65 offen offset:0 sc1\n"
"v_mfma_f32_16x16x32_bf16 v[32:35], a[232:235], a[32:35], v[32:35]\n"
"s_waitcnt lgkmcnt(4)\n"
"v_mfma_f32_16x16x32_bf16 v[36:39], a[240:243], a[32:35], v[36:39]\n"
"ds_read_b64_tr_b16 a[224:225], v189 offset:16384\n"
"v_mfma_f32_16x16x32_bf16 v[36:39], a[244:247], a[48:51], v[36:39]\n"
"ds_read_b64_tr_b16 a[226:227], v189 offset:20480\n"
"v_mfma_f32_16x16x32_bf16 v[44:47], a[244:247], a[52:55], v[44:47]\n"
"buffer_load_dwordx4 a[4:7], v207, %4, s65 offen offset:1024 sc1\n"
"v_mfma_f32_16x16x32_bf16 v[44:47], a[240:243], a[36:39], v[44:47]\n"
"v_mfma_f32_16x16x32_bf16 v[52:55], a[240:243], a[40:43], v[52:55]\n"
"ds_read_b64_tr_b16 a[228:229], v189 offset:24576\n"
"v_mfma_f32_16x16x32_bf16 v[52:55], a[244:247], a[56:59], v[52:55]\n"
"ds_read_b64_tr_b16 a[230:231], v189 offset:28672\n"
"v_mfma_f32_16x16x32_bf16 v[60:63], a[244:247], a[60:63], v[60:63]\n"
"buffer_load_dwordx4 a[8:11], v208, %4, s65 offen offset:0 sc1\n"
"v_mfma_f32_16x16x32_bf16 v[60:63], a[240:243], a[44:47], v[60:63]\n"
"s_waitcnt lgkmcnt(4)\n"
"v_mfma_f32_16x16x32_bf16 v[88:91], a[248:251], a[44:47], v[88:91]\n"
"ds_read_b64_tr_b16 a[232:233], v190 offset:16384\n"
"v_mfma_f32_16x16x32_bf16 v[88:91], a[252:255], a[60:63], v[88:91]\n"
"ds_read_b64_tr_b16 a[234:235], v190 offset:20480\n"
"v_mfma_f32_16x16x32_bf16 v[80:83], a[252:255], a[56:59], v[80:83]\n"
"buffer_load_dwordx4 a[12:15], v208, %4, s65 offen offset:1024 sc1\n"
"v_mfma_f32_16x16x32_bf16 v[80:83], a[248:251], a[40:43], v[80:83]\n"
"v_mfma_f32_16x16x32_bf16 v[72:75], a[248:251], a[36:39], v[72:75]\n"
"ds_read_b64_tr_b16 a[236:237], v190 offset:24576\n"
"v_mfma_f32_16x16x32_bf16 v[72:75], a[252:255], a[52:55], v[72:75]\n"
"ds_read_b64_tr_b16 a[238:239], v190 offset:28672\n"
"v_mfma_f32_16x16x32_bf16 v[64:67], a[252:255], a[48:51], v[64:67]\n"
"buffer_load_dwordx4 a[16:19], v207, %4, s65 offen offset:2048 sc1\n"
"v_mfma_f32_16x16x32_bf16 v[64:67], a[248:251], a[32:35], v[64:67]\n"
"s_waitcnt lgkmcnt(4)\n"
"v_mfma_f32_16x16x32_bf16 v[68:71], a[224:227], a[32:35], v[68:71]\n"
"s_waitcnt vmcnt(17)\n"
"s_barrier\n"
"ds_read_b64_tr_b16 a[240:241], v184 offset:32768\n"
"v_mfma_f32_16x16x32_bf16 v[68:71], a[228:231], a[48:51], v[68:71]\n"
"ds_read_b64_tr_b16 a[242:243], v184 offset:36864\n"
"v_mfma_f32_16x16x32_bf16 v[76:79], a[228:231], a[52:55], v[76:79]\n"
"buffer_load_dwordx4 a[20:23], v207, %4, s65 offen offset:3072 sc1\n"
"v_mfma_f32_16x16x32_bf16 v[76:79], a[224:227], a[36:39], v[76:79]\n"
"v_mfma_f32_16x16x32_bf16 v[84:87], a[224:227], a[40:43], v[84:87]\n"
"ds_read_b64_tr_b16 a[244:245], v184 offset:40960\n"
"v_mfma_f32_16x16x32_bf16 v[84:87], a[228:231], a[56:59], v[84:87]\n"
"ds_read_b64_tr_b16 a[246:247], v184 offset:45056\n"
"v_mfma_f32_16x16x32_bf16 v[92:95], a[228:231], a[60:63], v[92:95]\n"
"buffer_load_dwordx4 a[24:27], v208, %4, s65 offen offset:2048 sc1\n"
"v_mfma_f32_16x16x32_bf16 v[92:95], a[224:227], a[44:47], v[92:95]\n"
"s_waitcnt lgkmcnt(4)\n"
"v_mfma_f32_16x16x32_bf16 v[108:111], a[232:235], a[44:47], v[108:111]\n"
"ds_read_b64_tr_b16 a[248:249], v185 offset:32768\n"
"v_mfma_f32_16x16x32_bf16 v[108:111], a[236:239], a[60:63], v[108:111]\n"
"ds_read_b64_tr_b16 a[250:251], v185 offset:36864\n"
"v_mfma_f32_16x16x32_bf16 v[104:107], a[236:239], a[56:59], v[104:107]\n"
"buffer_load_dwordx4 a[28:31], v208, %4, s65 offen offset:3072 sc1\n"
"v_mfma_f32_16x16x32_bf16 v[104:107], a[232:235], a[40:43], v[104:107]\n"
"s_sub_u32 s66, s66, 1\n"
"v_mfma_f32_16x16x32_bf16 v[100:103], a[232:235], a[36:39], v[100:103]\n"
"ds_read_b64_tr_b16 a[252:253], v185 offset:40960\n"
"v_mfma_f32_16x16x32_bf16 v[100:103], a[236:239], a[52:55], v[100:103]\n"
"ds_read_b64_tr_b16 a[254:255], v185 offset:45056\n"
"v_mfma_f32_16x16x32_bf16 v[96:99], a[236:239], a[48:51], v[96:99]\n"
"v_mfma_f32_16x16x32_bf16 v[96:99], a[232:235], a[32:35], v[96:99]\n"
"s_cmp_lg_u32 s66, 0\n"
"s_cbranch_scc1 LOOP_E%=\n"
"s_waitcnt vmcnt(24) lgkmcnt(4)\n"
"v_mfma_f32_16x16x32_bf16 v[0:3], a[240:243], a[64:67], v[0:3]\n"
"ds_read_b64_tr_b16 a[224:225], v186 offset:32768\n"
"v_mfma_f32_16x16x32_bf16 v[0:3], a[244:247], a[80:83], v[0:3]\n"
"ds_read_b64_tr_b16 a[226:227], v186 offset:36864\n"
"v_mfma_f32_16x16x32_bf16 v[8:11], a[244:247], a[84:87], v[8:11]\n"
"v_mfma_f32_16x16x32_bf16 v[8:11], a[240:243], a[68:71], v[8:11]\n"
"v_mfma_f32_16x16x32_bf16 v[16:19], a[240:243], a[72:75], v[16:19]\n"
"ds_read_b64_tr_b16 a[228:229], v186 offset:40960\n"
"s_add_u32 s64, s64, 458752\n"
"v_mfma_f32_16x16x32_bf16 v[16:19], a[244:247], a[88:91], v[16:19]\n"
"ds_read_b64_tr_b16 a[230:231], v186 offset:45056\n"
"s_add_u32 m0, %15, 0\n"
"v_mfma_f32_16x16x32_bf16 v[24:27], a[244:247], a[92:95], v[24:27]\n"
"buffer_load_dwordx4 v209, %5, s64 offen offset:0 lds\n"
"v_mfma_f32_16x16x32_bf16 v[24:27], a[240:243], a[76:79], v[24:27]\n"
"s_waitcnt lgkmcnt(4)\n"
"v_mfma_f32_16x16x32_bf16 v[28:31], a[248:251], a[76:79], v[28:31]\n"
"ds_read_b64_tr_b16 a[232:233], v187 offset:32768\n"
"v_mfma_f32_16x16x32_bf16 v[28:31], a[252:255], a[92:95], v[28:31]\n"
"ds_read_b64_tr_b16 a[234:235], v187 offset:36864\n"
"v_mfma_f32_16x16x32_bf16 v[20:23], a[252:255], a[88:91], v[20:23]\n"
"buffer_load_dwordx4 v210, %5, s64 offen offset:1024 lds\n"
"v_mfma_f32_16x16x32_bf16 v[20:23], a[248:251], a[72:75], v[20:23]\n"
"v_mfma_f32_16x16x32_bf16 v[12:15], a[248:251], a[68:71], v[12:15]\n"
"ds_read_b64_tr_b16 a[236:237], v187 offset:40960\n"
"v_mfma_f32_16x16x32_bf16 v[12:15], a[252:255], a[84:87], v[12:15]\n"
"ds_read_b64_tr_b16 a[238:239], v187 offset:45056\n"
"v_mfma_f32_16x16x32_bf16 v[4:7], a[252:255], a[80:83], v[4:7]\n"
"buffer_load_dwordx4 v211, %5, s64 offen offset:2048 lds\n"
"v_mfma_f32_16x16x32_bf16 v[4:7], a[248:251], a[64:67], v[4:7]\n"
"s_waitcnt lgkmcnt(4)\n"
"v_mfma_f32_16x16x32_bf16 v[32:35], a[224:227], a[64:67], v[32:35]\n"
"ds_read_b64_tr_b16 a[240:241], v188 offset:32768\n"
"v_mfma_f32_16x16x32_bf16 v[32:35], a[228:231], a[80:83], v[32:35]\n"
"ds_read_b64_tr_b16 a[242:243], v188 offset:36864\n"
"v_mfma_f32_16x16x32_bf16 v[40:43], a[228:231], a[84:87], v[40:43]\n"
"buffer_load_dwordx4 v212, %5, s64 offen offset:3072 lds\n"
"v_mfma_f32_16x16x32_bf16 v[40:43], a[224:227], a[68:71], v[40:43]\n"
"v_mfma_f32_16x16x32_bf16 v[48:51], a[224:227], a[72:75], v[48:51]\n"
"ds_read_b64_tr_b16 a[244:245], v188 offset:40960\n"
"v_mfma_f32_16x16x32_bf16 v[48:51], a[228:231], a[88:91], v[48:51]\n"
"ds_read_b64_tr_b16 a[246:247], v188 offset:45056\n"
"s_add_u32 s65, s65, 4096\n"
"v_mfma_f32_16x16x32_bf16 v[56:59], a[228:231], a[92:95], v[56:59]\n"
"buffer_load_dwordx4 a[32:35], v207, %4, s65 offen offset:0 sc1\n"
"v_mfma_f32_16x16x32_bf16 v[56:59], a[224:227], a[76:79], v[56:59]\n"
"s_waitcnt lgkmcnt(4)\n"
"v_mfma_f32_16x16x32_bf16 v[60:63], a[232:235], a[76:79], v[60:63]\n"
"ds_read_b64_tr_b16 a[248:249], v189 offset:32768\n"
"v_mfma_f32_16x16x32_bf16 v[60:63], a[236:239], a[92:95], v[60:63]\n"
"ds_read_b64_tr_b16 a[250:251], v189 offset:36864\n"
"v_mfma_f32_16x16x32_bf16 v[52:55], a[236:239], a[88:91], v[52:55]\n"
"buffer_load_dwordx4 a[36:39], v207, %4, s65 offen offset:1024 sc1\n"
"v_mfma_f32_16x16x32_bf16 v[52:55], a[232:235], a[72:75], v[52:55]\n"
"v_mfma_f32_16x16x32_bf16 v[44:47], a[232:235], a[68:71], v[44:47]\n"
"ds_read_b64_tr_b16 a[252:253], v189 offset:40960\n"
"v_mfma_f32_16x16x32_bf16 v[44:47], a[236:239], a[84:87], v[44:47]\n"
"ds_read_b64_tr_b16 a[254:255], v189 offset:45056\n"
"v_mfma_f32_16x16x32_bf16 v[36:39], a[236:239], a[80:83], v[36:39]\n"
"buffer_load_dwordx4 a[40:43], v208, %4, s65 offen offset:0 sc1\n"
"v_mfma_f32_16x16x32_bf16 v[36:39], a[232:235], a[64:67], v[36:39]\n"
"s_waitcnt lgkmcnt(4)\n"
"v_mfma_f32_16x16x32_bf16 v[64:67], a[240:243], a[64:67], v[64:67]\n"
"ds_read_b64_tr_b16 a[224:225], v190 offset:32768\n"
"v_mfma_f32_16x16x32_bf16 v[64:67], a[244:247], a[80:83], v[64:67]\n"
"ds_read_b64_tr_b16 a[226:227], v190 offset:36864\n"
"v_mfma_f32_16x16x32_bf16 v[72:75], a[244:247], a[84:87], v[72:75]\n"
"buffer_load_dwordx4 a[44:47], v208, %4, s65 offen offset:1024 sc1\n"
"v_mfma_f32_16x16x32_bf16 v[72:75], a[240:243], a[68:71], v[72:75]\n"
"v_mfma_f32_16x16x32_bf16 v[80:83], a[240:243], a[72:75], v[80:83]\n"
"ds_read_b64_tr_b16 a[228:229], v190 offset:40960\n"
"v_mfma_f32_16x16x32_bf16 v[80:83], a[244:247], a[88:91], v[80:83]\n"
"ds_read_b64_tr_b16 a[230:231], v190 offset:45056\n"
"v_mfma_f32_16x16x32_bf16 v[88:91], a[244:247], a[92:95], v[88:91]\n"
"buffer_load_dwordx4 a[48:51], v207, %4, s65 offen offset:2048 sc1\n"
"v_mfma_f32_16x16x32_bf16 v[88:91], a[240:243], a[76:79], v[88:91]\n"
"s_waitcnt lgkmcnt(4)\n"
"v_mfma_f32_16x16x32_bf16 v[92:95], a[248:251], a[76:79], v[92:95]\n"
"s_waitcnt vmcnt(17)\n"
"s_barrier\n"
"ds_read_b64_tr_b16 a[232:233], v184 offset:49152\n"
"v_mfma_f32_16x16x32_bf16 v[92:95], a[252:255], a[92:95], v[92:95]\n"
"ds_read_b64_tr_b16 a[234:235], v184 offset:53248\n"
"v_mfma_f32_16x16x32_bf16 v[84:87], a[252:255], a[88:91], v[84:87]\n"
"buffer_load_dwordx4 a[52:55], v207, %4, s65 offen offset:3072 sc1\n"
"v_mfma_f32_16x16x32_bf16 v[84:87], a[248:251], a[72:75], v[84:87]\n"
"v_mfma_f32_16x16x32_bf16 v[76:79], a[248:251], a[68:71], v[76:79]\n"
"ds_read_b64_tr_b16 a[236:237], v184 offset:57344\n"
"v_mfma_f32_16x16x32_bf16 v[76:79], a[252:255], a[84:87], v[76:79]\n"
"ds_read_b64_tr_b16 a[238:239], v184 offset:61440\n"
"v_mfma_f32_16x16x32_bf16 v[68:71], a[252:255], a[80:83], v[68:71]\n"
"buffer_load_dwordx4 a[56:59], v208, %4, s65 offen offset:2048 sc1\n"
"v_mfma_f32_16x16x32_bf16 v[68:71], a[248:251], a[64:67], v[68:71]\n"
"s_waitcnt lgkmcnt(4)\n"
"v_mfma_f32_16x16x32_bf16 v[96:99], a[224:227], a[64:67], v[96:99]\n"
"ds_read_b64_tr_b16 a[240:241], v185 offset:49152\n"
"v_mfma_f32_16x16x32_bf16 v[96:99], a[228:231], a[80:83], v[96:99]\n"
"ds_read_b64_tr_b16 a[242:243], v185 offset:53248\n"
"v_mfma_f32_16x16x32_bf16 v[100:103], a[228:231], a[84:87], v[100:103]\n"
"buffer_load_dwordx4 a[60:63], v208, %4, s65 offen offset:3072 sc1\n"
"v_mfma_f32_16x16x32_bf16 v[100:103], a[224:227], a[68:71], v[100:103]\n"
"v_mfma_f32_16x16x32_bf16 v[104:107], a[224:227], a[72:75], v[104:107]\n"
"ds_read_b64_tr_b16 a[244:245], v185 offset:57344\n"
"v_mfma_f32_16x16x32_bf16 v[104:107], a[228:231], a[88:91], v[104:107]\n"
"ds_read_b64_tr_b16 a[246:247], v185 offset:61440\n"
"v_mfma_f32_16x16x32_bf16 v[108:111], a[228:231], a[92:95], v[108:111]\n"
"v_mfma_f32_16x16x32_bf16 v[108:111], a[224:227], a[76:79], v[108:111]\n"
"s_waitcnt vmcnt(24) lgkmcnt(4)\n"
"v_mfma_f32_16x16x32_bf16 v[24:27], a[232:235], a[108:111], v[24:27]\n"
"ds_read_b64_tr_b16 a[248:249], v186 offset:49152\n"
"v_mfma_f32_16x16x32_bf16 v[24:27], a[236:239], a[124:127], v[24:27]\n"
"ds_read_b64_tr_b16 a[250:251], v186 offset:53248\n"
"v_mfma_f32_16x16x32_bf16 v[16:19], a[236:239], a[120:123], v[16:19]\n"
"v_mfma_f32_16x16x32_bf16 v[16:19], a[232:235], a[104:107], v[16:19]\n"
"v_mfma_f32_16x16x32_bf16 v[8:11], a[232:235], a[100:103], v[8:11]\n"
"ds_read_b64_tr_b16 a[252:253], v186 offset:57344\n"
"s_add_u32 s64, s64, 458752\n"
"v_mfma_f32_16x16x32_bf16 v[8:11], a[236:239], a[116:119], v[8:11]\n"
"ds_read_b64_tr_b16 a[254:255], v186 offset:61440\n"
"s_add_u32 m0, %15, 16384\n"
"v_mfma_f32_16x16x32_bf16 v[0:3], a[236:239], a[112:115], v[0:3]\n"
"buffer_load_dwordx4 v209, %5, s64 offen offset:0 lds\n"
"v_mfma_f32_16x16x32_bf16 v[0:3], a[232:235], a[96:99], v[0:3]\n"
"s_waitcnt lgkmcnt(4)\n"
"v_mfma_f32_16x16x32_bf16 v[4:7], a[240:243], a[96:99], v[4:7]\n"
"ds_read_b64_tr_b16 a[224:225], v187 offset:49152\n"
"v_mfma_f32_16x16x32_bf16 v[4:7], a[244:247], a[112:115], v[4:7]\n"
"ds_read_b64_tr_b16 a[226:227], v187 offset:53248\n"
"v_mfma_f32_16x16x32_bf16 v[12:15], a[244:247], a[116:119], v[12:15]\n"
"buffer_load_dwordx4 v210, %5, s64 offen offset:1024 lds\n"
"v_mfma_f32_16x16x32_bf16 v[12:15], a[240:243], a[100:103], v[12:15]\n"
"v_mfma_f32_16x16x32_bf16 v[20:23], a[240:243], a[104:107], v[20:23]\n"
"ds_read_b64_tr_b16 a[228:229], v187 offset:57344\n"
"v_mfma_f32_16x16x32_bf16 v[20:23], a[244:247], a[120:123], v[20:23]\n"
"ds_read_b64_tr_b16 a[230:231], v187 offset:61440\n"
"v_mfma_f32_16x16x32_bf16 v[28:31], a[244:247], a[124:127], v[28:31]\n"
"buffer_load_dwordx4 v211, %5, s64 offen offset:2048 lds\n"
"v_mfma_f32_16x16x32_bf16 v[28:31], a[240:243], a[108:111], v[28:31]\n"
"s_waitcnt lgkmcnt(4)\n"
"v_mfma_f32_16x16x32_bf16 v[56:59], a[248:251], a[108:111], v[56:59]\n"
"ds_read_b64_tr_b16 a[232:233], v188 offset:49152\n"
"v_mfma_f32_16x16x32_bf16 v[56:59], a[252:255], a[124:127], v[56:59]\n"
"ds_read_b64_tr_b16 a[234:235], v188 offset:53248\n"
"v_mfma_f32_16x16x32_bf16 v[48:51], a[252:255], a[120:123], v[48:51]\n"
"buffer_load_dwordx4 v212, %5, s64 offen offset:3072 lds\n"
"v_mfma_f32_16x16x32_bf16 v[48:51], a[248:251], a[104:107], v[48:51]\n"
"v_mfma_f32_16x16x32_bf16 v[40:43], a[248:251], a[100:103], v[40:43]\n"
"ds_read_b64_tr_b16 a[236:237], v188 offset:57344\n"
"v_mfma_f32_16x16x32_bf16 v[40:43], a[252:255], a[116:119], v[40:43]\n"
"ds_read_b64_tr_b16 a[238:239], v188 offset:61440\n"
"s_add_u32 s65, s65, 4096\n"
"v_mfma_f32_16x16x32_bf16 v[32:35], a[252:255], a[112:115], v[32:35]\n"
"buffer_load_dwordx4 a[64:67], v207, %4, s65 offen offset:0 sc1\n"
"v_mfma_f32_16x16x32_bf16 v[32:35], a[248:251], a[96:99], v[32:35]\n"
"s_waitcnt lgkmcnt(4)\n"
"v_mfma_f32_16x16x32_bf16 v[36:39], a[224:227], a[96:99], v[36:39]\n"
"ds_read_b64_tr_b16 a[240:241], v189 offset:49152\n"
"v_mfma_f32_16x16x32_bf16 v[36:39], a[228:231], a[112:115], v[36:39]\n"
"ds_read_b64_tr_b16 a[242:243], v189 offset:53248\n"
"v_mfma_f32_16x16x32_bf16 v[44:47], a[228:231], a[116:119], v[44:47]\n"
"buffer_load_dwordx4 a[68:71], v207, %4, s65 offen offset:1024 sc1\n"
"v_mfma_f32_16x16x32_bf16 v[44:47], a[224:227], a[100:103], v[44:47]\n"
"v_mfma_f32_16x16x32_bf16 v[52:55], a[224:227], a[104:107], v[52:55]\n"
"ds_read_b64_tr_b16 a[244:245], v189 offset:57344\n"
"v_mfma_f32_16x16x32_bf16 v[52:55], a[228:231], a[120:123], v[52:55]\n"
"ds_read_b64_tr_b16 a[246:247], v189 offset:61440\n"
"v_mfma_f32_16x16x32_bf16 v[60:63], a[228:231], a[124:127], v[60:63]\n"
"buffer_load_dwordx4 a[72:75], v208, %4, s65 offen offset:0 sc1\n"
"v_mfma_f32_16x16x32_bf16 v[60:63], a[224:227], a[108:111], v[60:63]\n"
"s_waitcnt lgkmcnt(4)\n"
"v_mfma_f32_16x16x32_bf16 v[88:91], a[232:235], a[108:111], v[88:91]\n"
"ds_read_b64_tr_b16 a[248:249], v190 offset:49152\n"
"v_mfma_f32_16x16x32_bf16 v[88:91], a[236:239], a[124:127], v[88:91]\n"
"ds_read_b64_tr_b16 a[250:251], v190 offset:53248\n"
"v_mfma_f32_16x16x32_bf16 v[80:83], a[236:239], a[120:123], v[80:83]\n"
"buffer_load_dwordx4 a[76:79], v208, %4, s65 offen offset:1024 sc1\n"
"v_mfma_f32_16x16x32_bf16 v[80:83], a[232:235], a[104:107], v[80:83]\n"
"v_mfma_f32_16x16x32_bf16 v[72:75], a[232:235], a[100:103], v[72:75]\n"
"ds_read_b64_tr_b16 a[252:253], v190 offset:57344\n"
"v_mfma_f32_16x16x32_bf16 v[72:75], a[236:239], a[116:119], v[72:75]\n"
"ds_read_b64_tr_b16 a[254:255], v190 offset:61440\n"
"v_mfma_f32_16x16x32_bf16 v[64:67], a[236:239], a[112:115], v[64:67]\n"
"buffer_load_dwordx4 a[80:83], v207, %4, s65 offen offset:2048 sc1\n"
"v_mfma_f32_16x16x32_bf16 v[64:67], a[232:235], a[96:99], v[64:67]\n"
"s_waitcnt lgkmcnt(4)\n"
"v_mfma_f32_16x16x32_bf16 v[68:71], a[240:243], a[96:99], v[68:71]\n"
"s_waitcnt vmcnt(17)\n"
"s_barrier\n"
"ds_read_b64_tr_b16 a[224:225], v184 offset:0\n"
"v_mfma_f32_16x16x32_bf16 v[68:71], a[244:247], a[112:115], v[68:71]\n"
"ds_read_b64_tr_b16 a[226:227], v184 offset:4096\n"
"v_mfma_f32_16x16x32_bf16 v[76:79], a[244:247], a[116:119], v[76:79]\n"
"buffer_load_dwordx4 a[84:87], v207, %4, s65 offen offset:3072 sc1\n"
"v_mfma_f32_16x16x32_bf16 v[76:79], a[240:243], a[100:103], v[76:79]\n"
"v_mfma_f32_16x16x32_bf16 v[84:87], a[240:243], a[104:107], v[84:87]\n"
"ds_read_b64_tr_b16 a[228:229], v184 offset:8192\n"
"v_mfma_f32_16x16x32_bf16 v[84:87], a[244:247], a[120:123], v[84:87]\n"
"ds_read_b64_tr_b16 a[230:231], v184 offset:12288\n"
"v_mfma_f32_16x16x32_bf16 v[92:95], a[244:247], a[124:127], v[92:95]\n"
"buffer_load_dwordx4 a[88:91], v208, %4, s65 offen offset:2048 sc1\n"
"v_mfma_f32_16x16x32_bf16 v[92:95], a[240:243], a[108:111], v[92:95]\n"
"s_waitcnt lgkmcnt(4)\n"
"v_mfma_f32_16x16x32_bf16 v[108:111], a[248:251], a[108:111], v[108:111]\n"
"ds_read_b64_tr_b16 a[232:233], v185 offset:0\n"
"v_mfma_f32_16x16x32_bf16 v[108:111], a[252:255], a[124:127], v[108:111]\n"
"ds_read_b64_tr_b16 a[234:235], v185 offset:4096\n"
"v_mfma_f32_16x16x32_bf16 v[104:107], a[252:255], a[120:123], v[104:107]\n"
"buffer_load_dwordx4 a[92:95], v208, %4, s65 offen offset:3072 sc1\n"
"v_mfma_f32_16x16x32_bf16 v[104:107], a[248:251], a[104:107], v[104:107]\n"
"v_mfma_f32_16x16x32_bf16 v[100:103], a[248:251], a[100:103], v[100:103]\n"
"ds_read_b64_tr_b16 a[236:237], v185 offset:8192\n"
"v_mfma_f32_16x16x32_bf16 v[100:103], a[252:255], a[116:119], v[100:103]\n"
"ds_read_b64_tr_b16 a[238:239], v185 offset:12288\n"
"v_mfma_f32_16x16x32_bf16 v[96:99], a[252:255], a[112:115], v[96:99]\n"
"v_mfma_f32_16x16x32_bf16 v[96:99], a[248:251], a[96:99], v[96:99]\n"
"s_waitcnt vmcnt(24) lgkmcnt(4)\n"
"v_mfma_f32_16x16x32_bf16 v[0:3], a[224:227], a[0:3], v[0:3]\n"
"ds_read_b64_tr_b16 a[240:241], v186 offset:0\n"
"v_mfma_f32_16x16x32_bf16 v[0:3], a[228:231], a[16:19], v[0:3]\n"
"ds_read_b64_tr_b16 a[242:243], v186 offset:4096\n"
"v_mfma_f32_16x16x32_bf16 v[8:11], a[228:231], a[20:23], v[8:11]\n"
"v_mfma_f32_16x16x32_bf16 v[8:11], a[224:227], a[4:7], v[8:11]\n"
"v_mfma_f32_16x16x32_bf16 v[16:19], a[224:227], a[8:11], v[16:19]\n"
"ds_read_b64_tr_b16 a[244:245], v186 offset:8192\n"
"s_add_u32 s64, s64, 458752\n"
"v_mfma_f32_16x16x32_bf16 v[16:19], a[228:231], a[24:27], v[16:19]\n"
"ds_read_b64_tr_b16 a[246:247], v186 offset:12288\n"
"s_add_u32 m0, %15, 32768\n"
"v_mfma_f32_16x16x32_bf16 v[24:27], a[228:231], a[28:31], v[24:27]\n"
"buffer_load_dwordx4 v209, %5, s64 offen offset:0 lds\n"
"v_mfma_f32_16x16x32_bf16 v[24:27], a[224:227], a[12:15], v[24:27]\n"
"s_waitcnt lgkmcnt(4)\n"
"v_mfma_f32_16x16x32_bf16 v[28:31], a[232:235], a[12:15], v[28:31]\n"
"ds_read_b64_tr_b16 a[248:249], v187 offset:0\n"
"v_mfma_f32_16x16x32_bf16 v[28:31], a[236:239], a[28:31], v[28:31]\n"
"ds_read_b64_tr_b16 a[250:251], v187 offset:4096\n"
"v_mfma_f32_16x16x32_bf16 v[20:23], a[236:239], a[24:27], v[20:23]\n"
"buffer_load_dwordx4 v210, %5, s64 offen offset:1024 lds\n"
"v_mfma_f32_16x16x32_bf16 v[20:23], a[232:235], a[8:11], v[20:23]\n"
"v_mfma_f32_16x16x32_bf16 v[12:15], a[232:235], a[4:7], v[12:15]\n"
"ds_read_b64_tr_b16 a[252:253], v187 offset:8192\n"
"v_mfma_f32_16x16x32_bf16 v[12:15], a[236:239], a[20:23], v[12:15]\n"
"ds_read_b64_tr_b16 a[254:255], v187 offset:12288\n"
"v_mfma_f32_16x16x32_bf16 v[4:7], a[236:239], a[16:19], v[4:7]\n"
"buffer_load_dwordx4 v211, %5, s64 offen offset:2048 lds\n"
"v_mfma_f32_16x16x32_bf16 v[4:7], a[232:235], a[0:3], v[4:7]\n"
"s_waitcnt lgkmcnt(4)\n"
"v_mfma_f32_16x16x32_bf16 v[32:35], a[240:243], a[0:3], v[32:35]\n"
"ds_read_b64_tr_b16 a[224:225], v188 offset:0\n"
"v_mfma_f32_16x16x32_bf16 v[32:35], a[244:247], a[16:19], v[32:35]\n"
"ds_read_b64_tr_b16 a[226:227], v188 offset:4096\n"
"v_mfma_f32_16x16x32_bf16 v[40:43], a[244:247], a[20:23], v[40:43]\n"
"buffer_load_dwordx4 v212, %5, s64 offen offset:3072 lds\n"
"v_mfma_f32_16x16x32_bf16 v[40:43], a[240:243], a[4:7], v[40:43]\n"
"v_mfma_f32_16x16x32_bf16 v[48:51], a[240:243], a[8:11], v[48:51]\n"
"ds_read_b64_tr_b16 a[228:229], v188 offset:8192\n"
"v_mfma_f32_16x16x32_bf16 v[48:51], a[244:247], a[24:27], v[48:51]\n"
"ds_read_b64_tr_b16 a[230:231], v188 offset:12288\n"
"s_add_u32 s65, s65, 4096\n"
"v_mfma_f32_16x16x32_bf16 v[56:59], a[244:247], a[28:31], v[56:59]\n"
"buffer_load_dwordx4 a[96:99], v207, %4, s65 offen offset:0 sc1\n"
"v_mfma_f32_16x16x32_bf16 v[56:59], a[240:243], a[12:15], v[56:59]\n"
"s_waitcnt lgkmcnt(4)\n"
"v_mfma_f32_16x16x32_bf16 v[60:63], a[248:251], a[12:15], v[60:63]\n"
"ds_read_b64_tr_b16 a[232:233], v189 offset:0\n"
"v_mfma_f32_16x16x32_bf16 v[60:63], a[252:255], a[28:31], v[60:63]\n"
"ds_read_b64_tr_b16 a[234:235], v189 offset:4096\n"
"v_mfma_f32_16x16x32_bf16 v[52:55], a[252:255], a[24:27], v[52:55]\n"
"buffer_load_dwordx4 a[100:103], v207, %4, s65 offen offset:1024 sc1\n"
"v_mfma_f32_16x16x32_bf16 v[52:55], a[248:251], a[8:11], v[52:55]\n"
"v_mfma_f32_16x16x32_bf16 v[44:47], a[248:251], a[4:7], v[44:47]\n"
"ds_read_b64_tr_b16 a[236:237], v189 offset:8192\n"
"v_mfma_f32_16x16x32_bf16 v[44:47], a[252:255], a[20:23], v[44:47]\n"
"ds_read_b64_tr_b16 a[238:239], v189 offset:12288\n"
"v_mfma_f32_16x16x32_bf16 v[36:39], a[252:255], a[16:19], v[36:39]\n"
"buffer_load_dwordx4 a[104:107], v208, %4, s65 offen offset:0 sc1\n"
"v_mfma_f32_16x16x32_bf16 v[36:39], a[248:251], a[0:3], v[36:39]\n"
"s_waitcnt lgkmcnt(4)\n"
"v_mfma_f32_16x16x32_bf16 v[64:67], a[224:227], a[0:3], v[64:67]\n"
"ds_read_b64_tr_b16 a[240:241], v190 offset:0\n"
"v_mfma_f32_16x16x32_bf16 v[64:67], a[228:231], a[16:19], v[64:67]\n"
"ds_read_b64_tr_b16 a[242:243], v190 offset:4096\n"
"v_mfma_f32_16x16x32_bf16 v[72:75], a[228:231], a[20:23], v[72:75]\n"
"buffer_load_dwordx4 a[108:111], v208, %4, s65 offen offset:1024 sc1\n"
"v_mfma_f32_16x16x32_bf16 v[72:75], a[224:227], a[4:7], v[72:75]\n"
"v_mfma_f32_16x16x32_bf16 v[80:83], a[224:227], a[8:11], v[80:83]\n"
"ds_read_b64_tr_b16 a[244:245], v190 offset:8192\n"
"v_mfma_f32_16x16x32_bf16 v[80:83], a[228:231], a[24:27], v[80:83]\n"
"ds_read_b64_tr_b16 a[246:247], v190 offset:12288\n"
"v_mfma_f32_16x16x32_bf16 v[88:91], a[228:231], a[28:31], v[88:91]\n"
"buffer_load_dwordx4 a[112:115], v207, %4, s65 offen offset:2048 sc1\n"
"v_mfma_f32_16x16x32_bf16 v[88:91], a[224:227], a[12:15], v[88:91]\n"
"s_waitcnt lgkmcnt(4)\n"
"v_mfma_f32_16x16x32_bf16 v[92:95], a[232:235], a[12:15], v[92:95]\n"
"s_waitcnt vmcnt(17)\n"
"s_barrier\n"
"ds_read_b64_tr_b16 a[248:249], v184 offset:16384\n"
"v_mfma_f32_16x16x32_bf16 v[92:95], a[236:239], a[28:31], v[92:95]\n"
"ds_read_b64_tr_b16 a[250:251], v184 offset:20480\n"
"v_mfma_f32_16x16x32_bf16 v[84:87], a[236:239], a[24:27], v[84:87]\n"
"buffer_load_dwordx4 a[116:119], v207, %4, s65 offen offset:3072 sc1\n"
"v_mfma_f32_16x16x32_bf16 v[84:87], a[232:235], a[8:11], v[84:87]\n"
"v_mfma_f32_16x16x32_bf16 v[76:79], a[232:235], a[4:7], v[76:79]\n"
"ds_read_b64_tr_b16 a[252:253], v184 offset:24576\n"
"v_mfma_f32_16x16x32_bf16 v[76:79], a[236:239], a[20:23], v[76:79]\n"
"ds_read_b64_tr_b16 a[254:255], v184 offset:28672\n"
"v_mfma_f32_16x16x32_bf16 v[68:71], a[236:239], a[16:19], v[68:71]\n"
"buffer_load_dwordx4 a[120:123], v208, %4, s65 offen offset:2048 sc1\n"
"v_mfma_f32_16x16x32_bf16 v[68:71], a[232:235], a[0:3], v[68:71]\n"
"s_waitcnt lgkmcnt(4)\n"
"v_mfma_f32_16x16x32_bf16 v[96:99], a[240:243], a[0:3], v[96:99]\n"
"ds_read_b64_tr_b16 a[224:225], v185 offset:16384\n"
"v_mfma_f32_16x16x32_bf16 v[96:99], a[244:247], a[16:19], v[96:99]\n"
"ds_read_b64_tr_b16 a[226:227], v185 offset:20480\n"
"v_mfma_f32_16x16x32_bf16 v[100:103], a[244:247], a[20:23], v[100:103]\n"
"buffer_load_dwordx4 a[124:127], v208, %4, s65 offen offset:3072 sc1\n"
"v_mfma_f32_16x16x32_bf16 v[100:103], a[240:243], a[4:7], v[100:103]\n"
"v_mfma_f32_16x16x32_bf16 v[104:107], a[240:243], a[8:11], v[104:107]\n"
"ds_read_b64_tr_b16 a[228:229], v185 offset:24576\n"
"v_mfma_f32_16x16x32_bf16 v[104:107], a[244:247], a[24:27], v[104:107]\n"
"ds_read_b64_tr_b16 a[230:231], v185 offset:28672\n"
"v_mfma_f32_16x16x32_bf16 v[108:111], a[244:247], a[28:31], v[108:111]\n"
"v_mfma_f32_16x16x32_bf16 v[108:111], a[240:243], a[12:15], v[108:111]\n"
"s_waitcnt vmcnt(24) lgkmcnt(4)\n"
"v_mfma_f32_16x16x32_bf16 v[24:27], a[248:251], a[44:47], v[24:27]\n"
"ds_read_b64_tr_b16 a[232:233], v186 offset:16384\n"
"v_mfma_f32_16x16x32_bf16 v[24:27], a[252:255], a[60:63], v[24:27]\n"
"ds_read_b64_tr_b16 a[234:235], v186 offset:20480\n"
"v_mfma_f32_16x16x32_bf16 v[16:19], a[252:255], a[56:59], v[16:19]\n"
"v_mfma_f32_16x16x32_bf16 v[16:19], a[248:251], a[40:43], v[16:19]\n"
"v_mfma_f32_16x16x32_bf16 v[8:11], a[248:251], a[36:39], v[8:11]\n"
"ds_read_b64_tr_b16 a[236:237], v186 offset:24576\n"
"s_add_u32 s64, s64, 458752\n"
"v_mfma_f32_16x16x32_bf16 v[8:11], a[252:255], a[52:55], v[8:11]\n"
"ds_read_b64_tr_b16 a[238:239], v186 offset:28672\n"
"s_add_u32 m0, %15, 49152\n"
"v_mfma_f32_16x16x32_bf16 v[0:3], a[252:255], a[48:51], v[0:3]\n"
"buffer_load_dwordx4 v209, %5, s64 offen offset:0 lds\n"
"v_mfma_f32_16x16x32_bf16 v[0:3], a[248:251], a[32:35], v[0:3]\n"
"s_waitcnt lgkmcnt(4)\n"
"v_mfma_f32_16x16x32_bf16 v[4:7], a[224:227], a[32:35], v[4:7]\n"
"ds_read_b64_tr_b16 a[240:241], v187 offset:16384\n"
"v_mfma_f32_16x16x32_bf16 v[4:7], a[228:231], a[48:51], v[4:7]\n"
"ds_read_b64_tr_b16 a[242:243], v187 offset:20480\n"
"v_mfma_f32_16x16x32_bf16 v[12:15], a[228:231], a[52:55], v[12:15]\n"
"v_mfma_f32_16x16x32_bf16 v[12:15], a[224:227], a[36:39], v[12:15]\n"
"v_mfma_f32_16x16x32_bf16 v[20:23], a[224:227], a[40:43], v[20:23]\n"
"ds_read_b64_tr_b16 a[244:245], v187 offset:24576\n"
"v_mfma_f32_16x16x32_bf16 v[20:23], a[228:231], a[56:59], v[20:23]\n"
"ds_read_b64_tr_b16 a[246:247], v187 offset:28672\n"
"v_mfma_f32_16x16x32_bf16 v[28:31], a[228:231], a[60:63], v[28:31]\n"
"v_mfma_f32_16x16x32_bf16 v[28:31], a[224:227], a[44:47], v[28:31]\n"
"s_waitcnt lgkmcnt(4)\n"
"v_mfma_f32_16x16x32_bf16 v[56:59], a[232:235], a[44:47], v[56:59]\n"
"ds_read_b64_tr_b16 a[248:249], v188 offset:16384\n"
"v_mfma_f32_16x16x32_bf16 v[56:59], a[236:239], a[60:63], v[56:59]\n"
"ds_read_b64_tr_b16 a[250:251], v188 offset:20480\n"
"v_mfma_f32_16x16x32_bf16 v[48:51], a[236:239], a[56:59], v[48:51]\n"
"buffer_load_dwordx4 v210, %5, s64 offen offset:1024 lds\n"
"v_mfma_f32_16x16x32_bf16 v[48:51], a[232:235], a[40:43], v[48:51]\n"
"v_mfma_f32_16x16x32_bf16 v[40:43], a[232:235], a[36:39], v[40:43]\n"
"ds_read_b64_tr_b16 a[252:253], v188 offset:24576\n"
"v_mfma_f32_16x16x32_bf16 v[40:43], a[236:239], a[52:55], v[40:43]\n"
"ds_read_b64_tr_b16 a[254:255], v188 offset:28672\n"
"v_mfma_f32_16x16x32_bf16 v[32:35], a[236:239], a[48:51], v[32:35]\n"
"v_mfma_f32_16x16x32_bf16 v[32:35], a[232:235], a[32:35], v[32:35]\n"
"s_waitcnt lgkmcnt(4)\n"
"v_mfma_f32_16x16x32_bf16 v[36:39], a[240:243], a[32:35], v[36:39]\n"
"ds_read_b64_tr_b16 a[224:225], v189 offset:16384\n"
"v_mfma_f32_16x16x32_bf16 v[36:39], a[244:247], a[48:51], v[36:39]\n"
"ds_read_b64_tr_b16 a[226:227], v189 offset:20480\n"
"v_mfma_f32_16x16x32_bf16 v[44:47], a[244:247], a[52:55], v[44:47]\n"
"v_mfma_f32_16x16x32_bf16 v[44:47], a[240:243], a[36:39], v[44:47]\n"
"s_lshr_b32 s74, %15, 13\n"
"v_mfma_f32_16x16x32_bf16 v[52:55], a[240:243], a[40:43], v[52:55]\n"
"ds_read_b64_tr_b16 a[228:229], v189 offset:24576\n"
"v_mfma_f32_16x16x32_bf16 v[52:55], a[244:247], a[56:59], v[52:55]\n"
"ds_read_b64_tr_b16 a[230:231], v189 offset:28672\n"
"v_mfma_f32_16x16x32_bf16 v[60:63], a[244:247], a[60:63], v[60:63]\n"
"buffer_load_dwordx4 v211, %5, s64 offen offset:2048 lds\n"
"v_mfma_f32_16x16x32_bf16 v[60:63], a[240:243], a[44:47], v[60:63]\n"
"v_readlane_b32 s69, %20, s74\n"
"s_waitcnt lgkmcnt(4)\n"
"v_mfma_f32_16x16x32_bf16 v[88:91], a[248:251], a[44:47], v[88:91]\n"
"ds_read_b64_tr_b16 a[232:233], v190 offset:16384\n"
"v_mfma_f32_16x16x32_bf16 v[88:91], a[252:255], a[60:63], v[88:91]\n"
"ds_read_b64_tr_b16 a[234:235], v190 offset:20480\n"
"v_mfma_f32_16x16x32_bf16 v[80:83], a[252:255], a[56:59], v[80:83]\n"
"v_mfma_f32_16x16x32_bf16 v[80:83], a[248:251], a[40:43], v[80:83]\n"
"s_and_b32 s75, %15, 4096\n"
"v_mfma_f32_16x16x32_bf16 v[72:75], a[248:251], a[36:39], v[72:75]\n"
"ds_read_b64_tr_b16 a[236:237], v190 offset:24576\n"
"v_mfma_f32_16x16x32_bf16 v[72:75], a[252:255], a[52:55], v[72:75]\n"
"ds_read_b64_tr_b16 a[238:239], v190 offset:28672\n"
"v_mfma_f32_16x16x32_bf16 v[64:67], a[252:255], a[48:51], v[64:67]\n"
"v_mfma_f32_16x16x32_bf16 v[64:67], a[248:251], a[32:35], v[64:67]\n"
"s_lshl_b32 s75, s75, 3\n"
"s_waitcnt lgkmcnt(4)\n"
"v_mfma_f32_16x16x32_bf16 v[68:71], a[224:227], a[32:35], v[68:71]\n"
"s_waitcnt vmcnt(11)\n"
"s_barrier\n"
"ds_read_b64_tr_b16 a[240:241], v184 offset:32768\n"
"v_mfma_f32_16x16x32_bf16 v[68:71], a[228:231], a[48:51], v[68:71]\n"
"ds_read_b64_tr_b16 a[242:243], v184 offset:36864\n"
"v_mfma_f32_16x16x32_bf16 v[76:79], a[228:231], a[52:55], v[76:79]\n"
"buffer_load_dwordx4 v212, %5, s64 offen offset:3072 lds\n"
"v_mfma_f32_16x16x32_bf16 v[76:79], a[224:227], a[36:39], v[76:79]\n"
"v_mfma_f32_16x16x32_bf16 v[84:87], a[224:227], a[40:43], v[84:87]\n"
"ds_read_b64_tr_b16 a[244:245], v184 offset:40960\n"
"v_mfma_f32_16x16x32_bf16 v[84:87], a[228:231], a[56:59], v[84:87]\n"
"ds_read_b64_tr_b16 a[246:247], v184 offset:45056\n"
"v_mfma_f32_16x16x32_bf16 v[92:95], a[228:231], a[60:63], v[92:95]\n"
"v_mfma_f32_16x16x32_bf16 v[92:95], a[224:227], a[44:47], v[92:95]\n"
"s_waitcnt lgkmcnt(4)\n"
"v_mfma_f32_16x16x32_bf16 v[108:111], a[232:235], a[44:47], v[108:111]\n"
"ds_read_b64_tr_b16 a[248:249], v185 offset:32768\n"
"v_mfma_f32_16x16x32_bf16 v[108:111], a[236:239], a[60:63], v[108:111]\n"
"ds_read_b64_tr_b16 a[250:251], v185 offset:36864\n"
"v_mfma_f32_16x16x32_bf16 v[104:107], a[236:239], a[56:59], v[104:107]\n"
"v_mfma_f32_16x16x32_bf16 v[104:107], a[232:235], a[40:43], v[104:107]\n"
"v_mfma_f32_16x16x32_bf16 v[100:103], a[232:235], a[36:39], v[100:103]\n"
"ds_read_b64_tr_b16 a[252:253], v185 offset:40960\n"
"v_mfma_f32_16x16x32_bf16 v[100:103], a[236:239], a[52:55], v[100:103]\n"
"ds_read_b64_tr_b16 a[254:255], v185 offset:45056\n"
"v_mfma_f32_16x16x32_bf16 v[96:99], a[236:239], a[48:51], v[96:99]\n"
"v_mfma_f32_16x16x32_bf16 v[96:99], a[232:235], a[32:35], v[96:99]\n"
"s_waitcnt vmcnt(16) lgkmcnt(4)\n"
"v_mfma_f32_16x16x32_bf16 v[0:3], a[240:243], a[64:67], v[0:3]\n"
"ds_read_b64_tr_b16 a[224:225], v186 offset:32768\n"
"v_mfma_f32_16x16x32_bf16 v[0:3], a[244:247], a[80:83], v[0:3]\n"
"ds_read_b64_tr_b16 a[226:227], v186 offset:36864\n"
"v_mfma_f32_16x16x32_bf16 v[8:11], a[244:247], a[84:87], v[8:11]\n"
"s_lshl_b32 s73, s69, 16\n"
"s_add_u32 s73, s73, s75\n"
"v_mfma_f32_16x16x32_bf16 v[8:11], a[240:243], a[68:71], v[8:11]\n"
"v_mfma_f32_16x16x32_bf16 v[16:19], a[240:243], a[72:75], v[16:19]\n"
"ds_read_b64_tr_b16 a[228:229], v186 offset:40960\n"
"v_mfma_f32_16x16x32_bf16 v[16:19], a[244:247], a[88:91], v[16:19]\n"
"ds_read_b64_tr_b16 a[230:231], v186 offset:45056\n"
"v_mfma_f32_16x16x32_bf16 v[24:27], a[244:247], a[92:95], v[24:27]\n"
"s_add_u32 s74, s74, 2\n"
"v_readlane_b32 s69, %20, s74\n"
"v_mfma_f32_16x16x32_bf16 v[24:27], a[240:243], a[76:79], v[24:27]\n"
"s_add_u32 m0, %15, 65536\n"
"s_waitcnt lgkmcnt(4)\n"
"v_mfma_f32_16x16x32_bf16 v[28:31], a[248:251], a[76:79], v[28:31]\n"
"ds_read_b64_tr_b16 a[232:233], v187 offset:32768\n"
"v_mfma_f32_16x16x32_bf16 v[28:31], a[252:255], a[92:95], v[28:31]\n"
"ds_read_b64_tr_b16 a[234:235], v187 offset:36864\n"
"v_mfma_f32_16x16x32_bf16 v[20:23], a[252:255], a[88:91], v[20:23]\n"
"buffer_load_dwordx4 v224, %9, s73 offen offset:0 lds\n"
"v_mfma_f32_16x16x32_bf16 v[20:23], a[248:251], a[72:75], v[20:23]\n"
"v_mfma_f32_16x16x32_bf16 v[12:15], a[248:251], a[68:71], v[12:15]\n"
"ds_read_b64_tr_b16 a[236:237], v187 offset:40960\n"
"v_mfma_f32_16x16x32_bf16 v[12:15], a[252:255], a[84:87], v[12:15]\n"
"ds_read_b64_tr_b16 a[238:239], v187 offset:45056\n"
"v_mfma_f32_16x16x32_bf16 v[4:7], a[252:255], a[80:83], v[4:7]\n"
"buffer_load_dwordx4 v225, %9, s73 offen offset:1024 lds\n"
"v_mfma_f32_16x16x32_bf16 v[4:7], a[248:251], a[64:67], v[4:7]\n"
"s_waitcnt lgkmcnt(4)\n"
"v_mfma_f32_16x16x32_bf16 v[32:35], a[224:227], a[64:67], v[32:35]\n"
"ds_read_b64_tr_b16 a[240:241], v188 offset:32768\n"
"v_mfma_f32_16x16x32_bf16 v[32:35], a[228:231], a[80:83], v[32:35]\n"
"ds_read_b64_tr_b16 a[242:243], v188 offset:36864\n"
"v_mfma_f32_16x16x32_bf16 v[40:43], a[228:231], a[84:87], v[40:43]\n"
"buffer_load_dwordx4 v226, %9, s73 offen offset:2048 lds\n"
"v_mfma_f32_16x16x32_bf16 v[40:43], a[224:227], a[68:71], v[40:43]\n"
"v_mfma_f32_16x16x32_bf16 v[48:51], a[224:227], a[72:75], v[48:51]\n"
"ds_read_b64_tr_b16 a[244:245], v188 offset:40960\n"
"v_mfma_f32_16x16x32_bf16 v[48:51], a[228:231], a[88:91], v[48:51]\n"
"ds_read_b64_tr_b16 a[246:247], v188 offset:45056\n"
"v_mfma_f32_16x16x32_bf16 v[56:59], a[228:231], a[92:95], v[56:59]\n"
"buffer_load_dwordx4 v227, %9, s73 offen offset:3072 lds\n"
"v_mfma_f32_16x16x32_bf16 v[56:59], a[224:227], a[76:79], v[56:59]\n"
"s_waitcnt lgkmcnt(4)\n"
"v_mfma_f32_16x16x32_bf16 v[60:63], a[232:235], a[76:79], v[60:63]\n"
"ds_read_b64_tr_b16 a[248:249], v189 offset:32768\n"
"v_mfma_f32_16x16x32_bf16 v[60:63], a[236:239], a[92:95], v[60:63]\n"
"ds_read_b64_tr_b16 a[250:251], v189 offset:36864\n"
"v_mfma_f32_16x16x32_bf16 v[52:55], a[236:239], a[88:91], v[52:55]\n"
"s_lshl_b32 s73, s69, 16\n"
"s_add_u32 s73, s73, s75\n"
"v_mfma_f32_16x16x32_bf16 v[52:55], a[232:235], a[72:75], v[52:55]\n"
"v_mfma_f32_16x16x32_bf16 v[44:47], a[232:235], a[68:71], v[44:47]\n"
"ds_read_b64_tr_b16 a[252:253], v189 offset:40960\n"
"v_mfma_f32_16x16x32_bf16 v[44:47], a[236:239], a[84:87], v[44:47]\n"
"ds_read_b64_tr_b16 a[254:255], v189 offset:45056\n"
"v_mfma_f32_16x16x32_bf16 v[36:39], a[236:239], a[80:83], v[36:39]\n"
"s_add_u32 s74, s74, 2\n"
"v_readlane_b32 s69, %20, s74\n"
"v_mfma_f32_16x16x32_bf16 v[36:39], a[232:235], a[64:67], v[36:39]\n"
"s_add_u32 m0, %15, 81920\n"
"s_waitcnt lgkmcnt(4)\n"
"v_mfma_f32_16x16x32_bf16 v[64:67], a[240:243], a[64:67], v[64:67]\n"
"ds_read_b64_tr_b16 a[224:225], v190 offset:32768\n"
"v_mfma_f32_16x16x32_bf16 v[64:67], a[244:247], a[80:83], v[64:67]\n"
"ds_read_b64_tr_b16 a[226:227], v190 offset:36864\n"
"v_mfma_f32_16x16x32_bf16 v[72:75], a[244:247], a[84:87], v[72:75]\n"
"buffer_load_dwordx4 v224, %9, s73 offen offset:0 lds\n"
"v_mfma_f32_16x16x32_bf16 v[72:75], a[240:243], a[68:71], v[72:75]\n"
"v_mfma_f32_16x16x32_bf16 v[80:83], a[240:243], a[72:75], v[80:83]\n"
"ds_read_b64_tr_b16 a[228:229], v190 offset:40960\n"
"v_mfma_f32_16x16x32_bf16 v[80:83], a[244:247], a[88:91], v[80:83]\n"
"ds_read_b64_tr_b16 a[230:231], v190 offset:45056\n"
"v_mfma_f32_16x16x32_bf16 v[88:91], a[244:247], a[92:95], v[88:91]\n"
"buffer_load_dwordx4 v225, %9, s73 offen offset:1024 lds\n"
"v_mfma_f32_16x16x32_bf16 v[88:91], a[240:243], a[76:79], v[88:91]\n"
"s_waitcnt lgkmcnt(4)\n"
"v_mfma_f32_16x16x32_bf16 v[92:95], a[248:251], a[76:79], v[92:95]\n"
"s_waitcnt vmcnt(6)\n"
"s_barrier\n"
"ds_read_b64_tr_b16 a[232:233], v184 offset:49152\n"
"v_mfma_f32_16x16x32_bf16 v[92:95], a[252:255], a[92:95], v[92:95]\n"
"ds_read_b64_tr_b16 a[234:235], v184 offset:53248\n"
"v_mfma_f32_16x16x32_bf16 v[84:87], a[252:255], a[88:91], v[84:87]\n"
"buffer_load_dwordx4 v226, %9, s73 offen offset:2048 lds\n"
"v_mfma_f32_16x16x32_bf16 v[84:87], a[248:251], a[72:75], v[84:87]\n"
"v_mfma_f32_16x16x32_bf16 v[76:79], a[248:251], a[68:71], v[76:79]\n"
"ds_read_b64_tr_b16 a[236:237], v184 offset:57344\n"
"v_mfma_f32_16x16x32_bf16 v[76:79], a[252:255], a[84:87], v[76:79]\n"
"ds_read_b64_tr_b16 a[238:239], v184 offset:61440\n"
"v_mfma_f32_16x16x32_bf16 v[68:71], a[252:255], a[80:83], v[68:71]\n"
"buffer_load_dwordx4 v227, %9, s73 offen offset:3072 lds\n"
"v_mfma_f32_16x16x32_bf16 v[68:71], a[248:251], a[64:67], v[68:71]\n"
"s_waitcnt lgkmcnt(4)\n"
"v_mfma_f32_16x16x32_bf16 v[96:99], a[224:227], a[64:67], v[96:99]\n"
"ds_read_b64_tr_b16 a[240:241], v185 offset:49152\n"
"v_mfma_f32_16x16x32_bf16 v[96:99], a[228:231], a[80:83], v[96:99]\n"
"ds_read_b64_tr_b16 a[242:243], v185 offset:53248\n"
"v_mfma_f32_16x16x32_bf16 v[100:103], a[228:231], a[84:87], v[100:103]\n"
"v_mfma_f32_16x16x32_bf16 v[100:103], a[224:227], a[68:71], v[100:103]\n"
"v_mfma_f32_16x16x32_bf16 v[104:107], a[224:227], a[72:75], v[104:107]\n"
"ds_read_b64_tr_b16 a[244:245], v185 offset:57344\n"
"v_mfma_f32_16x16x32_bf16 v[104:107], a[228:231], a[88:91], v[104:107]\n"
"ds_read_b64_tr_b16 a[246:247], v185 offset:61440\n"
"v_mfma_f32_16x16x32_bf16 v[108:111], a[228:231], a[92:95], v[108:111]\n"
"v_mfma_f32_16x16x32_bf16 v[108:111], a[224:227], a[76:79], v[108:111]\n"
"s_waitcnt vmcnt(12) lgkmcnt(4)\n"
"v_mfma_f32_16x16x32_bf16 v[24:27], a[232:235], a[108:111], v[24:27]\n"
"ds_read_b64_tr_b16 a[248:249], v186 offset:49152\n"
"v_mfma_f32_16x16x32_bf16 v[24:27], a[236:239], a[124:127], v[24:27]\n"
"ds_read_b64_tr_b16 a[250:251], v186 offset:53248\n"
"v_mfma_f32_16x16x32_bf16 v[16:19], a[236:239], a[120:123], v[16:19]\n"
"s_lshl_b32 s73, s69, 16\n"
"s_add_u32 s73, s73, s75\n"
"v_mfma_f32_16x16x32_bf16 v[16:19], a[232:235], a[104:107], v[16:19]\n"
"v_mfma_f32_16x16x32_bf16 v[8:11], a[232:235], a[100:103], v[8:11]\n"
"ds_read_b64_tr_b16 a[252:253], v186 offset:57344\n"
"v_mfma_f32_16x16x32_bf16 v[8:11], a[236:239], a[116:119], v[8:11]\n"
"ds_read_b64_tr_b16 a[254:255], v186 offset:61440\n"
"v_mfma_f32_16x16x32_bf16 v[0:3], a[236:239], a[112:115], v[0:3]\n"
"s_add_u32 s74, s74, 2\n"
"v_readlane_b32 s69, %20, s74\n"
"v_mfma_f32_16x16x32_bf16 v[0:3], a[232:235], a[96:99], v[0:3]\n"
"s_add_u32 m0, %15, 98304\n"
"s_waitcnt lgkmcnt(4)\n"
"v_mfma_f32_16x16x32_bf16 v[4:7], a[240:243], a[96:99], v[4:7]\n"
"ds_read_b64_tr_b16 a[224:225], v187 offset:49152\n"
"v_mfma_f32_16x16x32_bf16 v[4:7], a[244:247], a[112:115], v[4:7]\n"
"ds_read_b64_tr_b16 a[226:227], v187 offset:53248\n"
"v_mfma_f32_16x16x32_bf16 v[12:15], a[244:247], a[116:119], v[12:15]\n"
"buffer_load_dwordx4 v224, %9, s73 offen offset:0 lds\n"
"v_mfma_f32_16x16x32_bf16 v[12:15], a[240:243], a[100:103], v[12:15]\n"
"v_mfma_f32_16x16x32_bf16 v[20:23], a[240:243], a[104:107], v[20:23]\n"
"ds_read_b64_tr_b16 a[228:229], v187 offset:57344\n"
"v_mfma_f32_16x16x32_bf16 v[20:23], a[244:247], a[120:123], v[20:23]\n"
"ds_read_b64_tr_b16 a[230:231], v187 offset:61440\n"
"v_mfma_f32_16x16x32_bf16 v[28:31], a[244:247], a[124:127], v[28:31]\n"
"buffer_load_dwordx4 v225, %9, s73 offen offset:1024 lds\n"
"v_mfma_f32_16x16x32_bf16 v[28:31], a[240:243], a[108:111], v[28:31]\n"
"s_waitcnt lgkmcnt(4)\n"
"v_mfma_f32_16x16x32_bf16 v[56:59], a[248:251], a[108:111], v[56:59]\n"
"ds_read_b64_tr_b16 a[232:233], v188 offset:49152\n"
"v_mfma_f32_16x16x32_bf16 v[56:59], a[252:255], a[124:127], v[56:59]\n"
"ds_read_b64_tr_b16 a[234:235], v188 offset:53248\n"
"v_mfma_f32_16x16x32_bf16 v[48:51], a[252:255], a[120:123], v[48:51]\n"
"buffer_load_dwordx4 v226, %9, s73 offen offset:2048 lds\n"
"v_mfma_f32_16x16x32_bf16 v[48:51], a[248:251], a[104:107], v[48:51]\n"
"v_mfma_f32_16x16x32_bf16 v[40:43], a[248:251], a[100:103], v[40:43]\n"
"ds_read_b64_tr_b16 a[236:237], v188 offset:57344\n"
"v_mfma_f32_16x16x32_bf16 v[40:43], a[252:255], a[116:119], v[40:43]\n"
"ds_read_b64_tr_b16 a[238:239], v188 offset:61440\n"
"v_mfma_f32_16x16x32_bf16 v[32:35], a[252:255], a[112:115], v[32:35]\n"
"buffer_load_dwordx4 v227, %9, s73 offen offset:3072 lds\n"
"v_mfma_f32_16x16x32_bf16 v[32:35], a[248:251], a[96:99], v[32:35]\n"
"s_waitcnt lgkmcnt(4)\n"
"v_mfma_f32_16x16x32_bf16 v[36:39], a[224:227], a[96:99], v[36:39]\n"
"ds_read_b64_tr_b16 a[240:241], v189 offset:49152\n"
"v_mfma_f32_16x16x32_bf16 v[36:39], a[228:231], a[112:115], v[36:39]\n"
"ds_read_b64_tr_b16 a[242:243], v189 offset:53248\n"
"v_mfma_f32_16x16x32_bf16 v[44:47], a[228:231], a[116:119], v[44:47]\n"
"s_lshl_b32 s73, s69, 16\n"
"s_add_u32 s73, s73, s75\n"
"v_mfma_f32_16x16x32_bf16 v[44:47], a[224:227], a[100:103], v[44:47]\n"
"v_mfma_f32_16x16x32_bf16 v[52:55], a[224:227], a[104:107], v[52:55]\n"
"ds_read_b64_tr_b16 a[244:245], v189 offset:57344\n"
"v_mfma_f32_16x16x32_bf16 v[52:55], a[228:231], a[120:123], v[52:55]\n"
"ds_read_b64_tr_b16 a[246:247], v189 offset:61440\n"
"v_mfma_f32_16x16x32_bf16 v[60:63], a[228:231], a[124:127], v[60:63]\n"
"s_add_u32 s74, s74, 2\n"
"v_readlane_b32 s69, %20, s74\n"
"v_mfma_f32_16x16x32_bf16 v[60:63], a[224:227], a[108:111], v[60:63]\n"
"s_add_u32 m0, %15, 114688\n"
"s_waitcnt lgkmcnt(4)\n"
"v_mfma_f32_16x16x32_bf16 v[88:91], a[232:235], a[108:111], v[88:91]\n"
"ds_read_b64_tr_b16 a[248:249], v190 offset:49152\n"
"v_mfma_f32_16x16x32_bf16 v[88:91], a[236:239], a[124:127], v[88:91]\n"
"ds_read_b64_tr_b16 a[250:251], v190 offset:53248\n"
"v_mfma_f32_16x16x32_bf16 v[80:83], a[236:239], a[120:123], v[80:83]\n"
"buffer_load_dwordx4 v224, %9, s73 offen offset:0 lds\n"
"v_mfma_f32_16x16x32_bf16 v[80:83], a[232:235], a[104:107], v[80:83]\n"
"v_mfma_f32_16x16x32_bf16 v[72:75], a[232:235], a[100:103], v[72:75]\n"
"ds_read_b64_tr_b16 a[252:253], v190 offset:57344\n"
"v_mfma_f32_16x16x32_bf16 v[72:75], a[236:239], a[116:119], v[72:75]\n"
"ds_read_b64_tr_b16 a[254:255], v190 offset:61440\n"
"v_mfma_f32_16x16x32_bf16 v[64:67], a[236:239], a[112:115], v[64:67]\n"
"buffer_load_dwordx4 v225, %9, s73 offen offset:1024 lds\n"
"v_mfma_f32_16x16x32_bf16 v[64:67], a[232:235], a[96:99], v[64:67]\n"
"s_waitcnt lgkmcnt(4)\n"
"v_mfma_f32_16x16x32_bf16 v[68:71], a[240:243], a[96:99], v[68:71]\n"
"v_mfma_f32_16x16x32_bf16 v[68:71], a[244:247], a[112:115], v[68:71]\n"
"v_mfma_f32_16x16x32_bf16 v[76:79], a[244:247], a[116:119], v[76:79]\n"
"buffer_load_dwordx4 v226, %9, s73 offen offset:2048 lds\n"
"v_mfma_f32_16x16x32_bf16 v[76:79], a[240:243], a[100:103], v[76:79]\n"
"v_mfma_f32_16x16x32_bf16 v[84:87], a[240:243], a[104:107], v[84:87]\n"
"v_mfma_f32_16x16x32_bf16 v[84:87], a[244:247], a[120:123], v[84:87]\n"
"v_mfma_f32_16x16x32_bf16 v[92:95], a[244:247], a[124:127], v[92:95]\n"
"buffer_load_dwordx4 v227, %9, s73 offen offset:3072 lds\n"
"v_mfma_f32_16x16x32_bf16 v[92:95], a[240:243], a[108:111], v[92:95]\n"
"s_waitcnt lgkmcnt(0)\n"
"v_mfma_f32_16x16x32_bf16 v[108:111], a[248:251], a[108:111], v[108:111]\n"
"v_mfma_f32_16x16x32_bf16 v[108:111], a[252:255], a[124:127], v[108:111]\n"
"v_mfma_f32_16x16x32_bf16 v[104:107], a[252:255], a[120:123], v[104:107]\n"
"v_mfma_f32_16x16x32_bf16 v[104:107], a[248:251], a[104:107], v[104:107]\n"
"v_mfma_f32_16x16x32_bf16 v[100:103], a[248:251], a[100:103], v[100:103]\n"
"v_mfma_f32_16x16x32_bf16 v[100:103], a[252:255], a[116:119], v[100:103]\n"
"v_mfma_f32_16x16x32_bf16 v[96:99], a[252:255], a[112:115], v[96:99]\n"
"v_mfma_f32_16x16x32_bf16 v[96:99], a[248:251], a[96:99], v[96:99]\n"
"s_lshl_b32 s70, %16, 11\n"
"s_add_u32 s71, s70, 4096\n"
"s_nop 3\n"
"v_pk_mul_f32 v[184:185], v[0:1], v[0:1]\n"
"v_pk_mul_f32 v[186:187], v[2:3], v[2:3]\n"
"v_pk_mul_f32 v[188:189], v[4:5], v[4:5]\n"
"v_pk_mul_f32 v[190:191], v[6:7], v[6:7]\n"
"v_pk_fma_f32 v[184:185], v[184:185], v[200:201], v[202:203]\n"
"v_pk_fma_f32 v[186:187], v[186:187], v[200:201], v[202:203]\n"
"v_pk_fma_f32 v[188:189], v[188:189], v[200:201], v[202:203]\n"
"v_pk_fma_f32 v[190:191], v[190:191], v[200:201], v[202:203]\n"
"v_pk_mul_f32 v[184:185], v[184:185], v[0:1]\n"
"v_pk_mul_f32 v[186:187], v[186:187], v[2:3]\n"
"v_pk_mul_f32 v[188:189], v[188:189], v[4:5]\n"
"v_pk_mul_f32 v[190:191], v[190:191], v[6:7]\n"
"v_exp_f32 v184, v184\n"
"v_exp_f32 v185, v185\n"
"v_exp_f32 v186, v186\n"
"v_exp_f32 v187, v187\n"
"v_exp_f32 v188, v188\n"
"v_exp_f32 v189, v189\n"
"v_exp_f32 v190, v190\n"
"v_exp_f32 v191, v191\n"
"v_pk_add_f32 v[184:185], v[184:185], v[204:205]\n"
"v_pk_add_f32 v[186:187], v[186:187], v[204:205]\n"
"v_pk_add_f32 v[188:189], v[188:189], v[204:205]\n"
"v_pk_add_f32 v[190:191], v[190:191], v[204:205]\n"
"v_rcp_f32 v184, v184\n"
"v_rcp_f32 v185, v185\n"
"v_rcp_f32 v186, v186\n"
"v_rcp_f32 v187, v187\n"
"v_rcp_f32 v188, v188\n"
"v_rcp_f32 v189, v189\n"
"v_rcp_f32 v190, v190\n"
"v_rcp_f32 v191, v191\n"
"v_pk_mul_f32 v[184:185], v[184:185], v[0:1]\n"
"v_pk_mul_f32 v[186:187], v[186:187], v[2:3]\n"
"v_pk_mul_f32 v[188:189], v[188:189], v[4:5]\n"
"v_pk_mul_f32 v[190:191], v[190:191], v[6:7]\n"
"v_cvt_pk_bf16_f32 v128, v184, v185\n"
"v_cvt_pk_bf16_f32 v129, v186, v187\n"
"v_cvt_pk_bf16_f32 v130, v188, v189\n"
"v_cvt_pk_bf16_f32 v131, v190, v191\n"
"buffer_store_dwordx4 v[128:131], v222, %7, s70 offen offset:0 sc1\n"
"v_pk_mul_f32 v[192:193], v[8:9], v[8:9]\n"
"v_pk_mul_f32 v[194:195], v[10:11], v[10:11]\n"
"v_pk_mul_f32 v[196:197], v[12:13], v[12:13]\n"
"v_pk_mul_f32 v[198:199], v[14:15], v[14:15]\n"
"v_pk_fma_f32 v[192:193], v[192:193], v[200:201], v[202:203]\n"
"v_pk_fma_f32 v[194:195], v[194:195], v[200:201], v[202:203]\n"
"v_pk_fma_f32 v[196:197], v[196:197], v[200:201], v[202:203]\n"
"v_pk_fma_f32 v[198:199], v[198:199], v[200:201], v[202:203]\n"
"v_pk_mul_f32 v[192:193], v[192:193], v[8:9]\n"
"v_pk_mul_f32 v[194:195], v[194:195], v[10:11]\n"
"v_pk_mul_f32 v[196:197], v[196:197], v[12:13]\n"
"v_pk_mul_f32 v[198:199], v[198:199], v[14:15]\n"
"v_exp_f32 v192, v192\n"
"v_exp_f32 v193, v193\n"
"v_exp_f32 v194, v194\n"
"v_exp_f32 v195, v195\n"
"v_exp_f32 v196, v196\n"
"v_exp_f32 v197, v197\n"
"v_exp_f32 v198, v198\n"
"v_exp_f32 v199, v199\n"
"v_pk_add_f32 v[192:193], v[192:193], v[204:205]\n"
"v_pk_add_f32 v[194:195], v[194:195], v[204:205]\n"
"v_pk_add_f32 v[196:197], v[196:197], v[204:205]\n"
"v_pk_add_f32 v[198:199], v[198:199], v[204:205]\n"
"v_rcp_f32 v192, v192\n"
"v_rcp_f32 v193, v193\n"
"v_rcp_f32 v194, v194\n"
"v_rcp_f32 v195, v195\n"
"v_rcp_f32 v196, v196\n"
"v_rcp_f32 v197, v197\n"
"v_rcp_f32 v198, v198\n"
"v_rcp_f32 v199, v199\n"
"v_pk_mul_f32 v[192:193], v[192:193], v[8:9]\n"
"v_pk_mul_f32 v[194:195], v[194:195], v[10:11]\n"
"v_pk_mul_f32 v[196:197], v[196:197], v[12:13]\n"
"v_pk_mul_f32 v[198:199], v[198:199], v[14:15]\n"
"v_cvt_pk_bf16_f32 v132, v192, v193\n"
"v_cvt_pk_bf16_f32 v133, v194, v195\n"
"v_cvt_pk_bf16_f32 v134, v196, v197\n"
"v_cvt_pk_bf16_f32 v135, v198, v199\n"
"buffer_store_dwordx4 v[132:135], v222, %7, s70 offen offset:1024 sc1\n"
"v_pk_mul_f32 v[184:185], v[16:17], v[16:17]\n"
"v_pk_mul_f32 v[186:187], v[18:19], v[18:19]\n"
"v_pk_mul_f32 v[188:189], v[20:21], v[20:21]\n"
"v_pk_mul_f32 v[190:191], v[22:23], v[22:23]\n"
"v_pk_fma_f32 v[184:185], v[184:185], v[200:201], v[202:203]\n"
"v_pk_fma_f32 v[186:187], v[186:187], v[200:201], v[202:203]\n"
"v_pk_fma_f32 v[188:189], v[188:189], v[200:201], v[202:203]\n"
"v_pk_fma_f32 v[190:191], v[190:191], v[200:201], v[202:203]\n"
"v_pk_mul_f32 v[184:185], v[184:185], v[16:17]\n"
"v_pk_mul_f32 v[186:187], v[186:187], v[18:19]\n"
"v_pk_mul_f32 v[188:189], v[188:189], v[20:21]\n"
"v_pk_mul_f32 v[190:191], v[190:191], v[22:23]\n"
"v_exp_f32 v184, v184\n"
"v_exp_f32 v185, v185\n"
"v_exp_f32 v186, v186\n"
"v_exp_f32 v187, v187\n"
"v_exp_f32 v188, v188\n"
"v_exp_f32 v189, v189\n"
"v_exp_f32 v190, v190\n"
"v_exp_f32 v191, v191\n"
"v_pk_add_f32 v[184:185], v[184:185], v[204:205]\n"
"v_pk_add_f32 v[186:187], v[186:187], v[204:205]\n"
"v_pk_add_f32 v[188:189], v[188:189], v[204:205]\n"
"v_pk_add_f32 v[190:191], v[190:191], v[204:205]\n"
"v_rcp_f32 v184, v184\n"
"v_rcp_f32 v185, v185\n"
"v_rcp_f32 v186, v186\n"
"v_rcp_f32 v187, v187\n"
"v_rcp_f32 v188, v188\n"
"v_rcp_f32 v189, v189\n"
"v_rcp_f32 v190, v190\n"
"v_rcp_f32 v191, v191\n"
"v_pk_mul_f32 v[184:185], v[184:185], v[16:17]\n"
"v_pk_mul_f32 v[186:187], v[186:187], v[18:19]\n"
"v_pk_mul_f32 v[188:189], v[188:189], v[20:21]\n"
"v_pk_mul_f32 v[190:191], v[190:191], v[22:23]\n"
"v_cvt_pk_bf16_f32 v136, v184, v185\n"
"v_cvt_pk_bf16_f32 v137, v186, v187\n"
"v_cvt_pk_bf16_f32 v138, v188, v189\n"
"v_cvt_pk_bf16_f32 v139, v190, v191\n"
"buffer_store_dwordx4 v[136:139], v223, %7, s70 offen offset:0 sc1\n"
"v_pk_mul_f32 v[192:193], v[24:25], v[24:25]\n"
"v_pk_mul_f32 v[194:195], v[26:27], v[26:27]\n"
"v_pk_mul_f32 v[196:197], v[28:29], v[28:29]\n"
"v_pk_mul_f32 v[198:199], v[30:31], v[30:31]\n"
"v_pk_fma_f32 v[192:193], v[192:193], v[200:201], v[202:203]\n"
"v_pk_fma_f32 v[194:195], v[194:195], v[200:201], v[202:203]\n"
"v_pk_fma_f32 v[196:197], v[196:197], v[200:201], v[202:203]\n"
"v_pk_fma_f32 v[198:199], v[198:199], v[200:201], v[202:203]\n"
"v_pk_mul_f32 v[192:193], v[192:193], v[24:25]\n"
"v_pk_mul_f32 v[194:195], v[194:195], v[26:27]\n"
"v_pk_mul_f32 v[196:197], v[196:197], v[28:29]\n"
"v_pk_mul_f32 v[198:199], v[198:199], v[30:31]\n"
"v_exp_f32 v192, v192\n"
"v_exp_f32 v193, v193\n"
"v_exp_f32 v194, v194\n"
"v_exp_f32 v195, v195\n"
"v_exp_f32 v196, v196\n"
"v_exp_f32 v197, v197\n"
"v_exp_f32 v198, v198\n"
"v_exp_f32 v199, v199\n"
"v_pk_add_f32 v[192:193], v[192:193], v[204:205]\n"
"v_pk_add_f32 v[194:195], v[194:195], v[204:205]\n"
"v_pk_add_f32 v[196:197], v[196:197], v[204:205]\n"
"v_pk_add_f32 v[198:199], v[198:199], v[204:205]\n"
"v_rcp_f32 v192, v192\n"
"v_rcp_f32 v193, v193\n"
"v_rcp_f32 v194, v194\n"
"v_rcp_f32 v195, v195\n"
"v_rcp_f32 v196, v196\n"
"v_rcp_f32 v197, v197\n"
"v_rcp_f32 v198, v198\n"
"v_rcp_f32 v199, v199\n"
"v_pk_mul_f32 v[192:193], v[192:193], v[24:25]\n"
"v_pk_mul_f32 v[194:195], v[194:195], v[26:27]\n"
"v_pk_mul_f32 v[196:197], v[196:197], v[28:29]\n"
"v_pk_mul_f32 v[198:199], v[198:199], v[30:31]\n"
"v_cvt_pk_bf16_f32 v140, v192, v193\n"
"v_cvt_pk_bf16_f32 v141, v194, v195\n"
"v_cvt_pk_bf16_f32 v142, v196, v197\n"
"v_cvt_pk_bf16_f32 v143, v198, v199\n"
"buffer_store_dwordx4 v[140:143], v223, %7, s70 offen offset:1024 sc1\n"
"v_pk_mul_f32 v[184:185], v[32:33], v[32:33]\n"
"v_pk_mul_f32 v[186:187], v[34:35], v[34:35]\n"
"v_pk_mul_f32 v[188:189], v[36:37], v[36:37]\n"
"v_pk_mul_f32 v[190:191], v[38:39], v[38:39]\n"
"v_pk_fma_f32 v[184:185], v[184:185], v[200:201], v[202:203]\n"
"v_pk_fma_f32 v[186:187], v[186:187], v[200:201], v[202:203]\n"
"v_pk_fma_f32 v[188:189], v[188:189], v[200:201], v[202:203]\n"
"v_pk_fma_f32 v[190:191], v[190:191], v[200:201], v[202:203]\n"
"v_pk_mul_f32 v[184:185], v[184:185], v[32:33]\n"
"v_pk_mul_f32 v[186:187], v[186:187], v[34:35]\n"
"v_pk_mul_f32 v[188:189], v[188:189], v[36:37]\n"
"v_pk_mul_f32 v[190:191], v[190:191], v[38:39]\n"
"v_exp_f32 v184, v184\n"
"v_exp_f32 v185, v185\n"
"v_exp_f32 v186, v186\n"
"v_exp_f32 v187, v187\n"
"v_exp_f32 v188, v188\n"
"v_exp_f32 v189, v189\n"
"v_exp_f32 v190, v190\n"
"v_exp_f32 v191, v191\n"
"v_pk_add_f32 v[184:185], v[184:185], v[204:205]\n"
"v_pk_add_f32 v[186:187], v[186:187], v[204:205]\n"
"v_pk_add_f32 v[188:189], v[188:189], v[204:205]\n"
"v_pk_add_f32 v[190:191], v[190:191], v[204:205]\n"
"v_rcp_f32 v184, v184\n"
"v_rcp_f32 v185, v185\n"
"v_rcp_f32 v186, v186\n"
"v_rcp_f32 v187, v187\n"
"v_rcp_f32 v188, v188\n"
"v_rcp_f32 v189, v189\n"
"v_rcp_f32 v190, v190\n"
"v_rcp_f32 v191, v191\n"
"v_pk_mul_f32 v[184:185], v[184:185], v[32:33]\n"
"v_pk_mul_f32 v[186:187], v[186:187], v[34:35]\n"
"v_pk_mul_f32 v[188:189], v[188:189], v[36:37]\n"
"v_pk_mul_f32 v[190:191], v[190:191], v[38:39]\n"
"v_cvt_pk_bf16_f32 v144, v184, v185\n"
"v_cvt_pk_bf16_f32 v145, v186, v187\n"
"v_cvt_pk_bf16_f32 v146, v188, v189\n"
"v_cvt_pk_bf16_f32 v147, v190, v191\n"
"buffer_store_dwordx4 v[144:147], v222, %7, s70 offen offset:2048 sc1\n"
"v_pk_mul_f32 v[192:193], v[40:41], v[40:41]\n"
"v_pk_mul_f32 v[194:195], v[42:43], v[42:43]\n"
"v_pk_mul_f32 v[196:197], v[44:45], v[44:45]\n"
"v_pk_mul_f32 v[198:199], v[46:47], v[46:47]\n"
"v_pk_fma_f32 v[192:193], v[192:193], v[200:201], v[202:203]\n"
"v_pk_fma_f32 v[194:195], v[194:195], v[200:201], v[202:203]\n"
"v_pk_fma_f32 v[196:197], v[196:197], v[200:201], v[202:203]\n"
"v_pk_fma_f32 v[198:199], v[198:199], v[200:201], v[202:203]\n"
"v_pk_mul_f32 v[192:193], v[192:193], v[40:41]\n"
"v_pk_mul_f32 v[194:195], v[194:195], v[42:43]\n"
"v_pk_mul_f32 v[196:197], v[196:197], v[44:45]\n"
"v_pk_mul_f32 v[198:199], v[198:199], v[46:47]\n"
"v_exp_f32 v192, v192\n"
"v_exp_f32 v193, v193\n"
"v_exp_f32 v194, v194\n"
"v_exp_f32 v195, v195\n"
"v_exp_f32 v196, v196\n"
"v_exp_f32 v197, v197\n"
"v_exp_f32 v198, v198\n"
"v_exp_f32 v199, v199\n"
"v_pk_add_f32 v[192:193], v[192:193], v[204:205]\n"
"v_pk_add_f32 v[194:195], v[194:195], v[204:205]\n"
"v_pk_add_f32 v[196:197], v[196:197], v[204:205]\n"
"v_pk_add_f32 v[198:199], v[198:199], v[204:205]\n"
"v_rcp_f32 v192, v192\n"
"v_rcp_f32 v193, v193\n"
"v_rcp_f32 v194, v194\n"
"v_rcp_f32 v195, v195\n"
"v_rcp_f32 v196, v196\n"
"v_rcp_f32 v197, v197\n"
"v_rcp_f32 v198, v198\n"
"v_rcp_f32 v199, v199\n"
"v_pk_mul_f32 v[192:193], v[192:193], v[40:41]\n"
"v_pk_mul_f32 v[194:195], v[194:195], v[42:43]\n"
"v_pk_mul_f32 v[196:197], v[196:197], v[44:45]\n"
"v_pk_mul_f32 v[198:199], v[198:199], v[46:47]\n"
"v_cvt_pk_bf16_f32 v148, v192, v193\n"
"v_cvt_pk_bf16_f32 v149, v194, v195\n"
"v_cvt_pk_bf16_f32 v150, v196, v197\n"
"v_cvt_pk_bf16_f32 v151, v198, v199\n"
"buffer_store_dwordx4 v[148:151], v222, %7, s70 offen offset:3072 sc1\n"
"v_pk_mul_f32 v[184:185], v[48:49], v[48:49]\n"
"v_pk_mul_f32 v[186:187], v[50:51], v[50:51]\n"
"v_pk_mul_f32 v[188:189], v[52:53], v[52:53]\n"
"v_pk_mul_f32 v[190:191], v[54:55], v[54:55]\n"
"v_pk_fma_f32 v[184:185], v[184:185], v[200:201], v[202:203]\n"
"v_pk_fma_f32 v[186:187], v[186:187], v[200:201], v[202:203]\n"
"v_pk_fma_f32 v[188:189], v[188:189], v[200:201], v[202:203]\n"
"v_pk_fma_f32 v[190:191], v[190:191], v[200:201], v[202:203]\n"
"v_pk_mul_f32 v[184:185], v[184:185], v[48:49]\n"
"v_pk_mul_f32 v[186:187], v[186:187], v[50:51]\n"
"v_pk_mul_f32 v[188:189], v[188:189], v[52:53]\n"
"v_pk_mul_f32 v[190:191], v[190:191], v[54:55]\n"
"v_exp_f32 v184, v184\n"
"v_exp_f32 v185, v185\n"
"v_exp_f32 v186, v186\n"
"v_exp_f32 v187, v187\n"
"v_exp_f32 v188, v188\n"
"v_exp_f32 v189, v189\n"
"v_exp_f32 v190, v190\n"
"v_exp_f32 v191, v191\n"
"v_pk_add_f32 v[184:185], v[184:185], v[204:205]\n"
"v_pk_add_f32 v[186:187], v[186:187], v[204:205]\n"
"v_pk_add_f32 v[188:189], v[188:189], v[204:205]\n"
"v_pk_add_f32 v[190:191], v[190:191], v[204:205]\n"
"v_rcp_f32 v184, v184\n"
"v_rcp_f32 v185, v185\n"
"v_rcp_f32 v186, v186\n"
"v_rcp_f32 v187, v187\n"
"v_rcp_f32 v188, v188\n"
"v_rcp_f32 v189, v189\n"
"v_rcp_f32 v190, v190\n"
"v_rcp_f32 v191, v191\n"
"v_pk_mul_f32 v[184:185], v[184:185], v[48:49]\n"
"v_pk_mul_f32 v[186:187], v[186:187], v[50:51]\n"
"v_pk_mul_f32 v[188:189], v[188:189], v[52:53]\n"
"v_pk_mul_f32 v[190:191], v[190:191], v[54:55]\n"
"v_cvt_pk_bf16_f32 v152, v184, v185\n"
"v_cvt_pk_bf16_f32 v153, v186, v187\n"
"v_cvt_pk_bf16_f32 v154, v188, v189\n"
"v_cvt_pk_bf16_f32 v155, v190, v191\n"
"buffer_store_dwordx4 v[152:155], v223, %7, s70 offen offset:2048 sc1\n"
"v_pk_mul_f32 v[192:193], v[56:57], v[56:57]\n"
"v_pk_mul_f32 v[194:195], v[58:59], v[58:59]\n"
"v_pk_mul_f32 v[196:197], v[60:61], v[60:61]\n"
"v_pk_mul_f32 v[198:199], v[62:63], v[62:63]\n"
"v_pk_fma_f32 v[192:193], v[192:193], v[200:201], v[202:203]\n"
"v_pk_fma_f32 v[194:195], v[194:195], v[200:201], v[202:203]\n"
"v_pk_fma_f32 v[196:197], v[196:197], v[200:201], v[202:203]\n"
"v_pk_fma_f32 v[198:199], v[198:199], v[200:201], v[202:203]\n"
"v_pk_mul_f32 v[192:193], v[192:193], v[56:57]\n"
"v_pk_mul_f32 v[194:195], v[194:195], v[58:59]\n"
"v_pk_mul_f32 v[196:197], v[196:197], v[60:61]\n"
"v_pk_mul_f32 v[198:199], v[198:199], v[62:63]\n"
"v_exp_f32 v192, v192\n"
"v_exp_f32 v193, v193\n"
"v_exp_f32 v194, v194\n"
"v_exp_f32 v195, v195\n"
"v_exp_f32 v196, v196\n"
"v_exp_f32 v197, v197\n"
"v_exp_f32 v198, v198\n"
"v_exp_f32 v199, v199\n"
"v_pk_add_f32 v[192:193], v[192:193], v[204:205]\n"
"v_pk_add_f32 v[194:195], v[194:195], v[204:205]\n"
"v_pk_add_f32 v[196:197], v[196:197], v[204:205]\n"
"v_pk_add_f32 v[198:199], v[198:199], v[204:205]\n"
"v_rcp_f32 v192, v192\n"
"v_rcp_f32 v193, v193\n"
"v_rcp_f32 v194, v194\n"
"v_rcp_f32 v195, v195\n"
"v_rcp_f32 v196, v196\n"
"v_rcp_f32 v197, v197\n"
"v_rcp_f32 v198, v198\n"
"v_rcp_f32 v199, v199\n"
"v_pk_mul_f32 v[192:193], v[192:193], v[56:57]\n"
"v_pk_mul_f32 v[194:195], v[194:195], v[58:59]\n"
"v_pk_mul_f32 v[196:197], v[196:197], v[60:61]\n"
"v_pk_mul_f32 v[198:199], v[198:199], v[62:63]\n"
"v_cvt_pk_bf16_f32 v156, v192, v193\n"
"v_cvt_pk_bf16_f32 v157, v194, v195\n"
"v_cvt_pk_bf16_f32 v158, v196, v197\n"
"v_cvt_pk_bf16_f32 v159, v198, v199\n"
"buffer_store_dwordx4 v[156:159], v223, %7, s70 offen offset:3072 sc1\n"
"v_pk_mul_f32 v[184:185], v[64:65], v[64:65]\n"
"v_pk_mul_f32 v[186:187], v[66:67], v[66:67]\n"
"v_pk_mul_f32 v[188:189], v[68:69], v[68:69]\n"
"v_pk_mul_f32 v[190:191], v[70:71], v[70:71]\n"
"v_pk_fma_f32 v[184:185], v[184:185], v[200:201], v[202:203]\n"
"v_pk_fma_f32 v[186:187], v[186:187], v[200:201], v[202:203]\n"
"v_pk_fma_f32 v[188:189], v[188:189], v[200:201], v[202:203]\n"
"v_pk_fma_f32 v[190:191], v[190:191], v[200:201], v[202:203]\n"
"v_pk_mul_f32 v[184:185], v[184:185], v[64:65]\n"
"v_pk_mul_f32 v[186:187], v[186:187], v[66:67]\n"
"v_pk_mul_f32 v[188:189], v[188:189], v[68:69]\n"
"v_pk_mul_f32 v[190:191], v[190:191], v[70:71]\n"
"v_exp_f32 v184, v184\n"
"v_exp_f32 v185, v185\n"
"v_exp_f32 v186, v186\n"
"v_exp_f32 v187, v187\n"
"v_exp_f32 v188, v188\n"
"v_exp_f32 v189, v189\n"
"v_exp_f32 v190, v190\n"
"v_exp_f32 v191, v191\n"
"v_pk_add_f32 v[184:185], v[184:185], v[204:205]\n"
"v_pk_add_f32 v[186:187], v[186:187], v[204:205]\n"
"v_pk_add_f32 v[188:189], v[188:189], v[204:205]\n"
"v_pk_add_f32 v[190:191], v[190:191], v[204:205]\n"
"v_rcp_f32 v184, v184\n"
"v_rcp_f32 v185, v185\n"
"v_rcp_f32 v186, v186\n"
"v_rcp_f32 v187, v187\n"
"v_rcp_f32 v188, v188\n"
"v_rcp_f32 v189, v189\n"
"v_rcp_f32 v190, v190\n"
"v_rcp_f32 v191, v191\n"
"v_pk_mul_f32 v[184:185], v[184:185], v[64:65]\n"
"v_pk_mul_f32 v[186:187], v[186:187], v[66:67]\n"
"v_pk_mul_f32 v[188:189], v[188:189], v[68:69]\n"
"v_pk_mul_f32 v[190:191], v[190:191], v[70:71]\n"
"v_cvt_pk_bf16_f32 v160, v184, v185\n"
"v_cvt_pk_bf16_f32 v161, v186, v187\n"
"v_cvt_pk_bf16_f32 v162, v188, v189\n"
"v_cvt_pk_bf16_f32 v163, v190, v191\n"
"v_pk_mul_f32 v[192:193], v[72:73], v[72:73]\n"
"v_pk_mul_f32 v[194:195], v[74:75], v[74:75]\n"
"v_pk_mul_f32 v[196:197], v[76:77], v[76:77]\n"
"v_pk_mul_f32 v[198:199], v[78:79], v[78:79]\n"
"v_pk_fma_f32 v[192:193], v[192:193], v[200:201], v[202:203]\n"
"v_pk_fma_f32 v[194:195], v[194:195], v[200:201], v[202:203]\n"
"v_pk_fma_f32 v[196:197], v[196:197], v[200:201], v[202:203]\n"
"v_pk_fma_f32 v[198:199], v[198:199], v[200:201], v[202:203]\n"
"v_pk_mul_f32 v[192:193], v[192:193], v[72:73]\n"
"v_pk_mul_f32 v[194:195], v[194:195], v[74:75]\n"
"v_pk_mul_f32 v[196:197], v[196:197], v[76:77]\n"
"v_pk_mul_f32 v[198:199], v[198:199], v[78:79]\n"
"v_exp_f32 v192, v192\n"
"v_exp_f32 v193, v193\n"
"v_exp_f32 v194, v194\n"
"v_exp_f32 v195, v195\n"
"v_exp_f32 v196, v196\n"
"v_exp_f32 v197, v197\n"
"v_exp_f32 v198, v198\n"
"v_exp_f32 v199, v199\n"
"v_pk_add_f32 v[192:193], v[192:193], v[204:205]\n"
"v_pk_add_f32 v[194:195], v[194:195], v[204:205]\n"
"v_pk_add_f32 v[196:197], v[196:197], v[204:205]\n"
"v_pk_add_f32 v[198:199], v[198:199], v[204:205]\n"
"v_rcp_f32 v192, v192\n"
"v_rcp_f32 v193, v193\n"
"v_rcp_f32 v194, v194\n"
"v_rcp_f32 v195, v195\n"
"v_rcp_f32 v196, v196\n"
"v_rcp_f32 v197, v197\n"
"v_rcp_f32 v198, v198\n"
"v_rcp_f32 v199, v199\n"
"v_pk_mul_f32 v[192:193], v[192:193], v[72:73]\n"
"v_pk_mul_f32 v[194:195], v[194:195], v[74:75]\n"
"v_pk_mul_f32 v[196:197], v[196:197], v[76:77]\n"
"v_pk_mul_f32 v[198:199], v[198:199], v[78:79]\n"
"v_cvt_pk_bf16_f32 v164, v192, v193\n"
"v_cvt_pk_bf16_f32 v165, v194, v195\n"
"v_cvt_pk_bf16_f32 v166, v196, v197\n"
"v_cvt_pk_bf16_f32 v167, v198, v199\n"
"s_waitcnt vmcnt(0)\n"
"s_barrier\n"
"s_cmp_lg_u32 %15, 0\n"
"s_cbranch_scc1 AG%=\n"
"v_lshlrev_b32 v230, 2, %18\n"
"v_mov_b32 v231, 1\n"
"s_mov_b64 exec, 1\n"
"global_store_dword v230, v231, %13 offset:0 sc1\n"
"s_mov_b64 exec, -1\n"
"AG%=:\n"
"buffer_store_dwordx4 v[160:163], v222, %7, s71 offen offset:0 sc1\n"
"buffer_store_dwordx4 v[164:167], v222, %7, s71 offen offset:1024 sc1\n"
"v_pk_mul_f32 v[184:185], v[80:81], v[80:81]\n"
"v_pk_mul_f32 v[186:187], v[82:83], v[82:83]\n"
"v_pk_mul_f32 v[188:189], v[84:85], v[84:85]\n"
"v_pk_mul_f32 v[190:191], v[86:87], v[86:87]\n"
"v_pk_fma_f32 v[184:185], v[184:185], v[200:201], v[202:203]\n"
"v_pk_fma_f32 v[186:187], v[186:187], v[200:201], v[202:203]\n"
"v_pk_fma_f32 v[188:189], v[188:189], v[200:201], v[202:203]\n"
"v_pk_fma_f32 v[190:191], v[190:191], v[200:201], v[202:203]\n"
"v_pk_mul_f32 v[184:185], v[184:185], v[80:81]\n"
"v_pk_mul_f32 v[186:187], v[186:187], v[82:83]\n"
"v_pk_mul_f32 v[188:189], v[188:189], v[84:85]\n"
"v_pk_mul_f32 v[190:191], v[190:191], v[86:87]\n"
"v_exp_f32 v184, v184\n"
"v_exp_f32 v185, v185\n"
"v_exp_f32 v186, v186\n"
"v_exp_f32 v187, v187\n"
"v_exp_f32 v188, v188\n"
"v_exp_f32 v189, v189\n"
"v_exp_f32 v190, v190\n"
"v_exp_f32 v191, v191\n"
"v_pk_add_f32 v[184:185], v[184:185], v[204:205]\n"
"v_pk_add_f32 v[186:187], v[186:187], v[204:205]\n"
"v_pk_add_f32 v[188:189], v[188:189], v[204:205]\n"
"v_pk_add_f32 v[190:191], v[190:191], v[204:205]\n"
"v_rcp_f32 v184, v184\n"
"v_rcp_f32 v185, v185\n"
"v_rcp_f32 v186, v186\n"
"v_rcp_f32 v187, v187\n"
"v_rcp_f32 v188, v188\n"
"v_rcp_f32 v189, v189\n"
"v_rcp_f32 v190, v190\n"
"v_rcp_f32 v191, v191\n"
"v_pk_mul_f32 v[184:185], v[184:185], v[80:81]\n"
"v_pk_mul_f32 v[186:187], v[186:187], v[82:83]\n"
"v_pk_mul_f32 v[188:189], v[188:189], v[84:85]\n"
"v_pk_mul_f32 v[190:191], v[190:191], v[86:87]\n"
"v_cvt_pk_bf16_f32 v168, v184, v185\n"
"v_cvt_pk_bf16_f32 v169, v186, v187\n"
"v_cvt_pk_bf16_f32 v170, v188, v189\n"
"v_cvt_pk_bf16_f32 v171, v190, v191\n"
"buffer_store_dwordx4 v[168:171], v223, %7, s71 offen offset:0 sc1\n"
"v_pk_mul_f32 v[192:193], v[88:89], v[88:89]\n"
"v_pk_mul_f32 v[194:195], v[90:91], v[90:91]\n"
"v_pk_mul_f32 v[196:197], v[92:93], v[92:93]\n"
"v_pk_mul_f32 v[198:199], v[94:95], v[94:95]\n"
"v_pk_fma_f32 v[192:193], v[192:193], v[200:201], v[202:203]\n"
"v_pk_fma_f32 v[194:195], v[194:195], v[200:201], v[202:203]\n"
"v_pk_fma_f32 v[196:197], v[196:197], v[200:201], v[202:203]\n"
"v_pk_fma_f32 v[198:199], v[198:199], v[200:201], v[202:203]\n"
"v_pk_mul_f32 v[192:193], v[192:193], v[88:89]\n"
"v_pk_mul_f32 v[194:195], v[194:195], v[90:91]\n"
"v_pk_mul_f32 v[196:197], v[196:197], v[92:93]\n"
"v_pk_mul_f32 v[198:199], v[198:199], v[94:95]\n"
"v_exp_f32 v192, v192\n"
"v_exp_f32 v193, v193\n"
"v_exp_f32 v194, v194\n"
"v_exp_f32 v195, v195\n"
"v_exp_f32 v196, v196\n"
"v_exp_f32 v197, v197\n"
"v_exp_f32 v198, v198\n"
"v_exp_f32 v199, v199\n"
"v_pk_add_f32 v[192:193], v[192:193], v[204:205]\n"
"v_pk_add_f32 v[194:195], v[194:195], v[204:205]\n"
"v_pk_add_f32 v[196:197], v[196:197], v[204:205]\n"
"v_pk_add_f32 v[198:199], v[198:199], v[204:205]\n"
"v_rcp_f32 v192, v192\n"
"v_rcp_f32 v193, v193\n"
"v_rcp_f32 v194, v194\n"
"v_rcp_f32 v195, v195\n"
"v_rcp_f32 v196, v196\n"
"v_rcp_f32 v197, v197\n"
"v_rcp_f32 v198, v198\n"
"v_rcp_f32 v199, v199\n"
"v_pk_mul_f32 v[192:193], v[192:193], v[88:89]\n"
"v_pk_mul_f32 v[194:195], v[194:195], v[90:91]\n"
"v_pk_mul_f32 v[196:197], v[196:197], v[92:93]\n"
"v_pk_mul_f32 v[198:199], v[198:199], v[94:95]\n"
"v_cvt_pk_bf16_f32 v172, v192, v193\n"
"v_cvt_pk_bf16_f32 v173, v194, v195\n"
"v_cvt_pk_bf16_f32 v174, v196, v197\n"
"v_cvt_pk_bf16_f32 v175, v198, v199\n"
"buffer_store_dwordx4 v[172:175], v223, %7, s71 offen offset:1024 sc1\n"
"v_add_u32 v230, 262144, %21\n"
"v_pk_mul_f32 v[184:185], v[96:97], v[96:97]\n"
"v_pk_mul_f32 v[186:187], v[98:99], v[98:99]\n"
"v_pk_mul_f32 v[188:189], v[100:101], v[100:101]\n"
"v_pk_mul_f32 v[190:191], v[102:103], v[102:103]\n"
"v_pk_fma_f32 v[184:185], v[184:185], v[200:201], v[202:203]\n"
"v_pk_fma_f32 v[186:187], v[186:187], v[200:201], v[202:203]\n"
"v_pk_fma_f32 v[188:189], v[188:189], v[200:201], v[202:203]\n"
"v_pk_fma_f32 v[190:191], v[190:191], v[200:201], v[202:203]\n"
"v_pk_mul_f32 v[184:185], v[184:185], v[96:97]\n"
"v_pk_mul_f32 v[186:187], v[186:187], v[98:99]\n"
"v_pk_mul_f32 v[188:189], v[188:189], v[100:101]\n"
"v_pk_mul_f32 v[190:191], v[190:191], v[102:103]\n"
"v_exp_f32 v184, v184\n"
"v_exp_f32 v185, v185\n"
"v_exp_f32 v186, v186\n"
"v_exp_f32 v187, v187\n"
"v_exp_f32 v188, v188\n"
"v_exp_f32 v189, v189\n"
"v_exp_f32 v190, v190\n"
"v_exp_f32 v191, v191\n"
"v_pk_add_f32 v[184:185], v[184:185], v[204:205]\n"
"v_pk_add_f32 v[186:187], v[186:187], v[204:205]\n"
"v_pk_add_f32 v[188:189], v[188:189], v[204:205]\n"
"v_pk_add_f32 v[190:191], v[190:191], v[204:205]\n"
"v_rcp_f32 v184, v184\n"
"v_rcp_f32 v185, v185\n"
"v_rcp_f32 v186, v186\n"
"v_rcp_f32 v187, v187\n"
"v_rcp_f32 v188, v188\n"
"v_rcp_f32 v189, v189\n"
"v_rcp_f32 v190, v190\n"
"v_rcp_f32 v191, v191\n"
"v_pk_mul_f32 v[184:185], v[184:185], v[96:97]\n"
"v_pk_mul_f32 v[186:187], v[186:187], v[98:99]\n"
"v_pk_mul_f32 v[188:189], v[188:189], v[100:101]\n"
"v_pk_mul_f32 v[190:191], v[190:191], v[102:103]\n"
"v_cvt_pk_bf16_f32 v176, v184, v185\n"
"v_cvt_pk_bf16_f32 v178, v188, v189\n"
"v_cvt_pk_bf16_f32 v177, v186, v187\n"
"v_cvt_pk_bf16_f32 v179, v190, v191\n"
"s_nop 0\n"
"v_permlane16_swap_b32 v176, v178\n"
"v_permlane16_swap_b32 v177, v179\n"
"buffer_store_dwordx4 v[176:179], %21, %7, %17 offen sc1\n"
"v_pk_mul_f32 v[192:193], v[104:105], v[104:105]\n"
"v_pk_mul_f32 v[194:195], v[106:107], v[106:107]\n"
"v_pk_mul_f32 v[196:197], v[108:109], v[108:109]\n"
"v_pk_mul_f32 v[198:199], v[110:111], v[110:111]\n"
"v_pk_fma_f32 v[192:193], v[192:193], v[200:201], v[202:203]\n"
"v_pk_fma_f32 v[194:195], v[194:195], v[200:201], v[202:203]\n"
"v_pk_fma_f32 v[196:197], v[196:197], v[200:201], v[202:203]\n"
"v_pk_fma_f32 v[198:199], v[198:199], v[200:201], v[202:203]\n"
"v_pk_mul_f32 v[192:193], v[192:193], v[104:105]\n"
"v_pk_mul_f32 v[194:195], v[194:195], v[106:107]\n"
"v_pk_mul_f32 v[196:197], v[196:197], v[108:109]\n"
"v_pk_mul_f32 v[198:199], v[198:199], v[110:111]\n"
"v_exp_f32 v192, v192\n"
"v_exp_f32 v193, v193\n"
"v_exp_f32 v194, v194\n"
"v_exp_f32 v195, v195\n"
"v_exp_f32 v196, v196\n"
"v_exp_f32 v197, v197\n"
"v_exp_f32 v198, v198\n"
"v_exp_f32 v199, v199\n"
"v_pk_add_f32 v[192:193], v[192:193], v[204:205]\n"
"v_pk_add_f32 v[194:195], v[194:195], v[204:205]\n"
"v_pk_add_f32 v[196:197], v[196:197], v[204:205]\n"
"v_pk_add_f32 v[198:199], v[198:199], v[204:205]\n"
"v_rcp_f32 v192, v192\n"
"v_rcp_f32 v193, v193\n"
"v_rcp_f32 v194, v194\n"
"v_rcp_f32 v195, v195\n"
"v_rcp_f32 v196, v196\n"
"v_rcp_f32 v197, v197\n"
"v_rcp_f32 v198, v198\n"
"v_rcp_f32 v199, v199\n"
"v_pk_mul_f32 v[192:193], v[192:193], v[104:105]\n"
"v_pk_mul_f32 v[194:195], v[194:195], v[106:107]\n"
"v_pk_mul_f32 v[196:197], v[196:197], v[108:109]\n"
"v_pk_mul_f32 v[198:199], v[198:199], v[110:111]\n"
"v_cvt_pk_bf16_f32 v180, v192, v193\n"
"v_cvt_pk_bf16_f32 v182, v196, v197\n"
"v_cvt_pk_bf16_f32 v181, v194, v195\n"
"v_cvt_pk_bf16_f32 v183, v198, v199\n"
"s_nop 0\n"
"v_permlane16_swap_b32 v180, v182\n"
"v_permlane16_swap_b32 v181, v183\n"
"buffer_store_dwordx4 v[180:183], v230, %7, %17 offen sc1\n"
"s_waitcnt vmcnt(26)\n"
"s_barrier\n"
"ds_read_b64_tr_b16 a[224:225], v214 offset:0\n"
"ds_read_b64_tr_b16 a[226:227], v214 offset:4096\n"
"ds_read_b64_tr_b16 a[228:229], v214 offset:8192\n"
"ds_read_b64_tr_b16 a[230:231], v214 offset:12288\n"
"ds_read_b64_tr_b16 a[232:233], v215 offset:0\n"
"ds_read_b64_tr_b16 a[234:235], v215 offset:4096\n"
"ds_read_b64_tr_b16 a[236:237], v215 offset:8192\n"
"ds_read_b64_tr_b16 a[238:239], v215 offset:12288\n"
"s_mov_b32 s77, 3\n"
"v_readlane_b32 s76, %20, 3\n"
"v_bfe_u32 v229, %24, 2, 5\n"
"s_waitcnt lgkmcnt(4)\n"
"v_mfma_f32_16x16x32_bf16 v[0:3], a[224:227], v[128:131], 0\n"
"ds_read_b64_tr_b16 a[240:241], v216 offset:0\n"
"v_mfma_f32_16x16x32_bf16 v[0:3], a[228:231], v[144:147], v[0:3]\n"
"ds_read_b64_tr_b16 a[242:243], v216 offset:4096\n"
"v_mfma_f32_16x16x32_bf16 v[8:11], a[228:231], v[148:151], 0\n"
"global_load_dword v228, v229, %13 sc1\n"
"v_mfma_f32_16x16x32_bf16 v[8:11], a[224:227], v[132:135], v[8:11]\n"
"v_mfma_f32_16x16x32_bf16 v[16:19], a[224:227], v[136:139], 0\n"
"ds_read_b64_tr_b16 a[244:245], v216 offset:8192\n"
"v_mfma_f32_16x16x32_bf16 v[16:19], a[228:231], v[152:155], v[16:19]\n"
"ds_read_b64_tr_b16 a[246:247], v216 offset:12288\n"
"v_mfma_f32_16x16x32_bf16 v[24:27], a[228:231], v[156:159], 0\n"
"v_mfma_f32_16x16x32_bf16 v[24:27], a[224:227], v[140:143], v[24:27]\n"
"s_waitcnt lgkmcnt(4)\n"
"v_mfma_f32_16x16x32_bf16 v[28:31], a[232:235], v[140:143], 0\n"
"ds_read_b64_tr_b16 a[248:249], v217 offset:0\n"
"v_mfma_f32_16x16x32_bf16 v[28:31], a[236:239], v[156:159], v[28:31]\n"
"ds_read_b64_tr_b16 a[250:251], v217 offset:4096\n"
"v_mfma_f32_16x16x32_bf16 v[20:23], a[236:239], v[152:155], 0\n"
"v_mfma_f32_16x16x32_bf16 v[20:23], a[232:235], v[136:139], v[20:23]\n"
"v_mfma_f32_16x16x32_bf16 v[12:15], a[232:235], v[132:135], 0\n"
"ds_read_b64_tr_b16 a[252:253], v217 offset:8192\n"
"v_mfma_f32_16x16x32_bf16 v[12:15], a[236:239], v[148:151], v[12:15]\n"
"ds_read_b64_tr_b16 a[254:255], v217 offset:12288\n"
"v_mfma_f32_16x16x32_bf16 v[4:7], a[236:239], v[144:147], 0\n"
"v_mfma_f32_16x16x32_bf16 v[4:7], a[232:235], v[128:131], v[4:7]\n"
"s_waitcnt lgkmcnt(4)\n"
"v_mfma_f32_16x16x32_bf16 v[32:35], a[240:243], v[128:131], 0\n"
"ds_read_b64_tr_b16 a[224:225], v218 offset:0\n"
"v_mfma_f32_16x16x32_bf16 v[32:35], a[244:247], v[144:147], v[32:35]\n"
"ds_read_b64_tr_b16 a[226:227], v218 offset:4096\n"
"v_mfma_f32_16x16x32_bf16 v[40:43], a[244:247], v[148:151], 0\n"
"v_mfma_f32_16x16x32_bf16 v[40:43], a[240:243], v[132:135], v[40:43]\n"
"v_mfma_f32_16x16x32_bf16 v[48:51], a[240:243], v[136:139], 0\n"
"ds_read_b64_tr_b16 a[228:229], v218 offset:8192\n"
"v_mfma_f32_16x16x32_bf16 v[48:51], a[244:247], v[152:155], v[48:51]\n"
"ds_read_b64_tr_b16 a[230:231], v218 offset:12288\n"
"v_mfma_f32_16x16x32_bf16 v[56:59], a[244:247], v[156:159], 0\n"
"v_mfma_f32_16x16x32_bf16 v[56:59], a[240:243], v[140:143], v[56:59]\n"
"s_waitcnt lgkmcnt(4)\n"
"v_mfma_f32_16x16x32_bf16 v[60:63], a[248:251], v[140:143], 0\n"
"ds_read_b64_tr_b16 a[232:233], v219 offset:0\n"
"v_mfma_f32_16x16x32_bf16 v[60:63], a[252:255], v[156:159], v[60:63]\n"
"ds_read_b64_tr_b16 a[234:235], v219 offset:4096\n"
"v_mfma_f32_16x16x32_bf16 v[52:55], a[252:255], v[152:155], 0\n"
"v_mfma_f32_16x16x32_bf16 v[52:55], a[248:251], v[136:139], v[52:55]\n"
"s_waitcnt vmcnt(0)\n"
"v_cmp_eq_u32 vcc, 0, v228\n"
"s_cbranch_vccnz PGS%=\n"
"PGB%=:\n"
"v_mfma_f32_16x16x32_bf16 v[44:47], a[248:251], v[132:135], 0\n"
"ds_read_b64_tr_b16 a[236:237], v219 offset:8192\n"
"s_lshl_b32 s65, s76, 11\n"
"s_add_u32 s77, s77, 1\n"
"v_readlane_b32 s76, %20, s77\n"
"buffer_load_dwordx4 a[48:51], v222, %8, s65 offen offset:0 sc1\n"
"v_mfma_f32_16x16x32_bf16 v[44:47], a[252:255], v[148:151], v[44:47]\n"
"ds_read_b64_tr_b16 a[238:239], v219 offset:12288\n"
"v_mfma_f32_16x16x32_bf16 v[36:39], a[252:255], v[144:147], 0\n"
"v_mfma_f32_16x16x32_bf16 v[36:39], a[248:251], v[128:131], v[36:39]\n"
"buffer_load_dwordx4 a[52:55], v222, %8, s65 offen offset:1024 sc1\n"
"s_waitcnt lgkmcnt(4)\n"
"v_mfma_f32_16x16x32_bf16 v[64:67], a[224:227], v[128:131], 0\n"
"ds_read_b64_tr_b16 a[240:241], v220 offset:0\n"
"v_mfma_f32_16x16x32_bf16 v[64:67], a[228:231], v[144:147], v[64:67]\n"
"ds_read_b64_tr_b16 a[242:243], v220 offset:4096\n"
"v_mfma_f32_16x16x32_bf16 v[72:75], a[228:231], v[148:151], 0\n"
"buffer_load_dwordx4 a[56:59], v223, %8, s65 offen offset:0 sc1\n"
"v_mfma_f32_16x16x32_bf16 v[72:75], a[224:227], v[132:135], v[72:75]\n"
"v_mfma_f32_16x16x32_bf16 v[80:83], a[224:227], v[136:139], 0\n"
"ds_read_b64_tr_b16 a[244:245], v220 offset:8192\n"
"v_mfma_f32_16x16x32_bf16 v[80:83], a[228:231], v[152:155], v[80:83]\n"
"ds_read_b64_tr_b16 a[246:247], v220 offset:12288\n"
"buffer_load_dwordx4 a[60:63], v223, %8, s65 offen offset:1024 sc1\n"
"v_mfma_f32_16x16x32_bf16 v[88:91], a[228:231], v[156:159], 0\n"
"v_mfma_f32_16x16x32_bf16 v[88:91], a[224:227], v[140:143], v[88:91]\n"
"s_waitcnt lgkmcnt(4)\n"
"v_mfma_f32_16x16x32_bf16 v[92:95], a[232:235], v[140:143], 0\n"
"ds_read_b64_tr_b16 a[248:249], v221 offset:0\n"
"s_lshl_b32 s65, s76, 11\n"
"s_add_u32 s77, s77, 1\n"
"v_readlane_b32 s76, %20, s77\n"
"buffer_load_dwordx4 a[64:67], v222, %8, s65 offen offset:0 sc1\n"
"v_mfma_f32_16x16x32_bf16 v[92:95], a[236:239], v[156:159], v[92:95]\n"
"ds_read_b64_tr_b16 a[250:251], v221 offset:4096\n"
"v_mfma_f32_16x16x32_bf16 v[84:87], a[236:239], v[152:155], 0\n"
"v_mfma_f32_16x16x32_bf16 v[84:87], a[232:235], v[136:139], v[84:87]\n"
"buffer_load_dwordx4 a[68:71], v222, %8, s65 offen offset:1024 sc1\n"
"v_mfma_f32_16x16x32_bf16 v[76:79], a[232:235], v[132:135], 0\n"
"ds_read_b64_tr_b16 a[252:253], v221 offset:8192\n"
"v_mfma_f32_16x16x32_bf16 v[76:79], a[236:239], v[148:151], v[76:79]\n"
"ds_read_b64_tr_b16 a[254:255], v221 offset:12288\n"
"v_mfma_f32_16x16x32_bf16 v[68:71], a[236:239], v[144:147], 0\n"
"buffer_load_dwordx4 a[72:75], v223, %8, s65 offen offset:0 sc1\n"
"v_mfma_f32_16x16x32_bf16 v[68:71], a[232:235], v[128:131], v[68:71]\n"
"s_waitcnt lgkmcnt(4)\n"
"v_mfma_f32_16x16x32_bf16 v[96:99], a[240:243], v[128:131], 0\n"
"s_waitcnt vmcnt(8)\n"
"s_barrier\n"
"s_cmp_lg_u32 %15, 0\n"
"s_cbranch_scc1 AH%=\n"
"v_lshlrev_b32 v230, 2, %18\n"
"v_mov_b32 v231, 1\n"
"s_mov_b64 exec, 1\n"
"global_store_dword v230, v231, %13 offset:32 sc1\n"
"s_mov_b64 exec, -1\n"
"AH%=:\n"
"ds_read_b64_tr_b16 a[224:225], v214 offset:16384\n"
"v_mfma_f32_16x16x32_bf16 v[96:99], a[244:247], v[144:147], v[96:99]\n"
"ds_read_b64_tr_b16 a[226:227], v214 offset:20480\n"
"buffer_load_dwordx4 a[76:79], v223, %8, s65 offen offset:1024 sc1\n"
"v_mfma_f32_16x16x32_bf16 v[104:107], a[244:247], v[148:151], 0\n"
"v_mfma_f32_16x16x32_bf16 v[104:107], a[240:243], v[132:135], v[104:107]\n"
"v_mfma_f32_16x16x32_bf16 v[112:115], a[240:243], v[136:139], 0\n"
"ds_read_b64_tr_b16 a[228:229], v215 offset:16384\n"
"s_lshl_b32 s65, s76, 11\n"
"s_add_u32 s77, s77, 1\n"
"v_readlane_b32 s76, %20, s77\n"
"buffer_load_dwordx4 a[80:83], v222, %8, s65 offen offset:0 sc1\n"
"v_mfma_f32_16x16x32_bf16 v[112:115], a[244:247], v[152:155], v[112:115]\n"
"ds_read_b64_tr_b16 a[230:231], v215 offset:20480\n"
"v_mfma_f32_16x16x32_bf16 v[120:123], a[244:247], v[156:159], 0\n"
"v_mfma_f32_16x16x32_bf16 v[120:123], a[240:243], v[140:143], v[120:123]\n"
"buffer_load_dwordx4 a[84:87], v222, %8, s65 offen offset:1024 sc1\n"
"s_waitcnt lgkmcnt(4)\n"
"v_mfma_f32_16x16x32_bf16 v[124:127], a[248:251], v[140:143], 0\n"
"ds_read_b64_tr_b16 a[232:233], v216 offset:16384\n"
"v_mfma_f32_16x16x32_bf16 v[124:127], a[252:255], v[156:159], v[124:127]\n"
"ds_read_b64_tr_b16 a[234:235], v216 offset:20480\n"
"v_mfma_f32_16x16x32_bf16 v[116:119], a[252:255], v[152:155], 0\n"
"buffer_load_dwordx4 a[88:91], v223, %8, s65 offen offset:0 sc1\n"
"v_mfma_f32_16x16x32_bf16 v[116:119], a[248:251], v[136:139], v[116:119]\n"
"v_mfma_f32_16x16x32_bf16 v[108:111], a[248:251], v[132:135], 0\n"
"ds_read_b64_tr_b16 a[236:237], v217 offset:16384\n"
"v_mfma_f32_16x16x32_bf16 v[108:111], a[252:255], v[148:151], v[108:111]\n"
"ds_read_b64_tr_b16 a[238:239], v217 offset:20480\n"
"buffer_load_dwordx4 a[92:95], v223, %8, s65 offen offset:1024 sc1\n"
"v_mfma_f32_16x16x32_bf16 v[100:103], a[252:255], v[144:147], 0\n"
"v_mfma_f32_16x16x32_bf16 v[100:103], a[248:251], v[128:131], v[100:103]\n"
"s_waitcnt lgkmcnt(4)\n"
"v_mfma_f32_16x16x32_bf16 v[0:3], a[224:227], v[160:163], v[0:3]\n"
"ds_read_b64_tr_b16 a[240:241], v218 offset:16384\n"
"s_lshl_b32 s65, s76, 11\n"
"s_add_u32 s77, s77, 1\n"
"v_readlane_b32 s76, %20, s77\n"
"buffer_load_dwordx4 a[96:99], v222, %8, s65 offen offset:0 sc1\n"
"v_mfma_f32_16x16x32_bf16 v[8:11], a[224:227], v[164:167], v[8:11]\n"
"ds_read_b64_tr_b16 a[242:243], v218 offset:20480\n"
"v_mfma_f32_16x16x32_bf16 v[16:19], a[224:227], v[168:171], v[16:19]\n"
"v_mfma_f32_16x16x32_bf16 v[24:27], a[224:227], v[172:175], v[24:27]\n"
"buffer_load_dwordx4 a[100:103], v222, %8, s65 offen offset:1024 sc1\n"
"v_mfma_f32_16x16x32_bf16 v[28:31], a[228:231], v[172:175], v[28:31]\n"
"ds_read_b64_tr_b16 a[244:245], v219 offset:16384\n"
"v_mfma_f32_16x16x32_bf16 v[20:23], a[228:231], v[168:171], v[20:23]\n"
"ds_read_b64_tr_b16 a[246:247], v219 offset:20480\n"
"v_mfma_f32_16x16x32_bf16 v[12:15], a[228:231], v[164:167], v[12:15]\n"
"buffer_load_dwordx4 a[104:107], v223, %8, s65 offen offset:0 sc1\n"
"v_mfma_f32_16x16x32_bf16 v[4:7], a[228:231], v[160:163], v[4:7]\n"
"s_waitcnt lgkmcnt(4)\n"
"v_mfma_f32_16x16x32_bf16 v[32:35], a[232:235], v[160:163], v[32:35]\n"
"ds_read_b64_tr_b16 a[248:249], v220 offset:16384\n"
"v_mfma_f32_16x16x32_bf16 v[40:43], a[232:235], v[164:167], v[40:43]\n"
"ds_read_b64_tr_b16 a[250:251], v220 offset:20480\n"
"buffer_load_dwordx4 a[108:111], v223, %8, s65 offen offset:1024 sc1\n"
"v_mfma_f32_16x16x32_bf16 v[48:51], a[232:235], v[168:171], v[48:51]\n"
"v_mfma_f32_16x16x32_bf16 v[56:59], a[232:235], v[172:175], v[56:59]\n"
"v_mfma_f32_16x16x32_bf16 v[60:63], a[236:239], v[172:175], v[60:63]\n"
"ds_read_b64_tr_b16 a[252:253], v221 offset:16384\n"
"s_lshl_b32 s65, s76, 11\n"
"s_add_u32 s77, s77, 1\n"
"v_readlane_b32 s76, %20, s77\n"
"buffer_load_dwordx4 a[112:115], v222, %8, s65 offen offset:0 sc1\n"
"v_mfma_f32_16x16x32_bf16 v[52:55], a[236:239], v[168:171], v[52:55]\n"
"ds_read_b64_tr_b16 a[254:255], v221 offset:20480\n"
"v_mfma_f32_16x16x32_bf16 v[44:47], a[236:239], v[164:167], v[44:47]\n"
"v_mfma_f32_16x16x32_bf16 v[36:39], a[236:239], v[160:163], v[36:39]\n"
"buffer_load_dwordx4 a[116:119], v222, %8, s65 offen offset:1024 sc1\n"
"s_waitcnt lgkmcnt(4)\n"
"v_mfma_f32_16x16x32_bf16 v[64:67], a[240:243], v[160:163], v[64:67]\n"
"ds_read_b64_tr_b16 a[224:225], v214 offset:24576\n"
"v_mfma_f32_16x16x32_bf16 v[72:75], a[240:243], v[164:167], v[72:75]\n"
"ds_read_b64_tr_b16 a[226:227], v214 offset:28672\n"
"v_mfma_f32_16x16x32_bf16 v[80:83], a[240:243], v[168:171], v[80:83]\n"
"buffer_load_dwordx4 a[120:123], v223, %8, s65 offen offset:0 sc1\n"
"v_mfma_f32_16x16x32_bf16 v[88:91], a[240:243], v[172:175], v[88:91]\n"
"v_mfma_f32_16x16x32_bf16 v[92:95], a[244:247], v[172:175], v[92:95]\n"
"ds_read_b64_tr_b16 a[228:229], v215 offset:24576\n"
"v_mfma_f32_16x16x32_bf16 v[84:87], a[244:247], v[168:171], v[84:87]\n"
"ds_read_b64_tr_b16 a[230:231], v215 offset:28672\n"
"buffer_load_dwordx4 a[124:127], v223, %8, s65 offen offset:1024 sc1\n"
"v_mfma_f32_16x16x32_bf16 v[76:79], a[244:247], v[164:167], v[76:79]\n"
"v_mfma_f32_16x16x32_bf16 v[68:71], a[244:247], v[160:163], v[68:71]\n"
"s_waitcnt lgkmcnt(4)\n"
"v_mfma_f32_16x16x32_bf16 v[96:99], a[248:251], v[160:163], v[96:99]\n"
"ds_read_b64_tr_b16 a[232:233], v216 offset:24576\n"
"s_lshl_b32 s65, s76, 11\n"
"s_add_u32 s77, s77, 1\n"
"v_readlane_b32 s76, %20, s77\n"
"buffer_load_dwordx4 a[0:3], v222, %8, s65 offen offset:0 sc1\n"
"v_mfma_f32_16x16x32_bf16 v[104:107], a[248:251], v[164:167], v[104:107]\n"
"ds_read_b64_tr_b16 a[234:235], v216 offset:28672\n"
"v_mfma_f32_16x16x32_bf16 v[112:115], a[248:251], v[168:171], v[112:115]\n"
"v_mfma_f32_16x16x32_bf16 v[120:123], a[248:251], v[172:175], v[120:123]\n"
"buffer_load_dwordx4 a[4:7], v222, %8, s65 offen offset:1024 sc1\n"
"v_mfma_f32_16x16x32_bf16 v[124:127], a[252:255], v[172:175], v[124:127]\n"
"ds_read_b64_tr_b16 a[236:237], v217 offset:24576\n"
"v_mfma_f32_16x16x32_bf16 v[116:119], a[252:255], v[168:171], v[116:119]\n"
"ds_read_b64_tr_b16 a[238:239], v217 offset:28672\n"
"v_mfma_f32_16x16x32_bf16 v[108:111], a[252:255], v[164:167], v[108:111]\n"
"buffer_load_dwordx4 a[8:11], v223, %8, s65 offen offset:0 sc1\n"
"v_mfma_f32_16x16x32_bf16 v[100:103], a[252:255], v[160:163], v[100:103]\n"
"s_waitcnt vmcnt(19) lgkmcnt(4)\n"
"v_mfma_f32_16x16x32_bf16 v[0:3], a[224:227], a[48:51], v[0:3]\n"
"ds_read_b64_tr_b16 a[240:241], v218 offset:24576\n"
"v_mfma_f32_16x16x32_bf16 v[8:11], a[224:227], a[52:55], v[8:11]\n"
"ds_read_b64_tr_b16 a[242:243], v218 offset:28672\n"
"buffer_load_dwordx4 a[12:15], v223, %8, s65 offen offset:1024 sc1\n"
"v_mfma_f32_16x16x32_bf16 v[16:19], a[224:227], a[56:59], v[16:19]\n"
"v_mfma_f32_16x16x32_bf16 v[24:27], a[224:227], a[60:63], v[24:27]\n"
"v_mfma_f32_16x16x32_bf16 v[28:31], a[228:231], a[60:63], v[28:31]\n"
"ds_read_b64_tr_b16 a[244:245], v219 offset:24576\n"
"s_lshl_b32 s65, s76, 11\n"
"s_add_u32 s77, s77, 1\n"
"v_readlane_b32 s76, %20, s77\n"
"buffer_load_dwordx4 a[16:19], v222, %8, s65 offen offset:0 sc1\n"
"v_mfma_f32_16x16x32_bf16 v[20:23], a[228:231], a[56:59], v[20:23]\n"
"ds_read_b64_tr_b16 a[246:247], v219 offset:28672\n"
"v_mfma_f32_16x16x32_bf16 v[12:15], a[228:231], a[52:55], v[12:15]\n"
"v_mfma_f32_16x16x32_bf16 v[4:7], a[228:231], a[48:51], v[4:7]\n"
"buffer_load_dwordx4 a[20:23], v222, %8, s65 offen offset:1024 sc1\n"
"s_waitcnt lgkmcnt(4)\n"
"v_mfma_f32_16x16x32_bf16 v[32:35], a[232:235], a[48:51], v[32:35]\n"
"ds_read_b64_tr_b16 a[248:249], v220 offset:24576\n"
"v_mfma_f32_16x16x32_bf16 v[40:43], a[232:235], a[52:55], v[40:43]\n"
"ds_read_b64_tr_b16 a[250:251], v220 offset:28672\n"
"v_mfma_f32_16x16x32_bf16 v[48:51], a[232:235], a[56:59], v[48:51]\n"
"buffer_load_dwordx4 a[24:27], v223, %8, s65 offen offset:0 sc1\n"
"v_mfma_f32_16x16x32_bf16 v[56:59], a[232:235], a[60:63], v[56:59]\n"
"v_mfma_f32_16x16x32_bf16 v[60:63], a[236:239], a[60:63], v[60:63]\n"
"ds_read_b64_tr_b16 a[252:253], v221 offset:24576\n"
"v_mfma_f32_16x16x32_bf16 v[52:55], a[236:239], a[56:59], v[52:55]\n"
"ds_read_b64_tr_b16 a[254:255], v221 offset:28672\n"
"buffer_load_dwordx4 a[28:31], v223, %8, s65 offen offset:1024 sc1\n"
"v_mfma_f32_16x16x32_bf16 v[44:47], a[236:239], a[52:55], v[44:47]\n"
"v_mfma_f32_16x16x32_bf16 v[36:39], a[236:239], a[48:51], v[36:39]\n"
"s_waitcnt lgkmcnt(4)\n"
"v_mfma_f32_16x16x32_bf16 v[64:67], a[240:243], a[48:51], v[64:67]\n"
"s_waitcnt vmcnt(47)\n"
"s_barrier\n"
"ds_read_b64_tr_b16 a[224:225], v214 offset:32768\n"
"v_mfma_f32_16x16x32_bf16 v[72:75], a[240:243], a[52:55], v[72:75]\n"
"ds_read_b64_tr_b16 a[226:227], v214 offset:36864\n"
"v_mfma_f32_16x16x32_bf16 v[80:83], a[240:243], a[56:59], v[80:83]\n"
"v_mfma_f32_16x16x32_bf16 v[88:91], a[240:243], a[60:63], v[88:91]\n"
"v_mfma_f32_16x16x32_bf16 v[92:95], a[244:247], a[60:63], v[92:95]\n"
"ds_read_b64_tr_b16 a[228:229], v214 offset:40960\n"
"v_mfma_f32_16x16x32_bf16 v[84:87], a[244:247], a[56:59], v[84:87]\n"
"ds_read_b64_tr_b16 a[230:231], v214 offset:45056\n"
"v_mfma_f32_16x16x32_bf16 v[76:79], a[244:247], a[52:55], v[76:79]\n"
"v_mfma_f32_16x16x32_bf16 v[68:71], a[244:247], a[48:51], v[68:71]\n"
"s_waitcnt lgkmcnt(4)\n"
"v_mfma_f32_16x16x32_bf16 v[96:99], a[248:251], a[48:51], v[96:99]\n"
"ds_read_b64_tr_b16 a[232:233], v215 offset:32768\n"
"v_mfma_f32_16x16x32_bf16 v[104:107], a[248:251], a[52:55], v[104:107]\n"
"ds_read_b64_tr_b16 a[234:235], v215 offset:36864\n"
"v_mfma_f32_16x16x32_bf16 v[112:115], a[248:251], a[56:59], v[112:115]\n"
"v_mfma_f32_16x16x32_bf16 v[120:123], a[248:251], a[60:63], v[120:123]\n"
"v_mfma_f32_16x16x32_bf16 v[124:127], a[252:255], a[60:63], v[124:127]\n"
"ds_read_b64_tr_b16 a[236:237], v215 offset:40960\n"
"v_mfma_f32_16x16x32_bf16 v[116:119], a[252:255], a[56:59], v[116:119]\n"
"ds_read_b64_tr_b16 a[238:239], v215 offset:45056\n"
"v_mfma_f32_16x16x32_bf16 v[108:111], a[252:255], a[52:55], v[108:111]\n"
"v_mfma_f32_16x16x32_bf16 v[100:103], a[252:255], a[48:51], v[100:103]\n"
"s_mov_b32 s66, 2\n"
"LOOP_F%=:\n"
"s_waitcnt vmcnt(16) lgkmcnt(4)\n"
"v_mfma_f32_16x16x32_bf16 v[0:3], a[224:227], a[64:67], v[0:3]\n"
"ds_read_b64_tr_b16 a[240:241], v216 offset:32768\n"
"v_mfma_f32_16x16x32_bf16 v[0:3], a[228:231], a[80:83], v[0:3]\n"
"ds_read_b64_tr_b16 a[242:243], v216 offset:36864\n"
"v_mfma_f32_16x16x32_bf16 v[8:11], a[228:231], a[84:87], v[8:11]\n"
"v_mfma_f32_16x16x32_bf16 v[8:11], a[224:227], a[68:71], v[8:11]\n"
"s_lshl_b32 s73, s69, 16\n"
"v_mfma_f32_16x16x32_bf16 v[16:19], a[224:227], a[72:75], v[16:19]\n"
"ds_read_b64_tr_b16 a[244:245], v216 offset:40960\n"
"s_add_u32 s73, s73, s75\n"
"v_mfma_f32_16x16x32_bf16 v[16:19], a[228:231], a[88:91], v[16:19]\n"
"ds_read_b64_tr_b16 a[246:247], v216 offset:45056\n"
"s_add_u32 m0, %15, 65536\n"
"v_mfma_f32_16x16x32_bf16 v[24:27], a[228:231], a[92:95], v[24:27]\n"
"buffer_load_dwordx4 v224, %9, s73 offen offset:0 lds\n"
"v_mfma_f32_16x16x32_bf16 v[24:27], a[224:227], a[76:79], v[24:27]\n"
"s_waitcnt lgkmcnt(4)\n"
"v_mfma_f32_16x16x32_bf16 v[28:31], a[232:235], a[76:79], v[28:31]\n"
"ds_read_b64_tr_b16 a[248:249], v217 offset:32768\n"
"s_add_u32 s74, s74, 2\n"
"v_mfma_f32_16x16x32_bf16 v[28:31], a[236:239], a[92:95], v[28:31]\n"
"ds_read_b64_tr_b16 a[250:251], v217 offset:36864\n"
"v_readlane_b32 s69, %20, s74\n"
"v_mfma_f32_16x16x32_bf16 v[20:23], a[236:239], a[88:91], v[20:23]\n"
"buffer_load_dwordx4 v225, %9, s73 offen offset:1024 lds\n"
"v_mfma_f32_16x16x32_bf16 v[20:23], a[232:235], a[72:75], v[20:23]\n"
"v_mfma_f32_16x16x32_bf16 v[12:15], a[232:235], a[68:71], v[12:15]\n"
"ds_read_b64_tr_b16 a[252:253], v217 offset:40960\n"
"v_mfma_f32_16x16x32_bf16 v[12:15], a[236:239], a[84:87], v[12:15]\n"
"ds_read_b64_tr_b16 a[254:255], v217 offset:45056\n"
"v_mfma_f32_16x16x32_bf16 v[4:7], a[236:239], a[80:83], v[4:7]\n"
"buffer_load_dwordx4 v226, %9, s73 offen offset:2048 lds\n"
"v_mfma_f32_16x16x32_bf16 v[4:7], a[232:235], a[64:67], v[4:7]\n"
"s_waitcnt lgkmcnt(4)\n"
"v_mfma_f32_16x16x32_bf16 v[32:35], a[240:243], a[64:67], v[32:35]\n"
"ds_read_b64_tr_b16 a[224:225], v218 offset:32768\n"
"v_mfma_f32_16x16x32_bf16 v[32:35], a[244:247], a[80:83], v[32:35]\n"
"ds_read_b64_tr_b16 a[226:227], v218 offset:36864\n"
"v_mfma_f32_16x16x32_bf16 v[40:43], a[244:247], a[84:87], v[40:43]\n"
"buffer_load_dwordx4 v227, %9, s73 offen offset:3072 lds\n"
"v_mfma_f32_16x16x32_bf16 v[40:43], a[240:243], a[68:71], v[40:43]\n"
"v_mfma_f32_16x16x32_bf16 v[48:51], a[240:243], a[72:75], v[48:51]\n"
"ds_read_b64_tr_b16 a[228:229], v218 offset:40960\n"
"v_mfma_f32_16x16x32_bf16 v[48:51], a[244:247], a[88:91], v[48:51]\n"
"ds_read_b64_tr_b16 a[230:231], v218 offset:45056\n"
"v_mfma_f32_16x16x32_bf16 v[56:59], a[244:247], a[92:95], v[56:59]\n"
"v_mfma_f32_16x16x32_bf16 v[56:59], a[240:243], a[76:79], v[56:59]\n"
"s_waitcnt lgkmcnt(4)\n"
"v_mfma_f32_16x16x32_bf16 v[60:63], a[248:251], a[76:79], v[60:63]\n"
"ds_read_b64_tr_b16 a[232:233], v219 offset:32768\n"
"v_mfma_f32_16x16x32_bf16 v[60:63], a[252:255], a[92:95], v[60:63]\n"
"ds_read_b64_tr_b16 a[234:235], v219 offset:36864\n"
"s_lshl_b32 s65, s76, 11\n"
"v_mfma_f32_16x16x32_bf16 v[52:55], a[252:255], a[88:91], v[52:55]\n"
"buffer_load_dwordx4 a[32:35], v222, %8, s65 offen offset:0 sc1\n"
"v_mfma_f32_16x16x32_bf16 v[52:55], a[248:251], a[72:75], v[52:55]\n"
"v_mfma_f32_16x16x32_bf16 v[44:47], a[248:251], a[68:71], v[44:47]\n"
"ds_read_b64_tr_b16 a[236:237], v219 offset:40960\n"
"s_add_u32 s77, s77, 1\n"
"v_mfma_f32_16x16x32_bf16 v[44:47], a[252:255], a[84:87], v[44:47]\n"
"ds_read_b64_tr_b16 a[238:239], v219 offset:45056\n"
"v_readlane_b32 s76, %20, s77\n"
"v_mfma_f32_16x16x32_bf16 v[36:39], a[252:255], a[80:83], v[36:39]\n"
"buffer_load_dwordx4 a[36:39], v222, %8, s65 offen offset:1024 sc1\n"
"v_mfma_f32_16x16x32_bf16 v[36:39], a[248:251], a[64:67], v[36:39]\n"
"s_waitcnt lgkmcnt(4)\n"
"v_mfma_f32_16x16x32_bf16 v[64:67], a[224:227], a[64:67], v[64:67]\n"
"ds_read_b64_tr_b16 a[240:241], v220 offset:32768\n"
"v_mfma_f32_16x16x32_bf16 v[64:67], a[228:231], a[80:83], v[64:67]\n"
"ds_read_b64_tr_b16 a[242:243], v220 offset:36864\n"
"v_mfma_f32_16x16x32_bf16 v[72:75], a[228:231], a[84:87], v[72:75]\n"
"buffer_load_dwordx4 a[40:43], v223, %8, s65 offen offset:0 sc1\n"
"v_mfma_f32_16x16x32_bf16 v[72:75], a[224:227], a[68:71], v[72:75]\n"
"v_mfma_f32_16x16x32_bf16 v[80:83], a[224:227], a[72:75], v[80:83]\n"
"ds_read_b64_tr_b16 a[244:245], v220 offset:40960\n"
"v_mfma_f32_16x16x32_bf16 v[80:83], a[228:231], a[88:91], v[80:83]\n"
"ds_read_b64_tr_b16 a[246:247], v220 offset:45056\n"
"v_mfma_f32_16x16x32_bf16 v[88:91], a[228:231], a[92:95], v[88:91]\n"
"buffer_load_dwordx4 a[44:47], v223, %8, s65 offen offset:1024 sc1\n"
"v_mfma_f32_16x16x32_bf16 v[88:91], a[224:227], a[76:79], v[88:91]\n"
"s_waitcnt lgkmcnt(4)\n"
"v_mfma_f32_16x16x32_bf16 v[92:95], a[232:235], a[76:79], v[92:95]\n"
"ds_read_b64_tr_b16 a[248:249], v221 offset:32768\n"
"v_mfma_f32_16x16x32_bf16 v[92:95], a[236:239], a[92:95], v[92:95]\n"
"ds_read_b64_tr_b16 a[250:251], v221 offset:36864\n"
"v_mfma_f32_16x16x32_bf16 v[84:87], a[236:239], a[88:91], v[84:87]\n"
"v_mfma_f32_16x16x32_bf16 v[84:87], a[232:235], a[72:75], v[84:87]\n"
"v_mfma_f32_16x16x32_bf16 v[76:79], a[232:235], a[68:71], v[76:79]\n"
"ds_read_b64_tr_b16 a[252:253], v221 offset:40960\n"
"v_mfma_f32_16x16x32_bf16 v[76:79], a[236:239], a[84:87], v[76:79]\n"
"ds_read_b64_tr_b16 a[254:255], v221 offset:45056\n"
"s_lshl_b32 s65, s76, 11\n"
"v_mfma_f32_16x16x32_bf16 v[68:71], a[236:239], a[80:83], v[68:71]\n"
"buffer_load_dwordx4 a[48:51], v222, %8, s65 offen offset:0 sc1\n"
"v_mfma_f32_16x16x32_bf16 v[68:71], a[232:235], a[64:67], v[68:71]\n"
"s_waitcnt lgkmcnt(4)\n"
"v_mfma_f32_16x16x32_bf16 v[96:99], a[240:243], a[64:67], v[96:99]\n"
"s_waitcnt vmcnt(17)\n"
"s_barrier\n"
"ds_read_b64_tr_b16 a[224:225], v214 offset:49152\n"
"s_add_u32 s77, s77, 1\n"
"v_mfma_f32_16x16x32_bf16 v[96:99], a[244:247], a[80:83], v[96:99]\n"
"ds_read_b64_tr_b16 a[226:227], v214 offset:53248\n"
"v_readlane_b32 s76, %20, s77\n"
"v_mfma_f32_16x16x32_bf16 v[104:107], a[244:247], a[84:87], v[104:107]\n"
"buffer_load_dwordx4 a[52:55], v222, %8, s65 offen offset:1024 sc1\n"
"v_mfma_f32_16x16x32_bf16 v[104:107], a[240:243], a[68:71], v[104:107]\n"
"v_mfma_f32_16x16x32_bf16 v[112:115], a[240:243], a[72:75], v[112:115]\n"
"ds_read_b64_tr_b16 a[228:229], v214 offset:57344\n"
"v_mfma_f32_16x16x32_bf16 v[112:115], a[244:247], a[88:91], v[112:115]\n"
"ds_read_b64_tr_b16 a[230:231], v214 offset:61440\n"
"v_mfma_f32_16x16x32_bf16 v[120:123], a[244:247], a[92:95], v[120:123]\n"
"buffer_load_dwordx4 a[56:59], v223, %8, s65 offen offset:0 sc1\n"
"v_mfma_f32_16x16x32_bf16 v[120:123], a[240:243], a[76:79], v[120:123]\n"
"s_waitcnt lgkmcnt(4)\n"
"v_mfma_f32_16x16x32_bf16 v[124:127], a[248:251], a[76:79], v[124:127]\n"
"ds_read_b64_tr_b16 a[232:233], v215 offset:49152\n"
"v_mfma_f32_16x16x32_bf16 v[124:127], a[252:255], a[92:95], v[124:127]\n"
"ds_read_b64_tr_b16 a[234:235], v215 offset:53248\n"
"v_mfma_f32_16x16x32_bf16 v[116:119], a[252:255], a[88:91], v[116:119]\n"
"buffer_load_dwordx4 a[60:63], v223, %8, s65 offen offset:1024 sc1\n"
"v_mfma_f32_16x16x32_bf16 v[116:119], a[248:251], a[72:75], v[116:119]\n"
"v_mfma_f32_16x16x32_bf16 v[108:111], a[248:251], a[68:71], v[108:111]\n"
"ds_read_b64_tr_b16 a[236:237], v215 offset:57344\n"
"v_mfma_f32_16x16x32_bf16 v[108:111], a[252:255], a[84:87], v[108:111]\n"
"ds_read_b64_tr_b16 a[238:239], v215 offset:61440\n"
"v_mfma_f32_16x16x32_bf16 v[100:103], a[252:255], a[80:83], v[100:103]\n"
"v_mfma_f32_16x16x32_bf16 v[100:103], a[248:251], a[64:67], v[100:103]\n"
"s_waitcnt vmcnt(20) lgkmcnt(4)\n"
"v_mfma_f32_16x16x32_bf16 v[0:3], a[224:227], a[96:99], v[0:3]\n"
"ds_read_b64_tr_b16 a[240:241], v216 offset:49152\n"
"v_mfma_f32_16x16x32_bf16 v[0:3], a[228:231], a[112:115], v[0:3]\n"
"ds_read_b64_tr_b16 a[242:243], v216 offset:53248\n"
"v_mfma_f32_16x16x32_bf16 v[8:11], a[228:231], a[116:119], v[8:11]\n"
"v_mfma_f32_16x16x32_bf16 v[8:11], a[224:227], a[100:103], v[8:11]\n"
"s_lshl_b32 s73, s69, 16\n"
"v_mfma_f32_16x16x32_bf16 v[16:19], a[224:227], a[104:107], v[16:19]\n"
"ds_read_b64_tr_b16 a[244:245], v216 offset:57344\n"
"s_add_u32 s73, s73, s75\n"
"v_mfma_f32_16x16x32_bf16 v[16:19], a[228:231], a[120:123], v[16:19]\n"
"ds_read_b64_tr_b16 a[246:247], v216 offset:61440\n"
"s_add_u32 m0, %15, 81920\n"
"v_mfma_f32_16x16x32_bf16 v[24:27], a[228:231], a[124:127], v[24:27]\n"
"buffer_load_dwordx4 v224, %9, s73 offen offset:0 lds\n"
"v_mfma_f32_16x16x32_bf16 v[24:27], a[224:227], a[108:111], v[24:27]\n"
"s_waitcnt lgkmcnt(4)\n"
"v_mfma_f32_16x16x32_bf16 v[28:31], a[232:235], a[108:111], v[28:31]\n"
"ds_read_b64_tr_b16 a[248:249], v217 offset:49152\n"
"s_add_u32 s74, s74, 2\n"
"v_mfma_f32_16x16x32_bf16 v[28:31], a[236:239], a[124:127], v[28:31]\n"
"ds_read_b64_tr_b16 a[250:251], v217 offset:53248\n"
"v_readlane_b32 s69, %20, s74\n"
"v_mfma_f32_16x16x32_bf16 v[20:23], a[236:239], a[120:123], v[20:23]\n"
"buffer_load_dwordx4 v225, %9, s73 offen offset:1024 lds\n"
"v_mfma_f32_16x16x32_bf16 v[20:23], a[232:235], a[104:107], v[20:23]\n"
"v_mfma_f32_16x16x32_bf16 v[12:15], a[232:235], a[100:103], v[12:15]\n"
"ds_read_b64_tr_b16 a[252:253], v217 offset:57344\n"
"v_mfma_f32_16x16x32_bf16 v[12:15], a[236:239], a[116:119], v[12:15]\n"
"ds_read_b64_tr_b16 a[254:255], v217 offset:61440\n"
"v_mfma_f32_16x16x32_bf16 v[4:7], a[236:239], a[112:115], v[4:7]\n"
"buffer_load_dwordx4 v226, %9, s73 offen offset:2048 lds\n"
"v_mfma_f32_16x16x32_bf16 v[4:7], a[232:235], a[96:99], v[4:7]\n"
"s_waitcnt lgkmcnt(4)\n"
"v_mfma_f32_16x16x32_bf16 v[32:35], a[240:243], a[96:99], v[32:35]\n"
"ds_read_b64_tr_b16 a[224:225], v218 offset:49152\n"
"v_mfma_f32_16x16x32_bf16 v[32:35], a[244:247], a[112:115], v[32:35]\n"
"ds_read_b64_tr_b16 a[226:227], v218 offset:53248\n"
"v_mfma_f32_16x16x32_bf16 v[40:43], a[244:247], a[116:119], v[40:43]\n"
"buffer_load_dwordx4 v227, %9, s73 offen offset:3072 lds\n"
"v_mfma_f32_16x16x32_bf16 v[40:43], a[240:243], a[100:103], v[40:43]\n"
"v_mfma_f32_16x16x32_bf16 v[48:51], a[240:243], a[104:107], v[48:51]\n"
"ds_read_b64_tr_b16 a[228:229], v218 offset:57344\n"
"v_mfma_f32_16x16x32_bf16 v[48:51], a[244:247], a[120:123], v[48:51]\n"
"ds_read_b64_tr_b16 a[230:231], v218 offset:61440\n"
"v_mfma_f32_16x16x32_bf16 v[56:59], a[244:247], a[124:127], v[56:59]\n"
"v_mfma_f32_16x16x32_bf16 v[56:59], a[240:243], a[108:111], v[56:59]\n"
"s_waitcnt lgkmcnt(4)\n"
"v_mfma_f32_16x16x32_bf16 v[60:63], a[248:251], a[108:111], v[60:63]\n"
"ds_read_b64_tr_b16 a[232:233], v219 offset:49152\n"
"v_mfma_f32_16x16x32_bf16 v[60:63], a[252:255], a[124:127], v[60:63]\n"
"ds_read_b64_tr_b16 a[234:235], v219 offset:53248\n"
"s_lshl_b32 s65, s76, 11\n"
"v_mfma_f32_16x16x32_bf16 v[52:55], a[252:255], a[120:123], v[52:55]\n"
"buffer_load_dwordx4 a[64:67], v222, %8, s65 offen offset:0 sc1\n"
"v_mfma_f32_16x16x32_bf16 v[52:55], a[248:251], a[104:107], v[52:55]\n"
"v_mfma_f32_16x16x32_bf16 v[44:47], a[248:251], a[100:103], v[44:47]\n"
"ds_read_b64_tr_b16 a[236:237], v219 offset:57344\n"
"s_add_u32 s77, s77, 1\n"
"v_mfma_f32_16x16x32_bf16 v[44:47], a[252:255], a[116:119], v[44:47]\n"
"ds_read_b64_tr_b16 a[238:239], v219 offset:61440\n"
"v_readlane_b32 s76, %20, s77\n"
"v_mfma_f32_16x16x32_bf16 v[36:39], a[252:255], a[112:115], v[36:39]\n"
"buffer_load_dwordx4 a[68:71], v222, %8, s65 offen offset:1024 sc1\n"
"v_mfma_f32_16x16x32_bf16 v[36:39], a[248:251], a[96:99], v[36:39]\n"
"s_waitcnt lgkmcnt(4)\n"
"v_mfma_f32_16x16x32_bf16 v[64:67], a[224:227], a[96:99], v[64:67]\n"
"ds_read_b64_tr_b16 a[240:241], v220 offset:49152\n"
"v_mfma_f32_16x16x32_bf16 v[64:67], a[228:231], a[112:115], v[64:67]\n"
"ds_read_b64_tr_b16 a[242:243], v220 offset:53248\n"
"v_mfma_f32_16x16x32_bf16 v[72:75], a[228:231], a[116:119], v[72:75]\n"
"buffer_load_dwordx4 a[72:75], v223, %8, s65 offen offset:0 sc1\n"
"v_mfma_f32_16x16x32_bf16 v[72:75], a[224:227], a[100:103], v[72:75]\n"
"v_mfma_f32_16x16x32_bf16 v[80:83], a[224:227], a[104:107], v[80:83]\n"
"ds_read_b64_tr_b16 a[244:245], v220 offset:57344\n"
"v_mfma_f32_16x16x32_bf16 v[80:83], a[228:231], a[120:123], v[80:83]\n"
"ds_read_b64_tr_b16 a[246:247], v220 offset:61440\n"
"v_mfma_f32_16x16x32_bf16 v[88:91], a[228:231], a[124:127], v[88:91]\n"
"buffer_load_dwordx4 a[76:79], v223, %8, s65 offen offset:1024 sc1\n"
"v_mfma_f32_16x16x32_bf16 v[88:91], a[224:227], a[108:111], v[88:91]\n"
"s_waitcnt lgkmcnt(4)\n"
"v_mfma_f32_16x16x32_bf16 v[92:95], a[232:235], a[108:111], v[92:95]\n"
"ds_read_b64_tr_b16 a[248:249], v221 offset:49152\n"
"v_mfma_f32_16x16x32_bf16 v[92:95], a[236:239], a[124:127], v[92:95]\n"
"ds_read_b64_tr_b16 a[250:251], v221 offset:53248\n"
"v_mfma_f32_16x16x32_bf16 v[84:87], a[236:239], a[120:123], v[84:87]\n"
"v_mfma_f32_16x16x32_bf16 v[84:87], a[232:235], a[104:107], v[84:87]\n"
"v_mfma_f32_16x16x32_bf16 v[76:79], a[232:235], a[100:103], v[76:79]\n"
"ds_read_b64_tr_b16 a[252:253], v221 offset:57344\n"
"v_mfma_f32_16x16x32_bf16 v[76:79], a[236:239], a[116:119], v[76:79]\n"
"ds_read_b64_tr_b16 a[254:255], v221 offset:61440\n"
"s_lshl_b32 s65, s76, 11\n"
"v_mfma_f32_16x16x32_bf16 v[68:71], a[236:239], a[112:115], v[68:71]\n"
"buffer_load_dwordx4 a[80:83], v222, %8, s65 offen offset:0 sc1\n"
"v_mfma_f32_16x16x32_bf16 v[68:71], a[232:235], a[96:99], v[68:71]\n"
"s_waitcnt lgkmcnt(4)\n"
"v_mfma_f32_16x16x32_bf16 v[96:99], a[240:243], a[96:99], v[96:99]\n"
"s_waitcnt vmcnt(17)\n"
"s_barrier\n"
"ds_read_b64_tr_b16 a[224:225], v214 offset:0\n"
"s_add_u32 s77, s77, 1\n"
"v_mfma_f32_16x16x32_bf16 v[96:99], a[244:247], a[112:115], v[96:99]\n"
"ds_read_b64_tr_b16 a[226:227], v214 offset:4096\n"
"v_readlane_b32 s76, %20, s77\n"
"v_mfma_f32_16x16x32_bf16 v[104:107], a[244:247], a[116:119], v[104:107]\n"
"buffer_load_dwordx4 a[84:87], v222, %8, s65 offen offset:1024 sc1\n"
"v_mfma_f32_16x16x32_bf16 v[104:107], a[240:243], a[100:103], v[104:107]\n"
"v_mfma_f32_16x16x32_bf16 v[112:115], a[240:243], a[104:107], v[112:115]\n"
"ds_read_b64_tr_b16 a[228:229], v214 offset:8192\n"
"v_mfma_f32_16x16x32_bf16 v[112:115], a[244:247], a[120:123], v[112:115]\n"
"ds_read_b64_tr_b16 a[230:231], v214 offset:12288\n"
"v_mfma_f32_16x16x32_bf16 v[120:123], a[244:247], a[124:127], v[120:123]\n"
"buffer_load_dwordx4 a[88:91], v223, %8, s65 offen offset:0 sc1\n"
"v_mfma_f32_16x16x32_bf16 v[120:123], a[240:243], a[108:111], v[120:123]\n"
"s_waitcnt lgkmcnt(4)\n"
"v_mfma_f32_16x16x32_bf16 v[124:127], a[248:251], a[108:111], v[124:127]\n"
"ds_read_b64_tr_b16 a[232:233], v215 offset:0\n"
"v_mfma_f32_16x16x32_bf16 v[124:127], a[252:255], a[124:127], v[124:127]\n"
"ds_read_b64_tr_b16 a[234:235], v215 offset:4096\n"
"v_mfma_f32_16x16x32_bf16 v[116:119], a[252:255], a[120:123], v[116:119]\n"
"buffer_load_dwordx4 a[92:95], v223, %8, s65 offen offset:1024 sc1\n"
"v_mfma_f32_16x16x32_bf16 v[116:119], a[248:251], a[104:107], v[116:119]\n"
"v_mfma_f32_16x16x32_bf16 v[108:111], a[248:251], a[100:103], v[108:111]\n"
"ds_read_b64_tr_b16 a[236:237], v215 offset:8192\n"
"v_mfma_f32_16x16x32_bf16 v[108:111], a[252:255], a[116:119], v[108:111]\n"
"ds_read_b64_tr_b16 a[238:239], v215 offset:12288\n"
"v_mfma_f32_16x16x32_bf16 v[100:103], a[252:255], a[112:115], v[100:103]\n"
"v_mfma_f32_16x16x32_bf16 v[100:103], a[248:251], a[96:99], v[100:103]\n"
"s_waitcnt vmcnt(24) lgkmcnt(4)\n"
"v_mfma_f32_16x16x32_bf16 v[0:3], a[224:227], a[0:3], v[0:3]\n"
"ds_read_b64_tr_b16 a[240:241], v216 offset:0\n"
"v_mfma_f32_16x16x32_bf16 v[0:3], a[228:231], a[16:19], v[0:3]\n"
"ds_read_b64_tr_b16 a[242:243], v216 offset:4096\n"
"v_mfma_f32_16x16x32_bf16 v[8:11], a[228:231], a[20:23], v[8:11]\n"
"global_load_dword v228, v229, %13 offset:32 sc1\n"
"v_mfma_f32_16x16x32_bf16 v[8:11], a[224:227], a[4:7], v[8:11]\n"
"s_lshl_b32 s73, s69, 16\n"
"v_mfma_f32_16x16x32_bf16 v[16:19], a[224:227], a[8:11], v[16:19]\n"
"ds_read_b64_tr_b16 a[244:245], v216 offset:8192\n"
"s_add_u32 s73, s73, s75\n"
"v_mfma_f32_16x16x32_bf16 v[16:19], a[228:231], a[24:27], v[16:19]\n"
"ds_read_b64_tr_b16 a[246:247], v216 offset:12288\n"
"s_add_u32 m0, %15, 98304\n"
"v_mfma_f32_16x16x32_bf16 v[24:27], a[228:231], a[28:31], v[24:27]\n"
"buffer_load_dwordx4 v224, %9, s73 offen offset:0 lds\n"
"v_mfma_f32_16x16x32_bf16 v[24:27], a[224:227], a[12:15], v[24:27]\n"
"s_waitcnt lgkmcnt(4)\n"
"v_mfma_f32_16x16x32_bf16 v[28:31], a[232:235], a[12:15], v[28:31]\n"
"ds_read_b64_tr_b16 a[248:249], v217 offset:0\n"
"s_add_u32 s74, s74, 2\n"
"v_mfma_f32_16x16x32_bf16 v[28:31], a[236:239], a[28:31], v[28:31]\n"
"ds_read_b64_tr_b16 a[250:251], v217 offset:4096\n"
"v_readlane_b32 s69, %20, s74\n"
"v_mfma_f32_16x16x32_bf16 v[20:23], a[236:239], a[24:27], v[20:23]\n"
"buffer_load_dwordx4 v225, %9, s73 offen offset:1024 lds\n"
"v_mfma_f32_16x16x32_bf16 v[20:23], a[232:235], a[8:11], v[20:23]\n"
"v_mfma_f32_16x16x32_bf16 v[12:15], a[232:235], a[4:7], v[12:15]\n"
"ds_read_b64_tr_b16 a[252:253], v217 offset:8192\n"
"v_mfma_f32_16x16x32_bf16 v[12:15], a[236:239], a[20:23], v[12:15]\n"
"ds_read_b64_tr_b16 a[254:255], v217 offset:12288\n"
"v_mfma_f32_16x16x32_bf16 v[4:7], a[236:239], a[16:19], v[4:7]\n"
"buffer_load_dwordx4 v226, %9, s73 offen offset:2048 lds\n"
"v_mfma_f32_16x16x32_bf16 v[4:7], a[232:235], a[0:3], v[4:7]\n"
"s_waitcnt lgkmcnt(4)\n"
"v_mfma_f32_16x16x32_bf16 v[32:35], a[240:243], a[0:3], v[32:35]\n"
"ds_read_b64_tr_b16 a[224:225], v218 offset:0\n"
"v_mfma_f32_16x16x32_bf16 v[32:35], a[244:247], a[16:19], v[32:35]\n"
"ds_read_b64_tr_b16 a[226:227], v218 offset:4096\n"
"v_mfma_f32_16x16x32_bf16 v[40:43], a[244:247], a[20:23], v[40:43]\n"
"buffer_load_dwordx4 v227, %9, s73 offen offset:3072 lds\n"
"v_mfma_f32_16x16x32_bf16 v[40:43], a[240:243], a[4:7], v[40:43]\n"
"v_mfma_f32_16x16x32_bf16 v[48:51], a[240:243], a[8:11], v[48:51]\n"
"ds_read_b64_tr_b16 a[228:229], v218 offset:8192\n"
"v_mfma_f32_16x16x32_bf16 v[48:51], a[244:247], a[24:27], v[48:51]\n"
"ds_read_b64_tr_b16 a[230:231], v218 offset:12288\n"
"v_mfma_f32_16x16x32_bf16 v[56:59], a[244:247], a[28:31], v[56:59]\n"
"v_mfma_f32_16x16x32_bf16 v[56:59], a[240:243], a[12:15], v[56:59]\n"
"s_waitcnt lgkmcnt(4)\n"
"v_mfma_f32_16x16x32_bf16 v[60:63], a[248:251], a[12:15], v[60:63]\n"
"ds_read_b64_tr_b16 a[232:233], v219 offset:0\n"
"v_mfma_f32_16x16x32_bf16 v[60:63], a[252:255], a[28:31], v[60:63]\n"
"ds_read_b64_tr_b16 a[234:235], v219 offset:4096\n"
"s_lshl_b32 s65, s76, 11\n"
"v_mfma_f32_16x16x32_bf16 v[52:55], a[252:255], a[24:27], v[52:55]\n"
"buffer_load_dwordx4 a[96:99], v222, %8, s65 offen offset:0 sc1\n"
"v_mfma_f32_16x16x32_bf16 v[52:55], a[248:251], a[8:11], v[52:55]\n"
"v_mfma_f32_16x16x32_bf16 v[44:47], a[248:251], a[4:7], v[44:47]\n"
"ds_read_b64_tr_b16 a[236:237], v219 offset:8192\n"
"s_add_u32 s77, s77, 1\n"
"v_mfma_f32_16x16x32_bf16 v[44:47], a[252:255], a[20:23], v[44:47]\n"
"ds_read_b64_tr_b16 a[238:239], v219 offset:12288\n"
"v_readlane_b32 s76, %20, s77\n"
"v_mfma_f32_16x16x32_bf16 v[36:39], a[252:255], a[16:19], v[36:39]\n"
"buffer_load_dwordx4 a[100:103], v222, %8, s65 offen offset:1024 sc1\n"
"v_mfma_f32_16x16x32_bf16 v[36:39], a[248:251], a[0:3], v[36:39]\n"
"s_waitcnt lgkmcnt(4)\n"
"v_mfma_f32_16x16x32_bf16 v[64:67], a[224:227], a[0:3], v[64:67]\n"
"ds_read_b64_tr_b16 a[240:241], v220 offset:0\n"
"v_mfma_f32_16x16x32_bf16 v[64:67], a[228:231], a[16:19], v[64:67]\n"
"ds_read_b64_tr_b16 a[242:243], v220 offset:4096\n"
"v_mfma_f32_16x16x32_bf16 v[72:75], a[228:231], a[20:23], v[72:75]\n"
"buffer_load_dwordx4 a[104:107], v223, %8, s65 offen offset:0 sc1\n"
"v_mfma_f32_16x16x32_bf16 v[72:75], a[224:227], a[4:7], v[72:75]\n"
"v_mfma_f32_16x16x32_bf16 v[80:83], a[224:227], a[8:11], v[80:83]\n"
"ds_read_b64_tr_b16 a[244:245], v220 offset:8192\n"
"v_mfma_f32_16x16x32_bf16 v[80:83], a[228:231], a[24:27], v[80:83]\n"
"ds_read_b64_tr_b16 a[246:247], v220 offset:12288\n"
"v_mfma_f32_16x16x32_bf16 v[88:91], a[228:231], a[28:31], v[88:91]\n"
"buffer_load_dwordx4 a[108:111], v223, %8, s65 offen offset:1024 sc1\n"
"v_mfma_f32_16x16x32_bf16 v[88:91], a[224:227], a[12:15], v[88:91]\n"
"s_waitcnt lgkmcnt(4)\n"
"v_mfma_f32_16x16x32_bf16 v[92:95], a[232:235], a[12:15], v[92:95]\n"
"ds_read_b64_tr_b16 a[248:249], v221 offset:0\n"
"v_mfma_f32_16x16x32_bf16 v[92:95], a[236:239], a[28:31], v[92:95]\n"
"ds_read_b64_tr_b16 a[250:251], v221 offset:4096\n"
"v_mfma_f32_16x16x32_bf16 v[84:87], a[236:239], a[24:27], v[84:87]\n"
"v_mfma_f32_16x16x32_bf16 v[84:87], a[232:235], a[8:11], v[84:87]\n"
"v_mfma_f32_16x16x32_bf16 v[76:79], a[232:235], a[4:7], v[76:79]\n"
"ds_read_b64_tr_b16 a[252:253], v221 offset:8192\n"
"v_mfma_f32_16x16x32_bf16 v[76:79], a[236:239], a[20:23], v[76:79]\n"
"ds_read_b64_tr_b16 a[254:255], v221 offset:12288\n"
"s_lshl_b32 s65, s76, 11\n"
"v_mfma_f32_16x16x32_bf16 v[68:71], a[236:239], a[16:19], v[68:71]\n"
"buffer_load_dwordx4 a[112:115], v222, %8, s65 offen offset:0 sc1\n"
"v_mfma_f32_16x16x32_bf16 v[68:71], a[232:235], a[0:3], v[68:71]\n"
"s_waitcnt lgkmcnt(4)\n"
"v_mfma_f32_16x16x32_bf16 v[96:99], a[240:243], a[0:3], v[96:99]\n"
"s_waitcnt vmcnt(18)\n"
"s_barrier\n"
"ds_read_b64_tr_b16 a[224:225], v214 offset:16384\n"
"s_add_u32 s77, s77, 1\n"
"v_mfma_f32_16x16x32_bf16 v[96:99], a[244:247], a[16:19], v[96:99]\n"
"ds_read_b64_tr_b16 a[226:227], v214 offset:20480\n"
"v_readlane_b32 s76, %20, s77\n"
"v_mfma_f32_16x16x32_bf16 v[104:107], a[244:247], a[20:23], v[104:107]\n"
"buffer_load_dwordx4 a[116:119], v222, %8, s65 offen offset:1024 sc1\n"
"v_mfma_f32_16x16x32_bf16 v[104:107], a[240:243], a[4:7], v[104:107]\n"
"v_mfma_f32_16x16x32_bf16 v[112:115], a[240:243], a[8:11], v[112:115]\n"
"ds_read_b64_tr_b16 a[228:229], v214 offset:24576\n"
"v_mfma_f32_16x16x32_bf16 v[112:115], a[244:247], a[24:27], v[112:115]\n"
"ds_read_b64_tr_b16 a[230:231], v214 offset:28672\n"
"v_mfma_f32_16x16x32_bf16 v[120:123], a[244:247], a[28:31], v[120:123]\n"
"buffer_load_dwordx4 a[120:123], v223, %8, s65 offen offset:0 sc1\n"
"v_mfma_f32_16x16x32_bf16 v[120:123], a[240:243], a[12:15], v[120:123]\n"
"s_waitcnt lgkmcnt(4)\n"
"v_mfma_f32_16x16x32_bf16 v[124:127], a[248:251], a[12:15], v[124:127]\n"
"ds_read_b64_tr_b16 a[232:233], v215 offset:16384\n"
"v_mfma_f32_16x16x32_bf16 v[124:127], a[252:255], a[28:31], v[124:127]\n"
"ds_read_b64_tr_b16 a[234:235], v215 offset:20480\n"
"v_mfma_f32_16x16x32_bf16 v[116:119], a[252:255], a[24:27], v[116:119]\n"
"buffer_load_dwordx4 a[124:127], v223, %8, s65 offen offset:1024 sc1\n"
"v_mfma_f32_16x16x32_bf16 v[116:119], a[248:251], a[8:11], v[116:119]\n"
"v_mfma_f32_16x16x32_bf16 v[108:111], a[248:251], a[4:7], v[108:111]\n"
"ds_read_b64_tr_b16 a[236:237], v215 offset:24576\n"
"v_mfma_f32_16x16x32_bf16 v[108:111], a[252:255], a[20:23], v[108:111]\n"
"ds_read_b64_tr_b16 a[238:239], v215 offset:28672\n"
"v_mfma_f32_16x16x32_bf16 v[100:103], a[252:255], a[16:19], v[100:103]\n"
"s_waitcnt vmcnt(12)\n"
"v_cmp_eq_u32 vcc, 0, v228\n"
"v_mfma_f32_16x16x32_bf16 v[100:103], a[248:251], a[0:3], v[100:103]\n"
"s_cbranch_vccnz LGS%=\n"
"LGB%=:\n"
"s_waitcnt vmcnt(25) lgkmcnt(4)\n"
"v_mfma_f32_16x16x32_bf16 v[0:3], a[224:227], a[32:35], v[0:3]\n"
"ds_read_b64_tr_b16 a[240:241], v216 offset:16384\n"
"v_mfma_f32_16x16x32_bf16 v[0:3], a[228:231], a[48:51], v[0:3]\n"
"ds_read_b64_tr_b16 a[242:243], v216 offset:20480\n"
"v_mfma_f32_16x16x32_bf16 v[8:11], a[228:231], a[52:55], v[8:11]\n"
"v_mfma_f32_16x16x32_bf16 v[8:11], a[224:227], a[36:39], v[8:11]\n"
"s_lshl_b32 s73, s69, 16\n"
"v_mfma_f32_16x16x32_bf16 v[16:19], a[224:227], a[40:43], v[16:19]\n"
"ds_read_b64_tr_b16 a[244:245], v216 offset:24576\n"
"s_add_u32 s73, s73, s75\n"
"v_mfma_f32_16x16x32_bf16 v[16:19], a[228:231], a[56:59], v[16:19]\n"
"ds_read_b64_tr_b16 a[246:247], v216 offset:28672\n"
"s_add_u32 m0, %15, 114688\n"
"v_mfma_f32_16x16x32_bf16 v[24:27], a[228:231], a[60:63], v[24:27]\n"
"buffer_load_dwordx4 v224, %9, s73 offen offset:0 lds\n"
"v_mfma_f32_16x16x32_bf16 v[24:27], a[224:227], a[44:47], v[24:27]\n"
"s_waitcnt lgkmcnt(4)\n"
"v_mfma_f32_16x16x32_bf16 v[28:31], a[232:235], a[44:47], v[28:31]\n"
"ds_read_b64_tr_b16 a[248:249], v217 offset:16384\n"
"s_add_u32 s74, s74, 2\n"
"v_mfma_f32_16x16x32_bf16 v[28:31], a[236:239], a[60:63], v[28:31]\n"
"ds_read_b64_tr_b16 a[250:251], v217 offset:20480\n"
"v_readlane_b32 s69, %20, s74\n"
"v_mfma_f32_16x16x32_bf16 v[20:23], a[236:239], a[56:59], v[20:23]\n"
"buffer_load_dwordx4 v225, %9, s73 offen offset:1024 lds\n"
"v_mfma_f32_16x16x32_bf16 v[20:23], a[232:235], a[40:43], v[20:23]\n"
"v_mfma_f32_16x16x32_bf16 v[12:15], a[232:235], a[36:39], v[12:15]\n"
"ds_read_b64_tr_b16 a[252:253], v217 offset:24576\n"
"v_mfma_f32_16x16x32_bf16 v[12:15], a[236:239], a[52:55], v[12:15]\n"
"ds_read_b64_tr_b16 a[254:255], v217 offset:28672\n"
"v_mfma_f32_16x16x32_bf16 v[4:7], a[236:239], a[48:51], v[4:7]\n"
"buffer_load_dwordx4 v226, %9, s73 offen offset:2048 lds\n"
"v_mfma_f32_16x16x32_bf16 v[4:7], a[232:235], a[32:35], v[4:7]\n"
"s_waitcnt lgkmcnt(4)\n"
"v_mfma_f32_16x16x32_bf16 v[32:35], a[240:243], a[32:35], v[32:35]\n"
"ds_read_b64_tr_b16 a[224:225], v218 offset:16384\n"
"v_mfma_f32_16x16x32_bf16 v[32:35], a[244:247], a[48:51], v[32:35]\n"
"ds_read_b64_tr_b16 a[226:227], v218 offset:20480\n"
"v_mfma_f32_16x16x32_bf16 v[40:43], a[244:247], a[52:55], v[40:43]\n"
"buffer_load_dwordx4 v227, %9, s73 offen offset:3072 lds\n"
"v_mfma_f32_16x16x32_bf16 v[40:43], a[240:243], a[36:39], v[40:43]\n"
"v_mfma_f32_16x16x32_bf16 v[48:51], a[240:243], a[40:43], v[48:51]\n"
"ds_read_b64_tr_b16 a[228:229], v218 offset:24576\n"
"v_mfma_f32_16x16x32_bf16 v[48:51], a[244:247], a[56:59], v[48:51]\n"
"ds_read_b64_tr_b16 a[230:231], v218 offset:28672\n"
"v_mfma_f32_16x16x32_bf16 v[56:59], a[244:247], a[60:63], v[56:59]\n"
"v_mfma_f32_16x16x32_bf16 v[56:59], a[240:243], a[44:47], v[56:59]\n"
"s_waitcnt lgkmcnt(4)\n"
"v_mfma_f32_16x16x32_bf16 v[60:63], a[248:251], a[44:47], v[60:63]\n"
"ds_read_b64_tr_b16 a[232:233], v219 offset:16384\n"
"v_mfma_f32_16x16x32_bf16 v[60:63], a[252:255], a[60:63], v[60:63]\n"
"ds_read_b64_tr_b16 a[234:235], v219 offset:20480\n"
"s_lshl_b32 s65, s76, 11\n"
"v_mfma_f32_16x16x32_bf16 v[52:55], a[252:255], a[56:59], v[52:55]\n"
"buffer_load_dwordx4 a[0:3], v222, %8, s65 offen offset:0 sc1\n"
"v_mfma_f32_16x16x32_bf16 v[52:55], a[248:251], a[40:43], v[52:55]\n"
"v_mfma_f32_16x16x32_bf16 v[44:47], a[248:251], a[36:39], v[44:47]\n"
"ds_read_b64_tr_b16 a[236:237], v219 offset:24576\n"
"s_add_u32 s77, s77, 1\n"
"v_mfma_f32_16x16x32_bf16 v[44:47], a[252:255], a[52:55], v[44:47]\n"
"ds_read_b64_tr_b16 a[238:239], v219 offset:28672\n"
"v_readlane_b32 s76, %20, s77\n"
"v_mfma_f32_16x16x32_bf16 v[36:39], a[252:255], a[48:51], v[36:39]\n"
"buffer_load_dwordx4 a[4:7], v222, %8, s65 offen offset:1024 sc1\n"
"v_mfma_f32_16x16x32_bf16 v[36:39], a[248:251], a[32:35], v[36:39]\n"
"s_waitcnt lgkmcnt(4)\n"
"v_mfma_f32_16x16x32_bf16 v[64:67], a[224:227], a[32:35], v[64:67]\n"
"ds_read_b64_tr_b16 a[240:241], v220 offset:16384\n"
"v_mfma_f32_16x16x32_bf16 v[64:67], a[228:231], a[48:51], v[64:67]\n"
"ds_read_b64_tr_b16 a[242:243], v220 offset:20480\n"
"v_mfma_f32_16x16x32_bf16 v[72:75], a[228:231], a[52:55], v[72:75]\n"
"buffer_load_dwordx4 a[8:11], v223, %8, s65 offen offset:0 sc1\n"
"v_mfma_f32_16x16x32_bf16 v[72:75], a[224:227], a[36:39], v[72:75]\n"
"v_mfma_f32_16x16x32_bf16 v[80:83], a[224:227], a[40:43], v[80:83]\n"
"ds_read_b64_tr_b16 a[244:245], v220 offset:24576\n"
"v_mfma_f32_16x16x32_bf16 v[80:83], a[228:231], a[56:59], v[80:83]\n"
"ds_read_b64_tr_b16 a[246:247], v220 offset:28672\n"
"v_mfma_f32_16x16x32_bf16 v[88:91], a[228:231], a[60:63], v[88:91]\n"
"buffer_load_dwordx4 a[12:15], v223, %8, s65 offen offset:1024 sc1\n"
"v_mfma_f32_16x16x32_bf16 v[88:91], a[224:227], a[44:47], v[88:91]\n"
"s_waitcnt lgkmcnt(4)\n"
"v_mfma_f32_16x16x32_bf16 v[92:95], a[232:235], a[44:47], v[92:95]\n"
"ds_read_b64_tr_b16 a[248:249], v221 offset:16384\n"
"v_mfma_f32_16x16x32_bf16 v[92:95], a[236:239], a[60:63], v[92:95]\n"
"ds_read_b64_tr_b16 a[250:251], v221 offset:20480\n"
"v_mfma_f32_16x16x32_bf16 v[84:87], a[236:239], a[56:59], v[84:87]\n"
"v_mfma_f32_16x16x32_bf16 v[84:87], a[232:235], a[40:43], v[84:87]\n"
"v_mfma_f32_16x16x32_bf16 v[76:79], a[232:235], a[36:39], v[76:79]\n"
"ds_read_b64_tr_b16 a[252:253], v221 offset:24576\n"
"v_mfma_f32_16x16x32_bf16 v[76:79], a[236:239], a[52:55], v[76:79]\n"
"ds_read_b64_tr_b16 a[254:255], v221 offset:28672\n"
"s_lshl_b32 s65, s76, 11\n"
"v_mfma_f32_16x16x32_bf16 v[68:71], a[236:239], a[48:51], v[68:71]\n"
"buffer_load_dwordx4 a[16:19], v222, %8, s65 offen offset:0 sc1\n"
"v_mfma_f32_16x16x32_bf16 v[68:71], a[232:235], a[32:35], v[68:71]\n"
"s_waitcnt lgkmcnt(4)\n"
"v_mfma_f32_16x16x32_bf16 v[96:99], a[240:243], a[32:35], v[96:99]\n"
"s_waitcnt vmcnt(17)\n"
"s_barrier\n"
"ds_read_b64_tr_b16 a[224:225], v214 offset:32768\n"
"s_add_u32 s77, s77, 1\n"
"v_mfma_f32_16x16x32_bf16 v[96:99], a[244:247], a[48:51], v[96:99]\n"
"ds_read_b64_tr_b16 a[226:227], v214 offset:36864\n"
"v_readlane_b32 s76, %20, s77\n"
"v_mfma_f32_16x16x32_bf16 v[104:107], a[244:247], a[52:55], v[104:107]\n"
"buffer_load_dwordx4 a[20:23], v222, %8, s65 offen offset:1024 sc1\n"
"v_mfma_f32_16x16x32_bf16 v[104:107], a[240:243], a[36:39], v[104:107]\n"
"v_mfma_f32_16x16x32_bf16 v[112:115], a[240:243], a[40:43], v[112:115]\n"
"ds_read_b64_tr_b16 a[228:229], v214 offset:40960\n"
"v_mfma_f32_16x16x32_bf16 v[112:115], a[244:247], a[56:59], v[112:115]\n"
"ds_read_b64_tr_b16 a[230:231], v214 offset:45056\n"
"v_mfma_f32_16x16x32_bf16 v[120:123], a[244:247], a[60:63], v[120:123]\n"
"buffer_load_dwordx4 a[24:27], v223, %8, s65 offen offset:0 sc1\n"
"v_mfma_f32_16x16x32_bf16 v[120:123], a[240:243], a[44:47], v[120:123]\n"
"s_waitcnt lgkmcnt(4)\n"
"v_mfma_f32_16x16x32_bf16 v[124:127], a[248:251], a[44:47], v[124:127]\n"
"ds_read_b64_tr_b16 a[232:233], v215 offset:32768\n"
"v_mfma_f32_16x16x32_bf16 v[124:127], a[252:255], a[60:63], v[124:127]\n"
"ds_read_b64_tr_b16 a[234:235], v215 offset:36864\n"
"v_mfma_f32_16x16x32_bf16 v[116:119], a[252:255], a[56:59], v[116:119]\n"
"buffer_load_dwordx4 a[28:31], v223, %8, s65 offen offset:1024 sc1\n"
"v_mfma_f32_16x16x32_bf16 v[116:119], a[248:251], a[40:43], v[116:119]\n"
"s_sub_u32 s66, s66, 1\n"
"v_mfma_f32_16x16x32_bf16 v[108:111], a[248:251], a[36:39], v[108:111]\n"
"ds_read_b64_tr_b16 a[236:237], v215 offset:40960\n"
"v_mfma_f32_16x16x32_bf16 v[108:111], a[252:255], a[52:55], v[108:111]\n"
"ds_read_b64_tr_b16 a[238:239], v215 offset:45056\n"
"v_mfma_f32_16x16x32_bf16 v[100:103], a[252:255], a[48:51], v[100:103]\n"
"v_mfma_f32_16x16x32_bf16 v[100:103], a[248:251], a[32:35], v[100:103]\n"
"s_cmp_lg_u32 s66, 0\n"
"s_cbranch_scc1 LOOP_F%=\n"
"v_add_u32 v225, 8192, v225\n"
"v_add_u32 v226, 16384, v226\n"
"v_add_u32 v227, 24576, v227\n"
"s_lshr_b32 s75, s75, 2\n"
"s_waitcnt vmcnt(25) lgkmcnt(4)\n"
"v_mfma_f32_16x16x32_bf16 v[0:3], a[224:227], a[64:67], v[0:3]\n"
"ds_read_b64_tr_b16 a[240:241], v216 offset:32768\n"
"s_lshl_b32 s73, s69, 16\n"
"s_add_u32 s73, s73, s75\n"
"v_mfma_f32_16x16x32_bf16 v[0:3], a[228:231], a[80:83], v[0:3]\n"
"ds_read_b64_tr_b16 a[242:243], v216 offset:36864\n"
"s_add_u32 m0, %15, 65536\n"
"v_mfma_f32_16x16x32_bf16 v[8:11], a[228:231], a[84:87], v[8:11]\n"
"buffer_load_dwordx4 v224, %9, s73 offen offset:0 lds\n"
"v_mfma_f32_16x16x32_bf16 v[8:11], a[224:227], a[68:71], v[8:11]\n"
"s_barrier\n"
"v_mfma_f32_16x16x32_bf16 v[16:19], a[224:227], a[72:75], v[16:19]\n"
"ds_read_b64_tr_b16 a[244:245], v216 offset:40960\n"
"s_add_u32 s74, s74, 2\n"
"v_mfma_f32_16x16x32_bf16 v[16:19], a[228:231], a[88:91], v[16:19]\n"
"ds_read_b64_tr_b16 a[246:247], v216 offset:45056\n"
"v_readlane_b32 s69, %20, s74\n"
"v_mfma_f32_16x16x32_bf16 v[24:27], a[228:231], a[92:95], v[24:27]\n"
"buffer_load_dwordx4 v225, %9, s73 offen offset:1024 lds\n"
"v_mfma_f32_16x16x32_bf16 v[24:27], a[224:227], a[76:79], v[24:27]\n"
"s_waitcnt lgkmcnt(4)\n"
"v_mfma_f32_16x16x32_bf16 v[28:31], a[232:235], a[76:79], v[28:31]\n"
"ds_read_b64_tr_b16 a[248:249], v217 offset:32768\n"
"v_mfma_f32_16x16x32_bf16 v[28:31], a[236:239], a[92:95], v[28:31]\n"
"ds_read_b64_tr_b16 a[250:251], v217 offset:36864\n"
"v_mfma_f32_16x16x32_bf16 v[20:23], a[236:239], a[88:91], v[20:23]\n"
"buffer_load_dwordx4 v226, %9, s73 offen offset:2048 lds\n"
"v_mfma_f32_16x16x32_bf16 v[20:23], a[232:235], a[72:75], v[20:23]\n"
"v_mfma_f32_16x16x32_bf16 v[12:15], a[232:235], a[68:71], v[12:15]\n"
"ds_read_b64_tr_b16 a[252:253], v217 offset:40960\n"
"v_mfma_f32_16x16x32_bf16 v[12:15], a[236:239], a[84:87], v[12:15]\n"
"ds_read_b64_tr_b16 a[254:255], v217 offset:45056\n"
"v_mfma_f32_16x16x32_bf16 v[4:7], a[236:239], a[80:83], v[4:7]\n"
"buffer_load_dwordx4 v227, %9, s73 offen offset:3072 lds\n"
"v_mfma_f32_16x16x32_bf16 v[4:7], a[232:235], a[64:67], v[4:7]\n"
"s_lshl_b32 s73, s69, 16\n"
"s_waitcnt lgkmcnt(4)\n"
"v_mfma_f32_16x16x32_bf16 v[32:35], a[240:243], a[64:67], v[32:35]\n"
"ds_read_b64_tr_b16 a[224:225], v218 offset:32768\n"
"s_add_u32 s73, s73, s75\n"
"v_mfma_f32_16x16x32_bf16 v[32:35], a[244:247], a[80:83], v[32:35]\n"
"ds_read_b64_tr_b16 a[226:227], v218 offset:36864\n"
"s_add_u32 m0, %15, 81920\n"
"v_mfma_f32_16x16x32_bf16 v[40:43], a[244:247], a[84:87], v[40:43]\n"
"buffer_load_dwordx4 v224, %9, s73 offen offset:0 lds\n"
"v_mfma_f32_16x16x32_bf16 v[40:43], a[240:243], a[68:71], v[40:43]\n"
"v_mfma_f32_16x16x32_bf16 v[48:51], a[240:243], a[72:75], v[48:51]\n"
"ds_read_b64_tr_b16 a[228:229], v218 offset:40960\n"
"s_add_u32 s74, s74, 2\n"
"v_mfma_f32_16x16x32_bf16 v[48:51], a[244:247], a[88:91], v[48:51]\n"
"ds_read_b64_tr_b16 a[230:231], v218 offset:45056\n"
"v_readlane_b32 s69, %20, s74\n"
"v_mfma_f32_16x16x32_bf16 v[56:59], a[244:247], a[92:95], v[56:59]\n"
"buffer_load_dwordx4 v225, %9, s73 offen offset:1024 lds\n"
"v_mfma_f32_16x16x32_bf16 v[56:59], a[240:243], a[76:79], v[56:59]\n"
"s_waitcnt lgkmcnt(4)\n"
"v_mfma_f32_16x16x32_bf16 v[60:63], a[248:251], a[76:79], v[60:63]\n"
"ds_read_b64_tr_b16 a[232:233], v219 offset:32768\n"
"v_mfma_f32_16x16x32_bf16 v[60:63], a[252:255], a[92:95], v[60:63]\n"
"ds_read_b64_tr_b16 a[234:235], v219 offset:36864\n"
"v_mfma_f32_16x16x32_bf16 v[52:55], a[252:255], a[88:91], v[52:55]\n"
"buffer_load_dwordx4 v226, %9, s73 offen offset:2048 lds\n"
"v_mfma_f32_16x16x32_bf16 v[52:55], a[248:251], a[72:75], v[52:55]\n"
"v_mfma_f32_16x16x32_bf16 v[44:47], a[248:251], a[68:71], v[44:47]\n"
"ds_read_b64_tr_b16 a[236:237], v219 offset:40960\n"
"v_mfma_f32_16x16x32_bf16 v[44:47], a[252:255], a[84:87], v[44:47]\n"
"ds_read_b64_tr_b16 a[238:239], v219 offset:45056\n"
"v_mfma_f32_16x16x32_bf16 v[36:39], a[252:255], a[80:83], v[36:39]\n"
"buffer_load_dwordx4 v227, %9, s73 offen offset:3072 lds\n"
"v_mfma_f32_16x16x32_bf16 v[36:39], a[248:251], a[64:67], v[36:39]\n"
"s_waitcnt lgkmcnt(4)\n"
"v_mfma_f32_16x16x32_bf16 v[64:67], a[224:227], a[64:67], v[64:67]\n"
"s_waitcnt vmcnt(16)\n"
"s_barrier\n"
"ds_read_b64_tr_b16 a[240:241], v214 offset:49152\n"
"v_mfma_f32_16x16x32_bf16 v[64:67], a[228:231], a[80:83], v[64:67]\n"
"ds_read_b64_tr_b16 a[242:243], v214 offset:53248\n"
"s_lshl_b32 s65, s76, 11\n"
"v_mfma_f32_16x16x32_bf16 v[72:75], a[228:231], a[84:87], v[72:75]\n"
"buffer_load_dwordx4 a[32:35], v222, %8, s65 offen offset:0 sc1\n"
"v_mfma_f32_16x16x32_bf16 v[72:75], a[224:227], a[68:71], v[72:75]\n"
"v_mfma_f32_16x16x32_bf16 v[80:83], a[224:227], a[72:75], v[80:83]\n"
"ds_read_b64_tr_b16 a[244:245], v214 offset:57344\n"
"s_add_u32 s77, s77, 1\n"
"v_mfma_f32_16x16x32_bf16 v[80:83], a[228:231], a[88:91], v[80:83]\n"
"ds_read_b64_tr_b16 a[246:247], v214 offset:61440\n"
"v_readlane_b32 s76, %20, s77\n"
"v_mfma_f32_16x16x32_bf16 v[88:91], a[228:231], a[92:95], v[88:91]\n"
"buffer_load_dwordx4 a[36:39], v222, %8, s65 offen offset:1024 sc1\n"
"v_mfma_f32_16x16x32_bf16 v[88:91], a[224:227], a[76:79], v[88:91]\n"
"s_waitcnt lgkmcnt(4)\n"
"v_mfma_f32_16x16x32_bf16 v[92:95], a[232:235], a[76:79], v[92:95]\n"
"ds_read_b64_tr_b16 a[248:249], v215 offset:49152\n"
"v_mfma_f32_16x16x32_bf16 v[92:95], a[236:239], a[92:95], v[92:95]\n"
"ds_read_b64_tr_b16 a[250:251], v215 offset:53248\n"
"v_mfma_f32_16x16x32_bf16 v[84:87], a[236:239], a[88:91], v[84:87]\n"
"buffer_load_dwordx4 a[40:43], v223, %8, s65 offen offset:0 sc1\n"
"v_mfma_f32_16x16x32_bf16 v[84:87], a[232:235], a[72:75], v[84:87]\n"
"v_mfma_f32_16x16x32_bf16 v[76:79], a[232:235], a[68:71], v[76:79]\n"
"ds_read_b64_tr_b16 a[252:253], v215 offset:57344\n"
"v_mfma_f32_16x16x32_bf16 v[76:79], a[236:239], a[84:87], v[76:79]\n"
"ds_read_b64_tr_b16 a[254:255], v215 offset:61440\n"
"v_mfma_f32_16x16x32_bf16 v[68:71], a[236:239], a[80:83], v[68:71]\n"
"buffer_load_dwordx4 a[44:47], v223, %8, s65 offen offset:1024 sc1\n"
"v_mfma_f32_16x16x32_bf16 v[68:71], a[232:235], a[64:67], v[68:71]\n"
"s_waitcnt vmcnt(24) lgkmcnt(4)\n"
"v_mfma_f32_16x16x32_bf16 v[0:3], a[240:243], a[96:99], v[0:3]\n"
"ds_read_b64_tr_b16 a[224:225], v216 offset:49152\n"
"v_mfma_f32_16x16x32_bf16 v[0:3], a[244:247], a[112:115], v[0:3]\n"
"ds_read_b64_tr_b16 a[226:227], v216 offset:53248\n"
"s_lshl_b32 s65, s76, 11\n"
"v_mfma_f32_16x16x32_bf16 v[8:11], a[244:247], a[116:119], v[8:11]\n"
"buffer_load_dwordx4 a[48:51], v222, %8, s65 offen offset:0 sc1\n"
"v_mfma_f32_16x16x32_bf16 v[8:11], a[240:243], a[100:103], v[8:11]\n"
"v_mfma_f32_16x16x32_bf16 v[16:19], a[240:243], a[104:107], v[16:19]\n"
"ds_read_b64_tr_b16 a[228:229], v216 offset:57344\n"
"s_add_u32 s77, s77, 1\n"
"v_mfma_f32_16x16x32_bf16 v[16:19], a[244:247], a[120:123], v[16:19]\n"
"ds_read_b64_tr_b16 a[230:231], v216 offset:61440\n"
"v_readlane_b32 s76, %20, s77\n"
"v_mfma_f32_16x16x32_bf16 v[24:27], a[244:247], a[124:127], v[24:27]\n"
"buffer_load_dwordx4 a[52:55], v222, %8, s65 offen offset:1024 sc1\n"
"v_mfma_f32_16x16x32_bf16 v[24:27], a[240:243], a[108:111], v[24:27]\n"
"s_waitcnt lgkmcnt(4)\n"
"v_mfma_f32_16x16x32_bf16 v[28:31], a[248:251], a[108:111], v[28:31]\n"
"ds_read_b64_tr_b16 a[232:233], v217 offset:49152\n"
"v_mfma_f32_16x16x32_bf16 v[28:31], a[252:255], a[124:127], v[28:31]\n"
"ds_read_b64_tr_b16 a[234:235], v217 offset:53248\n"
"v_mfma_f32_16x16x32_bf16 v[20:23], a[252:255], a[120:123], v[20:23]\n"
"buffer_load_dwordx4 a[56:59], v223, %8, s65 offen offset:0 sc1\n"
"v_mfma_f32_16x16x32_bf16 v[20:23], a[248:251], a[104:107], v[20:23]\n"
"v_mfma_f32_16x16x32_bf16 v[12:15], a[248:251], a[100:103], v[12:15]\n"
"ds_read_b64_tr_b16 a[236:237], v217 offset:57344\n"
"v_mfma_f32_16x16x32_bf16 v[12:15], a[252:255], a[116:119], v[12:15]\n"
"ds_read_b64_tr_b16 a[238:239], v217 offset:61440\n"
"v_mfma_f32_16x16x32_bf16 v[4:7], a[252:255], a[112:115], v[4:7]\n"
"buffer_load_dwordx4 a[60:63], v223, %8, s65 offen offset:1024 sc1\n"
"v_mfma_f32_16x16x32_bf16 v[4:7], a[248:251], a[96:99], v[4:7]\n"
"s_waitcnt lgkmcnt(4)\n"
"v_mfma_f32_16x16x32_bf16 v[32:35], a[224:227], a[96:99], v[32:35]\n"
"s_waitcnt vmcnt(12)\n"
"s_barrier\n"
"ds_read_b64_tr_b16 a[240:241], v214 offset:0\n"
"v_mfma_f32_16x16x32_bf16 v[32:35], a[228:231], a[112:115], v[32:35]\n"
"ds_read_b64_tr_b16 a[242:243], v214 offset:4096\n"
"v_mfma_f32_16x16x32_bf16 v[40:43], a[228:231], a[116:119], v[40:43]\n"
"v_mfma_f32_16x16x32_bf16 v[40:43], a[224:227], a[100:103], v[40:43]\n"
"v_add_u32 v223, 0x10000, %24\n"
"v_mfma_f32_16x16x32_bf16 v[48:51], a[224:227], a[104:107], v[48:51]\n"
"ds_read_b64_tr_b16 a[244:245], v214 offset:8192\n"
"v_mfma_f32_16x16x32_bf16 v[48:51], a[228:231], a[120:123], v[48:51]\n"
"ds_read_b64_tr_b16 a[246:247], v214 offset:12288\n"
"v_mfma_f32_16x16x32_bf16 v[56:59], a[228:231], a[124:127], v[56:59]\n"
"v_mfma_f32_16x16x32_bf16 v[56:59], a[224:227], a[108:111], v[56:59]\n"
"s_xor_b32 s70, %19, 0\n"
"s_waitcnt lgkmcnt(4)\n"
"v_mfma_f32_16x16x32_bf16 v[60:63], a[232:235], a[108:111], v[60:63]\n"
"ds_read_b64_tr_b16 a[248:249], v215 offset:0\n"
"v_mfma_f32_16x16x32_bf16 v[60:63], a[236:239], a[124:127], v[60:63]\n"
"ds_read_b64_tr_b16 a[250:251], v215 offset:4096\n"
"v_mfma_f32_16x16x32_bf16 v[52:55], a[236:239], a[120:123], v[52:55]\n"
"v_mfma_f32_16x16x32_bf16 v[52:55], a[232:235], a[104:107], v[52:55]\n"
"s_xor_b32 s71, %19, 2048\n"
"v_mfma_f32_16x16x32_bf16 v[44:47], a[232:235], a[100:103], v[44:47]\n"
"ds_read_b64_tr_b16 a[252:253], v215 offset:8192\n"
"v_mfma_f32_16x16x32_bf16 v[44:47], a[236:239], a[116:119], v[44:47]\n"
"ds_read_b64_tr_b16 a[254:255], v215 offset:12288\n"
"v_mfma_f32_16x16x32_bf16 v[36:39], a[236:239], a[112:115], v[36:39]\n"
"v_mfma_f32_16x16x32_bf16 v[36:39], a[232:235], a[96:99], v[36:39]\n"
"s_xor_b32 s72, %19, 4096\n"
"s_waitcnt vmcnt(16) lgkmcnt(4)\n"
"v_mfma_f32_16x16x32_bf16 v[0:3], a[240:243], a[0:3], v[0:3]\n"
"s_waitcnt vmcnt(8)\n"
"s_barrier\n"
"ds_read_b64_tr_b16 a[224:225], v214 offset:16384\n"
"v_mfma_f32_16x16x32_bf16 v[0:3], a[244:247], a[16:19], v[0:3]\n"
"ds_read_b64_tr_b16 a[226:227], v214 offset:20480\n"
"v_mfma_f32_16x16x32_bf16 v[8:11], a[244:247], a[20:23], v[8:11]\n"
"v_mfma_f32_16x16x32_bf16 v[8:11], a[240:243], a[4:7], v[8:11]\n"
"v_mfma_f32_16x16x32_bf16 v[16:19], a[240:243], a[8:11], v[16:19]\n"
"ds_read_b64_tr_b16 a[228:229], v214 offset:24576\n"
"v_mfma_f32_16x16x32_bf16 v[16:19], a[244:247], a[24:27], v[16:19]\n"
"ds_read_b64_tr_b16 a[230:231], v214 offset:28672\n"
"v_mfma_f32_16x16x32_bf16 v[24:27], a[244:247], a[28:31], v[24:27]\n"
"buffer_load_dwordx4 v[128:131], v222, %11, 0 offen offset:0 sc1\n"
"v_mfma_f32_16x16x32_bf16 v[24:27], a[240:243], a[12:15], v[24:27]\n"
"s_waitcnt lgkmcnt(4)\n"
"v_mfma_f32_16x16x32_bf16 v[28:31], a[248:251], a[12:15], v[28:31]\n"
"ds_read_b64_tr_b16 a[232:233], v215 offset:16384\n"
"v_mfma_f32_16x16x32_bf16 v[28:31], a[252:255], a[28:31], v[28:31]\n"
"ds_read_b64_tr_b16 a[234:235], v215 offset:20480\n"
"v_mfma_f32_16x16x32_bf16 v[20:23], a[252:255], a[24:27], v[20:23]\n"
"buffer_load_dwordx4 v[132:135], v222, %11, 0 offen offset:1024 sc1\n"
"v_mfma_f32_16x16x32_bf16 v[20:23], a[248:251], a[8:11], v[20:23]\n"
"v_mfma_f32_16x16x32_bf16 v[12:15], a[248:251], a[4:7], v[12:15]\n"
"ds_read_b64_tr_b16 a[236:237], v215 offset:24576\n"
"v_mfma_f32_16x16x32_bf16 v[12:15], a[252:255], a[20:23], v[12:15]\n"
"ds_read_b64_tr_b16 a[238:239], v215 offset:28672\n"
"v_mfma_f32_16x16x32_bf16 v[4:7], a[252:255], a[16:19], v[4:7]\n"
"buffer_load_dwordx4 v[136:139], v223, %11, 0 offen offset:0 sc1\n"
"v_mfma_f32_16x16x32_bf16 v[4:7], a[248:251], a[0:3], v[4:7]\n"
"s_waitcnt vmcnt(3) lgkmcnt(4)\n"
"v_mfma_f32_16x16x32_bf16 v[0:3], a[224:227], a[32:35], v[0:3]\n"
"ds_read_b64_tr_b16 a[240:241], v216 offset:0\n"
"v_mfma_f32_16x16x32_bf16 v[0:3], a[228:231], a[48:51], v[0:3]\n"
"ds_read_b64_tr_b16 a[242:243], v216 offset:4096\n"
"v_mfma_f32_16x16x32_bf16 v[8:11], a[228:231], a[52:55], v[8:11]\n"
"buffer_load_dwordx4 v[140:143], v223, %11, 0 offen offset:1024 sc1\n"
"v_mfma_f32_16x16x32_bf16 v[8:11], a[224:227], a[36:39], v[8:11]\n"
"v_mfma_f32_16x16x32_bf16 v[16:19], a[224:227], a[40:43], v[16:19]\n"
"ds_read_b64_tr_b16 a[244:245], v216 offset:8192\n"
"v_mfma_f32_16x16x32_bf16 v[16:19], a[228:231], a[56:59], v[16:19]\n"
"ds_read_b64_tr_b16 a[246:247], v216 offset:12288\n"
"v_mfma_f32_16x16x32_bf16 v[24:27], a[228:231], a[60:63], v[24:27]\n"
"v_mfma_f32_16x16x32_bf16 v[24:27], a[224:227], a[44:47], v[24:27]\n"
"s_waitcnt lgkmcnt(4)\n"
"v_mfma_f32_16x16x32_bf16 v[28:31], a[232:235], a[44:47], v[28:31]\n"
"ds_read_b64_tr_b16 a[248:249], v217 offset:0\n"
"v_mfma_f32_16x16x32_bf16 v[28:31], a[236:239], a[60:63], v[28:31]\n"
"ds_read_b64_tr_b16 a[250:251], v217 offset:4096\n"
"v_mfma_f32_16x16x32_bf16 v[20:23], a[236:239], a[56:59], v[20:23]\n"
"v_mfma_f32_16x16x32_bf16 v[20:23], a[232:235], a[40:43], v[20:23]\n"
"v_mfma_f32_16x16x32_bf16 v[12:15], a[232:235], a[36:39], v[12:15]\n"
"ds_read_b64_tr_b16 a[252:253], v217 offset:8192\n"
"v_mfma_f32_16x16x32_bf16 v[12:15], a[236:239], a[52:55], v[12:15]\n"
"ds_read_b64_tr_b16 a[254:255], v217 offset:12288\n"
"v_mfma_f32_16x16x32_bf16 v[4:7], a[236:239], a[48:51], v[4:7]\n"
"v_mfma_f32_16x16x32_bf16 v[4:7], a[232:235], a[32:35], v[4:7]\n"
"s_waitcnt lgkmcnt(4)\n"
"v_mfma_f32_16x16x32_bf16 v[32:35], a[240:243], a[0:3], v[32:35]\n"
"ds_read_b64_tr_b16 a[224:225], v216 offset:16384\n"
"v_mfma_f32_16x16x32_bf16 v[32:35], a[244:247], a[16:19], v[32:35]\n"
"ds_read_b64_tr_b16 a[226:227], v216 offset:20480\n"
"v_mfma_f32_16x16x32_bf16 v[40:43], a[244:247], a[20:23], v[40:43]\n"
"v_mfma_f32_16x16x32_bf16 v[40:43], a[240:243], a[4:7], v[40:43]\n"
"v_mfma_f32_16x16x32_bf16 v[48:51], a[240:243], a[8:11], v[48:51]\n"
"ds_read_b64_tr_b16 a[228:229], v216 offset:24576\n"
"v_mfma_f32_16x16x32_bf16 v[48:51], a[244:247], a[24:27], v[48:51]\n"
"ds_read_b64_tr_b16 a[230:231], v216 offset:28672\n"
"v_mfma_f32_16x16x32_bf16 v[56:59], a[244:247], a[28:31], v[56:59]\n"
"v_cvt_pk_bf16_f32 v24, v24, v25\n"
"v_cvt_pk_bf16_f32 v25, v26, v27\n"
"v_mfma_f32_16x16x32_bf16 v[56:59], a[240:243], a[12:15], v[56:59]\n"
"v_cvt_pk_bf16_f32 v26, v28, v29\n"
"v_cvt_pk_bf16_f32 v27, v30, v31\n"
"s_waitcnt lgkmcnt(4)\n"
"v_mfma_f32_16x16x32_bf16 v[60:63], a[248:251], a[12:15], v[60:63]\n"
"ds_read_b64_tr_b16 a[232:233], v217 offset:16384\n"
"v_mfma_f32_16x16x32_bf16 v[60:63], a[252:255], a[28:31], v[60:63]\n"
"ds_read_b64_tr_b16 a[234:235], v217 offset:20480\n"
"v_mfma_f32_16x16x32_bf16 v[52:55], a[252:255], a[24:27], v[52:55]\n"
"buffer_store_dwordx4 v[24:27], v223, %10, s70 offen offset:1024 sc1\n"
"v_mfma_f32_16x16x32_bf16 v[52:55], a[248:251], a[8:11], v[52:55]\n"
"v_mfma_f32_16x16x32_bf16 v[44:47], a[248:251], a[4:7], v[44:47]\n"
"ds_read_b64_tr_b16 a[236:237], v217 offset:24576\n"
"v_mfma_f32_16x16x32_bf16 v[44:47], a[252:255], a[20:23], v[44:47]\n"
"ds_read_b64_tr_b16 a[238:239], v217 offset:28672\n"
"v_mfma_f32_16x16x32_bf16 v[36:39], a[252:255], a[16:19], v[36:39]\n"
"v_cvt_pk_bf16_f32 v16, v16, v17\n"
"v_cvt_pk_bf16_f32 v17, v18, v19\n"
"v_mfma_f32_16x16x32_bf16 v[36:39], a[248:251], a[0:3], v[36:39]\n"
"v_cvt_pk_bf16_f32 v18, v20, v21\n"
"v_cvt_pk_bf16_f32 v19, v22, v23\n"
"s_waitcnt lgkmcnt(4)\n"
"v_mfma_f32_16x16x32_bf16 v[32:35], a[224:227], a[32:35], v[32:35]\n"
"ds_read_b64_tr_b16 a[240:241], v218 offset:49152\n"
"v_mfma_f32_16x16x32_bf16 v[32:35], a[228:231], a[48:51], v[32:35]\n"
"ds_read_b64_tr_b16 a[242:243], v218 offset:53248\n"
"v_mfma_f32_16x16x32_bf16 v[40:43], a[228:231], a[52:55], v[40:43]\n"
"buffer_store_dwordx4 v[16:19], v223, %10, s70 offen offset:0 sc1\n"
"v_mfma_f32_16x16x32_bf16 v[40:43], a[224:227], a[36:39], v[40:43]\n"
"v_mfma_f32_16x16x32_bf16 v[48:51], a[224:227], a[40:43], v[48:51]\n"
"ds_read_b64_tr_b16 a[244:245], v218 offset:57344\n"
"v_mfma_f32_16x16x32_bf16 v[48:51], a[228:231], a[56:59], v[48:51]\n"
"ds_read_b64_tr_b16 a[246:247], v218 offset:61440\n"
"v_mfma_f32_16x16x32_bf16 v[56:59], a[228:231], a[60:63], v[56:59]\n"
"v_cvt_pk_bf16_f32 v8, v8, v9\n"
"v_cvt_pk_bf16_f32 v9, v10, v11\n"
"v_mfma_f32_16x16x32_bf16 v[56:59], a[224:227], a[44:47], v[56:59]\n"
"v_cvt_pk_bf16_f32 v10, v12, v13\n"
"v_cvt_pk_bf16_f32 v11, v14, v15\n"
"s_waitcnt lgkmcnt(4)\n"
"v_mfma_f32_16x16x32_bf16 v[60:63], a[232:235], a[44:47], v[60:63]\n"
"ds_read_b64_tr_b16 a[248:249], v219 offset:49152\n"
"v_mfma_f32_16x16x32_bf16 v[60:63], a[236:239], a[60:63], v[60:63]\n"
"ds_read_b64_tr_b16 a[250:251], v219 offset:53248\n"
"v_mfma_f32_16x16x32_bf16 v[52:55], a[236:239], a[56:59], v[52:55]\n"
"buffer_store_dwordx4 v[8:11], v222, %10, s70 offen offset:1024 sc1\n"
"v_mfma_f32_16x16x32_bf16 v[52:55], a[232:235], a[40:43], v[52:55]\n"
"v_mfma_f32_16x16x32_bf16 v[44:47], a[232:235], a[36:39], v[44:47]\n"
"ds_read_b64_tr_b16 a[252:253], v219 offset:57344\n"
"v_mfma_f32_16x16x32_bf16 v[44:47], a[236:239], a[52:55], v[44:47]\n"
"ds_read_b64_tr_b16 a[254:255], v219 offset:61440\n"
"v_mfma_f32_16x16x32_bf16 v[36:39], a[236:239], a[48:51], v[36:39]\n"
"v_cvt_pk_bf16_f32 v0, v0, v1\n"
"v_cvt_pk_bf16_f32 v1, v2, v3\n"
"v_mfma_f32_16x16x32_bf16 v[36:39], a[232:235], a[32:35], v[36:39]\n"
"v_cvt_pk_bf16_f32 v2, v4, v5\n"
"v_cvt_pk_bf16_f32 v3, v6, v7\n"
"s_waitcnt lgkmcnt(4)\n"
"v_mfma_f32_16x16x32_bf16 v[64:67], a[240:243], a[96:99], v[64:67]\n"
"ds_read_b64_tr_b16 a[224:225], v218 offset:0\n"
"v_mfma_f32_16x16x32_bf16 v[64:67], a[244:247], a[112:115], v[64:67]\n"
"ds_read_b64_tr_b16 a[226:227], v218 offset:4096\n"
"v_mfma_f32_16x16x32_bf16 v[72:75], a[244:247], a[116:119], v[72:75]\n"
"buffer_store_dwordx4 v[0:3], v222, %10, s70 offen offset:0 sc1\n"
"v_mfma_f32_16x16x32_bf16 v[72:75], a[240:243], a[100:103], v[72:75]\n"
"v_mfma_f32_16x16x32_bf16 v[80:83], a[240:243], a[104:107], v[80:83]\n"
"ds_read_b64_tr_b16 a[228:229], v218 offset:8192\n"
"v_mfma_f32_16x16x32_bf16 v[80:83], a[244:247], a[120:123], v[80:83]\n"
"ds_read_b64_tr_b16 a[230:231], v218 offset:12288\n"
"v_mfma_f32_16x16x32_bf16 v[88:91], a[244:247], a[124:127], v[88:91]\n"
"v_cvt_pk_bf16_f32 v56, v56, v57\n"
"v_cvt_pk_bf16_f32 v57, v58, v59\n"
"v_mfma_f32_16x16x32_bf16 v[88:91], a[240:243], a[108:111], v[88:91]\n"
"v_cvt_pk_bf16_f32 v58, v60, v61\n"
"v_cvt_pk_bf16_f32 v59, v62, v63\n"
"s_waitcnt lgkmcnt(4)\n"
"v_mfma_f32_16x16x32_bf16 v[92:95], a[248:251], a[108:111], v[92:95]\n"
"ds_read_b64_tr_b16 a[232:233], v219 offset:0\n"
"v_mfma_f32_16x16x32_bf16 v[92:95], a[252:255], a[124:127], v[92:95]\n"
"ds_read_b64_tr_b16 a[234:235], v219 offset:4096\n"
"v_mfma_f32_16x16x32_bf16 v[84:87], a[252:255], a[120:123], v[84:87]\n"
"buffer_store_dwordx4 v[56:59], v223, %10, s71 offen offset:1024 sc1\n"
"v_mfma_f32_16x16x32_bf16 v[84:87], a[248:251], a[104:107], v[84:87]\n"
"v_mfma_f32_16x16x32_bf16 v[76:79], a[248:251], a[100:103], v[76:79]\n"
"ds_read_b64_tr_b16 a[236:237], v219 offset:8192\n"
"v_mfma_f32_16x16x32_bf16 v[76:79], a[252:255], a[116:119], v[76:79]\n"
"ds_read_b64_tr_b16 a[238:239], v219 offset:12288\n"
"v_mfma_f32_16x16x32_bf16 v[68:71], a[252:255], a[112:115], v[68:71]\n"
"v_mfma_f32_16x16x32_bf16 v[68:71], a[248:251], a[96:99], v[68:71]\n"
"s_waitcnt lgkmcnt(4)\n"
"v_mfma_f32_16x16x32_bf16 v[64:67], a[224:227], a[0:3], v[64:67]\n"
"ds_read_b64_tr_b16 a[240:241], v218 offset:16384\n"
"v_mfma_f32_16x16x32_bf16 v[64:67], a[228:231], a[16:19], v[64:67]\n"
"ds_read_b64_tr_b16 a[242:243], v218 offset:20480\n"
"v_mfma_f32_16x16x32_bf16 v[72:75], a[228:231], a[20:23], v[72:75]\n"
"v_cvt_pk_bf16_f32 v48, v48, v49\n"
"v_cvt_pk_bf16_f32 v49, v50, v51\n"
"v_mfma_f32_16x16x32_bf16 v[72:75], a[224:227], a[4:7], v[72:75]\n"
"v_cvt_pk_bf16_f32 v50, v52, v53\n"
"v_cvt_pk_bf16_f32 v51, v54, v55\n"
"v_mfma_f32_16x16x32_bf16 v[80:83], a[224:227], a[8:11], v[80:83]\n"
"ds_read_b64_tr_b16 a[244:245], v218 offset:24576\n"
"v_mfma_f32_16x16x32_bf16 v[80:83], a[228:231], a[24:27], v[80:83]\n"
"ds_read_b64_tr_b16 a[246:247], v218 offset:28672\n"
"v_mfma_f32_16x16x32_bf16 v[88:91], a[228:231], a[28:31], v[88:91]\n"
"buffer_store_dwordx4 v[48:51], v223, %10, s71 offen offset:0 sc1\n"
"v_mfma_f32_16x16x32_bf16 v[88:91], a[224:227], a[12:15], v[88:91]\n"
"s_waitcnt lgkmcnt(4)\n"
"v_mfma_f32_16x16x32_bf16 v[92:95], a[232:235], a[12:15], v[92:95]\n"
"ds_read_b64_tr_b16 a[248:249], v219 offset:16384\n"
"v_mfma_f32_16x16x32_bf16 v[92:95], a[236:239], a[28:31], v[92:95]\n"
"ds_read_b64_tr_b16 a[250:251], v219 offset:20480\n"
"v_mfma_f32_16x16x32_bf16 v[84:87], a[236:239], a[24:27], v[84:87]\n"
"v_mfma_f32_16x16x32_bf16 v[84:87], a[232:235], a[8:11], v[84:87]\n"
"v_mfma_f32_16x16x32_bf16 v[76:79], a[232:235], a[4:7], v[76:79]\n"
"ds_read_b64_tr_b16 a[252:253], v219 offset:24576\n"
"v_mfma_f32_16x16x32_bf16 v[76:79], a[236:239], a[20:23], v[76:79]\n"
"ds_read_b64_tr_b16 a[254:255], v219 offset:28672\n"
"v_mfma_f32_16x16x32_bf16 v[68:71], a[236:239], a[16:19], v[68:71]\n"
"v_cvt_pk_bf16_f32 v40, v40, v41\n"
"v_cvt_pk_bf16_f32 v41, v42, v43\n"
"v_mfma_f32_16x16x32_bf16 v[68:71], a[232:235], a[0:3], v[68:71]\n"
"v_cvt_pk_bf16_f32 v42, v44, v45\n"
"v_cvt_pk_bf16_f32 v43, v46, v47\n"
"s_waitcnt lgkmcnt(4)\n"
"v_mfma_f32_16x16x32_bf16 v[64:67], a[240:243], a[32:35], v[64:67]\n"
"ds_read_b64_tr_b16 a[224:225], v220 offset:32768\n"
"v_mfma_f32_16x16x32_bf16 v[64:67], a[244:247], a[48:51], v[64:67]\n"
"ds_read_b64_tr_b16 a[226:227], v220 offset:36864\n"
"v_mfma_f32_16x16x32_bf16 v[72:75], a[244:247], a[52:55], v[72:75]\n"
"buffer_store_dwordx4 v[40:43], v222, %10, s71 offen offset:1024 sc1\n"
"v_mfma_f32_16x16x32_bf16 v[72:75], a[240:243], a[36:39], v[72:75]\n"
"v_mfma_f32_16x16x32_bf16 v[80:83], a[240:243], a[40:43], v[80:83]\n"
"ds_read_b64_tr_b16 a[228:229], v220 offset:40960\n"
"v_mfma_f32_16x16x32_bf16 v[80:83], a[244:247], a[56:59], v[80:83]\n"
"ds_read_b64_tr_b16 a[230:231], v220 offset:45056\n"
"v_mfma_f32_16x16x32_bf16 v[88:91], a[244:247], a[60:63], v[88:91]\n"
"v_mfma_f32_16x16x32_bf16 v[88:91], a[240:243], a[44:47], v[88:91]\n"
"s_waitcnt lgkmcnt(4)\n"
"v_mfma_f32_16x16x32_bf16 v[92:95], a[248:251], a[44:47], v[92:95]\n"
"ds_read_b64_tr_b16 a[232:233], v221 offset:32768\n"
"v_mfma_f32_16x16x32_bf16 v[92:95], a[252:255], a[60:63], v[92:95]\n"
"ds_read_b64_tr_b16 a[234:235], v221 offset:36864\n"
"v_mfma_f32_16x16x32_bf16 v[84:87], a[252:255], a[56:59], v[84:87]\n"
"v_cvt_pk_bf16_f32 v32, v32, v33\n"
"v_cvt_pk_bf16_f32 v33, v34, v35\n"
"v_mfma_f32_16x16x32_bf16 v[84:87], a[248:251], a[40:43], v[84:87]\n"
"v_cvt_pk_bf16_f32 v34, v36, v37\n"
"v_cvt_pk_bf16_f32 v35, v38, v39\n"
"v_mfma_f32_16x16x32_bf16 v[76:79], a[248:251], a[36:39], v[76:79]\n"
"ds_read_b64_tr_b16 a[236:237], v221 offset:40960\n"
"v_mfma_f32_16x16x32_bf16 v[76:79], a[252:255], a[52:55], v[76:79]\n"
"ds_read_b64_tr_b16 a[238:239], v221 offset:45056\n"
"v_mfma_f32_16x16x32_bf16 v[68:71], a[252:255], a[48:51], v[68:71]\n"
"buffer_store_dwordx4 v[32:35], v222, %10, s71 offen offset:0 sc1\n"
"v_mfma_f32_16x16x32_bf16 v[68:71], a[248:251], a[32:35], v[68:71]\n"
"s_waitcnt lgkmcnt(4)\n"
"v_mfma_f32_16x16x32_bf16 v[96:99], a[224:227], a[64:67], v[96:99]\n"
"ds_read_b64_tr_b16 a[240:241], v220 offset:49152\n"
"v_mfma_f32_16x16x32_bf16 v[96:99], a[228:231], a[80:83], v[96:99]\n"
"ds_read_b64_tr_b16 a[242:243], v220 offset:53248\n"
"v_mfma_f32_16x16x32_bf16 v[104:107], a[228:231], a[84:87], v[104:107]\n"
"v_mfma_f32_16x16x32_bf16 v[104:107], a[224:227], a[68:71], v[104:107]\n"
"v_mfma_f32_16x16x32_bf16 v[112:115], a[224:227], a[72:75], v[112:115]\n"
"ds_read_b64_tr_b16 a[244:245], v220 offset:57344\n"
"v_mfma_f32_16x16x32_bf16 v[112:115], a[228:231], a[88:91], v[112:115]\n"
"ds_read_b64_tr_b16 a[246:247], v220 offset:61440\n"
"v_mfma_f32_16x16x32_bf16 v[120:123], a[228:231], a[92:95], v[120:123]\n"
"v_cvt_pk_bf16_f32 v88, v88, v89\n"
"v_cvt_pk_bf16_f32 v89, v90, v91\n"
"v_mfma_f32_16x16x32_bf16 v[120:123], a[224:227], a[76:79], v[120:123]\n"
"v_cvt_pk_bf16_f32 v90, v92, v93\n"
"v_cvt_pk_bf16_f32 v91, v94, v95\n"
"s_waitcnt lgkmcnt(4)\n"
"v_mfma_f32_16x16x32_bf16 v[124:127], a[232:235], a[76:79], v[124:127]\n"
"ds_read_b64_tr_b16 a[248:249], v221 offset:49152\n"
"v_mfma_f32_16x16x32_bf16 v[124:127], a[236:239], a[92:95], v[124:127]\n"
"ds_read_b64_tr_b16 a[250:251], v221 offset:53248\n"
"v_mfma_f32_16x16x32_bf16 v[116:119], a[236:239], a[88:91], v[116:119]\n"
"buffer_store_dwordx4 v[88:91], v223, %10, s72 offen offset:1024 sc1\n"
"v_mfma_f32_16x16x32_bf16 v[116:119], a[232:235], a[72:75], v[116:119]\n"
"v_cvt_pk_bf16_f32 v80, v80, v81\n"
"v_cvt_pk_bf16_f32 v81, v82, v83\n"
"v_mfma_f32_16x16x32_bf16 v[108:111], a[232:235], a[68:71], v[108:111]\n"
"ds_read_b64_tr_b16 a[252:253], v221 offset:57344\n"
"v_mfma_f32_16x16x32_bf16 v[108:111], a[236:239], a[84:87], v[108:111]\n"
"ds_read_b64_tr_b16 a[254:255], v221 offset:61440\n"
"v_mfma_f32_16x16x32_bf16 v[100:103], a[236:239], a[80:83], v[100:103]\n"
"v_cvt_pk_bf16_f32 v82, v84, v85\n"
"v_cvt_pk_bf16_f32 v83, v86, v87\n"
"v_mfma_f32_16x16x32_bf16 v[100:103], a[232:235], a[64:67], v[100:103]\n"
"buffer_store_dwordx4 v[80:83], v223, %10, s72 offen offset:0 sc1\n"
"s_waitcnt lgkmcnt(4)\n"
"v_mfma_f32_16x16x32_bf16 v[96:99], a[240:243], a[96:99], v[96:99]\n"
"ds_read_b64_tr_b16 a[224:225], v220 offset:0\n"
"v_mfma_f32_16x16x32_bf16 v[96:99], a[244:247], a[112:115], v[96:99]\n"
"ds_read_b64_tr_b16 a[226:227], v220 offset:4096\n"
"v_mfma_f32_16x16x32_bf16 v[104:107], a[244:247], a[116:119], v[104:107]\n"
"v_cvt_pk_bf16_f32 v72, v72, v73\n"
"v_cvt_pk_bf16_f32 v73, v74, v75\n"
"v_mfma_f32_16x16x32_bf16 v[104:107], a[240:243], a[100:103], v[104:107]\n"
"v_cvt_pk_bf16_f32 v74, v76, v77\n"
"v_cvt_pk_bf16_f32 v75, v78, v79\n"
"v_mfma_f32_16x16x32_bf16 v[112:115], a[240:243], a[104:107], v[112:115]\n"
"ds_read_b64_tr_b16 a[228:229], v220 offset:8192\n"
"v_mfma_f32_16x16x32_bf16 v[112:115], a[244:247], a[120:123], v[112:115]\n"
"ds_read_b64_tr_b16 a[230:231], v220 offset:12288\n"
"v_mfma_f32_16x16x32_bf16 v[120:123], a[244:247], a[124:127], v[120:123]\n"
"buffer_store_dwordx4 v[72:75], v222, %10, s72 offen offset:1024 sc1\n"
"v_mfma_f32_16x16x32_bf16 v[120:123], a[240:243], a[108:111], v[120:123]\n"
"v_cvt_pk_bf16_f32 v64, v64, v65\n"
"v_cvt_pk_bf16_f32 v65, v66, v67\n"
"s_waitcnt lgkmcnt(4)\n"
"v_mfma_f32_16x16x32_bf16 v[124:127], a[248:251], a[108:111], v[124:127]\n"
"ds_read_b64_tr_b16 a[232:233], v221 offset:0\n"
"v_mfma_f32_16x16x32_bf16 v[124:127], a[252:255], a[124:127], v[124:127]\n"
"ds_read_b64_tr_b16 a[234:235], v221 offset:4096\n"
"v_mfma_f32_16x16x32_bf16 v[116:119], a[252:255], a[120:123], v[116:119]\n"
"v_cvt_pk_bf16_f32 v66, v68, v69\n"
"v_cvt_pk_bf16_f32 v67, v70, v71\n"
"v_mfma_f32_16x16x32_bf16 v[116:119], a[248:251], a[104:107], v[116:119]\n"
"buffer_store_dwordx4 v[64:67], v222, %10, s72 offen offset:0 sc1\n"
"v_mfma_f32_16x16x32_bf16 v[108:111], a[248:251], a[100:103], v[108:111]\n"
"ds_read_b64_tr_b16 a[236:237], v221 offset:8192\n"
"v_mfma_f32_16x16x32_bf16 v[108:111], a[252:255], a[116:119], v[108:111]\n"
"ds_read_b64_tr_b16 a[238:239], v221 offset:12288\n"
"v_mfma_f32_16x16x32_bf16 v[100:103], a[252:255], a[112:115], v[100:103]\n"
"v_and_b32 v229, 48, %24\n"
"v_lshlrev_b32 v229, 1, v229\n"
"v_mfma_f32_16x16x32_bf16 v[100:103], a[248:251], a[96:99], v[100:103]\n"
"s_waitcnt lgkmcnt(4)\n"
"v_mfma_f32_16x16x32_bf16 v[96:99], a[224:227], a[0:3], v[96:99]\n"
"ds_read_b64_tr_b16 a[240:241], v220 offset:16384\n"
"v_mfma_f32_16x16x32_bf16 v[96:99], a[228:231], a[16:19], v[96:99]\n"
"ds_read_b64_tr_b16 a[242:243], v220 offset:20480\n"
"v_mfma_f32_16x16x32_bf16 v[104:107], a[228:231], a[20:23], v[104:107]\n"
"global_load_dword v144, v229, %14 sc1\n"
"v_mfma_f32_16x16x32_bf16 v[104:107], a[224:227], a[4:7], v[104:107]\n"
"v_mfma_f32_16x16x32_bf16 v[112:115], a[224:227], a[8:11], v[112:115]\n"
"ds_read_b64_tr_b16 a[244:245], v220 offset:24576\n"
"v_mfma_f32_16x16x32_bf16 v[112:115], a[228:231], a[24:27], v[112:115]\n"
"ds_read_b64_tr_b16 a[246:247], v220 offset:28672\n"
"v_mfma_f32_16x16x32_bf16 v[120:123], a[228:231], a[28:31], v[120:123]\n"
"v_mfma_f32_16x16x32_bf16 v[120:123], a[224:227], a[12:15], v[120:123]\n"
"s_waitcnt lgkmcnt(4)\n"
"v_mfma_f32_16x16x32_bf16 v[124:127], a[232:235], a[12:15], v[124:127]\n"
"ds_read_b64_tr_b16 a[248:249], v221 offset:16384\n"
"v_mfma_f32_16x16x32_bf16 v[124:127], a[236:239], a[28:31], v[124:127]\n"
"ds_read_b64_tr_b16 a[250:251], v221 offset:20480\n"
"v_mfma_f32_16x16x32_bf16 v[116:119], a[236:239], a[24:27], v[116:119]\n"
"v_mfma_f32_16x16x32_bf16 v[116:119], a[232:235], a[8:11], v[116:119]\n"
"v_mfma_f32_16x16x32_bf16 v[108:111], a[232:235], a[4:7], v[108:111]\n"
"ds_read_b64_tr_b16 a[252:253], v221 offset:24576\n"
"v_mfma_f32_16x16x32_bf16 v[108:111], a[236:239], a[20:23], v[108:111]\n"
"ds_read_b64_tr_b16 a[254:255], v221 offset:28672\n"
"v_mfma_f32_16x16x32_bf16 v[100:103], a[236:239], a[16:19], v[100:103]\n"
"v_mfma_f32_16x16x32_bf16 v[100:103], a[232:235], a[0:3], v[100:103]\n"
"s_waitcnt lgkmcnt(4)\n"
"v_mfma_f32_16x16x32_bf16 v[96:99], a[240:243], a[32:35], v[96:99]\n"
"v_mfma_f32_16x16x32_bf16 v[96:99], a[244:247], a[48:51], v[96:99]\n"
"v_mfma_f32_16x16x32_bf16 v[104:107], a[244:247], a[52:55], v[104:107]\n"
"v_mfma_f32_16x16x32_bf16 v[104:107], a[240:243], a[36:39], v[104:107]\n"
"v_mfma_f32_16x16x32_bf16 v[112:115], a[240:243], a[40:43], v[112:115]\n"
"v_mfma_f32_16x16x32_bf16 v[112:115], a[244:247], a[56:59], v[112:115]\n"
"v_mfma_f32_16x16x32_bf16 v[120:123], a[244:247], a[60:63], v[120:123]\n"
"v_mfma_f32_16x16x32_bf16 v[120:123], a[240:243], a[44:47], v[120:123]\n"
"s_waitcnt lgkmcnt(0)\n"
"v_mfma_f32_16x16x32_bf16 v[124:127], a[248:251], a[44:47], v[124:127]\n"
"v_mfma_f32_16x16x32_bf16 v[124:127], a[252:255], a[60:63], v[124:127]\n"
"v_mfma_f32_16x16x32_bf16 v[116:119], a[252:255], a[56:59], v[116:119]\n"
"v_mfma_f32_16x16x32_bf16 v[116:119], a[248:251], a[40:43], v[116:119]\n"
"v_mfma_f32_16x16x32_bf16 v[108:111], a[248:251], a[36:39], v[108:111]\n"
"v_mfma_f32_16x16x32_bf16 v[108:111], a[252:255], a[52:55], v[108:111]\n"
"v_mfma_f32_16x16x32_bf16 v[100:103], a[252:255], a[48:51], v[100:103]\n"
"v_mfma_f32_16x16x32_bf16 v[100:103], a[248:251], a[32:35], v[100:103]\n"
"s_nop 7\n"
"s_waitcnt vmcnt(0)\n"
"s_branch ENDB%=\n"
"LGS%=:\n"
"s_mov_b32 s67, 0x40000\n"
"v_bfe_u32 v229, %24, 2, 5\n"
"LGP%=:\n"
"global_load_dword v228, v229, %13 offset:32 sc1\n"
"s_waitcnt vmcnt(0)\n"
"v_cmp_eq_u32 vcc, 0, v228\n"
"s_sub_u32 s67, s67, 1\n"
"s_cbranch_scc1 LGPX%=\n"
"s_cmp_eq_u64 vcc, 0\n"
"s_cbranch_scc1 LGPX%=\n"
"s_sleep 2\n"
"s_branch LGP%=\n"
"LGPX%=:\n"
"s_branch LGB%=\n"
"PGS%=:\n"
"s_mov_b32 s67, 0x40000\n"
"v_bfe_u32 v229, %24, 2, 5\n"
"PG%=:\n"
"global_load_dword v228, v229, %13 offset:0 sc1\n"
"s_waitcnt vmcnt(0)\n"
"v_cmp_eq_u32 vcc, 0, v228\n"
"s_sub_u32 s67, s67, 1\n"
"s_cbranch_scc1 PGX%=\n"
"s_cmp_eq_u64 vcc, 0\n"
"s_cbranch_scc1 PGX%=\n"
"s_sleep 2\n"
"s_branch PG%=\n"
"PGX%=:\n"
"s_branch PGB%=\n"
"ENDB%=:\n"
"s_nop 1\n"
    : "=&{v[96:111]}"(fa[0]), "=&{v[112:127]}"(fa[1]), "=&{v[128:143]}"(xq), "=&{v144}"(cv0)
    : "s"(srdY), "s"(srdW1), "s"(srdB), "s"(srdG), "s"(srdGX), "s"(srdW2), "s"(srdP), "s"(srdX1), "s"(cY), "s"(cG), "s"(cF), "s"(w4k), "s"(k0), "s"(k6), "s"(jj), "s"(px), "v"(vSQ), "v"(vS6), "v"(vRE), "v"(vRF), "v"(vL16), "v"(vD0), "v"(vD1), "v"(vD2), "v"(vD3), "v"(vFD), "v"(vB0), "v"(vB6)
    : "v0", "v1", "v2", "v3", "v4", "v5", "v6", "v7", "v8", "v9", "v10", "v11", "v12", "v13", "v14", "v15", "v16", "v17", "v18", "v19", "v20", "v21", "v22", "v23", "v24", "v25", "v26", "v27", "v28", "v29", "v30", "v31", "v32", "v33", "v34", "v35", "v36", "v37", "v38", "v39", "v40", "v41", "v42", "v43", "v44", "v45", "v46", "v47", "v48", "v49", "v50", "v51", "v52", "v53", "v54", "v55", "v56", "v57", "v58", "v59", "v60", "v61", "v62", "v63", "v64", "v65", "v66", "v67", "v68", "v69", "v70", "v71", "v72", "v73", "v74", "v75", "v76", "v77", "v78", "v79", "v80", "v81", "v82", "v83", "v84", "v85", "v86", "v87", "v88", "v89", "v90", "v91", "v92", "v93", "v94", "v95", "v145", "v146", "v147", "v148", "v149", "v150", "v151", "v152", "v153", "v154", "v155", "v156", "v157", "v158", "v159", "v160", "v161", "v162", "v163", "v164", "v165", "v166", "v167", "v168", "v169", "v170", "v171", "v172", "v173", "v174", "v175", "v176", "v177", "v178", "v179", "v180", "v181", "v182", "v183", "v184", "v185", "v186", "v187", "v188", "v189", "v190", "v191", "v192", "v193", "v194", "v195", "v196", "v197", "v198", "v199", "v200", "v201", "v202", "v203", "v204", "v205", "v206", "v207", "v208", "v209", "v210", "v211", "v212", "v213", "v214", "v215", "v216", "v217", "v218", "v219", "v220", "v221", "v222", "v223", "v224", "v225", "v226", "v227", "v228", "v229", "v230", "v231", "v232", "v233", "a0", "a1", "a2", "a3", "a4", "a5", "a6", "a7", "a8", "a9", "a10", "a11", "a12", "a13", "a14", "a15", "a16", "a17", "a18", "a19", "a20", "a21", "a22", "a23", "a24", "a25", "a26", "a27", "a28", "a29", "a30", "a31", "a32", "a33", "a34", "a35", "a36", "a37", "a38", "a39", "a40", "a41", "a42", "a43", "a44", "a45", "a46", "a47", "a48", "a49", "a50", "a51", "a52", "a53", "a54", "a55", "a56", "a57", "a58", "a59", "a60", "a61", "a62", "a63", "a64", "a65", "a66", "a67", "a68", "a69", "a70", "a71", "a72", "a73", "a74", "a75", "a76", "a77", "a78", "a79", "a80", "a81", "a82", "a83", "a84", "a85", "a86", "a87", "a88", "a89", "a90", "a91", "a92", "a93", "a94", "a95", "a96", "a97", "a98", "a99", "a100", "a101", "a102", "a103", "a104", "a105", "a106", "a107", "a108", "a109", "a110", "a111", "a112", "a113", "a114", "a115", "a116", "a117", "a118", "a119", "a120", "a121", "a122", "a123", "a124", "a125", "a126", "a127", "a128", "a129", "a130", "a131", "a132", "a133", "a134", "a135", "a136", "a137", "a138", "a139", "a140", "a141", "a142", "a143", "a144", "a145", "a146", "a147", "a148", "a149", "a150", "a151", "a152", "a153", "a154", "a155", "a156", "a157", "a158", "a159", "a160", "a161", "a162", "a163", "a164", "a165", "a166", "a167", "a168", "a169", "a170", "a171", "a172", "a173", "a174", "a175", "a176", "a177", "a178", "a179", "a180", "a181", "a182", "a183", "a184", "a185", "a186", "a187", "a188", "a189", "a190", "a191", "a192", "a193", "a194", "a195", "a196", "a197", "a198", "a199", "a200", "a201", "a202", "a203", "a204", "a205", "a206", "a207", "a208", "a209", "a210", "a211", "a212", "a213", "a214", "a215", "a216", "a217", "a218", "a219", "a220", "a221", "a222", "a223", "a224", "a225", "a226", "a227", "a228", "a229", "a230", "a231", "a232", "a233", "a234", "a235", "a236", "a237", "a238", "a239", "a240", "a241", "a242", "a243", "a244", "a245", "a246", "a247", "a248", "a249", "a250", "a251", "a252", "a253", "a254", "a255", "s64", "s65", "s66", "s67", "s68", "s69", "s70", "s71", "s72", "s73", "s74", "s75", "s76", "s77", "vcc", "scc", "memory");
  }
  {
    PUBLISH(cq + 640u + head);
    const __amdgpu_buffer_rsrc_t srdP = SRD(ws + WS_P2 + (size_t)mb0 * 65536u + cgp * 2048u, (12u << 20) + 65536u + 2048u);
    unsigned* fl = cq + 640u + sk * 32u + (lane & 3u) * 8u + jj;
    unsigned cv = cv0;
    const u32x4 g4 = *(const u32x4*)(lds + L_PAR + 10240u + (cgp * 4u + gq) * 16u), b4 = *(const u32x4*)(lds + L_PAR + 12288u + (cgp * 4u + gq) * 16u);
    u32x4 pp[4][3] = {};
    unsigned todo = 7u;
#pragma nounroll
    for (unsigned spins = 0; todo != 0u && spins < (1u << 18); ++spins) {
      const unsigned nz = (unsigned)__builtin_amdgcn_ballot_w64(cv != 0u);
      asm volatile("" ::: "memory");
#pragma unroll
      for (int k = 0; k < 3; ++k) {
        const unsigned g = (sk + 1u + k) & 3u;
        if ((todo & (1u << k)) && ((nz >> g) & 1u)) {
#pragma unroll
          for (int m = 0; m < 4; ++m) pp[m][k] = ld_sc1(srdP, lane * 16u + (m & 1) * 1024u, (g << 22) + ((m >> 1) << 16));
          todo &= ~(1u << k);
        }
      }
      if (todo) { __builtin_amdgcn_s_sleep(SLP); cv = __hip_atomic_load((gu32*)fl, RLX_AGENT); }
    }
#pragma unroll
    for (int m = 0; m < 4; ++m) {
      unsigned o[4];
#pragma unroll
      for (int e = 0; e < 4; ++e) {
        const f32x2 own = {fa[m >> 1][8 * (m & 1) + 2 * e], fa[m >> 1][8 * (m & 1) + 2 * e + 1]};
        o[e] = pk2(fma2(up2(g4[e]), add2(add2(add2(add2(own, b4[e]), pp[m][0][e]), pp[m][1][e]), pp[m][2][e]), up2(xq[4 * m + e])));
      }
      auto r0 = __builtin_amdgcn_permlane16_swap(o[0], o[2], false, false);
      auto r1 = __builtin_amdgcn_permlane16_swap(o[1], o[3], false, false);
      const u32x4 ov = {r0[0], r1[0], r0[1], r1[1]};
      *(u32x4*)(out + (size_t)(mb0 * 32u + m * 16u + (lane & 15u)) * 1024 + cgp * 32u + (gq & 1u) * 16u + (gq >> 1) * 8u) = ov;
    }
  }
  asm volatile("K_MAIN_CODE_END:" ::: "memory");
}

extern "C" void kernel_launch(void* const* d_in, const int* in_sizes, int n_in, void* d_out, int out_size, void* d_ws, size_t ws_size, hipStream_t stream) {
  char* ws = (char*)d_ws;
  k_main<<<256, 256, 0, stream>>>((const u16*)d_in[0], (const char*)d_in[1], (const char*)d_in[2], (const char*)d_in[3], (const char*)d_in[4],
                                  (const char*)d_in[6], (const u16*)d_in[7], (const char*)d_in[8], (const u16*)d_in[9], (const u16*)d_in[10], (const char*)d_in[11], ws, (u16*)d_out);
}
```
